```python
import torch

def _softmax(x, axis=-1):
    return torch.softmax(x, dim=axis)
def _gelu(x):
    return torch.nn.functional.gelu(x, approximate="tanh")
def _silu(x):
    return torch.nn.functional.silu(x)
def _sigmoid(x):
    return torch.sigmoid(x)
def _layernorm(x, axis=-1, eps=1e-5):
    m = x.mean(dim=axis, keepdim=True)
    v = x.var(dim=axis, keepdim=True, unbiased=False)
    return (x - m) / torch.sqrt(v + eps)
def _scatter_add(tgt, idx, src, axis=0):
    return tgt.clone().index_add_(axis, idx, src)
def _segment_sum(src, seg, n):
    out = torch.zeros((n,) + src.shape[1:], dtype=src.dtype)
    return out.index_add_(0, seg, src)
def _pad(x, pw):
    flat = tuple(v for pair in reversed(pw) for v in pair)
    return torch.nn.functional.pad(x, flat)

kernel_name = 'sequential_preLN__linear_attn_block__swiglu_ffn__prompt'

def setup_inputs():

    _w = (lambda a: (lambda t: t.to(torch.bfloat16) if t.is_floating_point() else t)(torch.as_tensor(a)))
    def _A_setup():
        B, S, D = 1, 2048, 576
        return {
            "x": _w((lambda *s: torch.randn(*s, dtype=torch.bfloat16) * 0.05)(B, S, D)),
            "wqkv": _w((lambda *s: torch.randn(*s, dtype=torch.bfloat16) * 0.05)(D, 3 * D)),
            "wo": _w((lambda *s: torch.randn(*s, dtype=torch.bfloat16) * 0.05)(D, D)),
        }
    _A_in = _A_setup()

    def _B_setup():
        B, S, D, F = 1, 2048, 576, 4608
        return {
            "x": _w((lambda *s: torch.randn(*s, dtype=torch.bfloat16) * 0.05)(B, S, D)),
            "w_gate": _w((lambda *s: torch.randn(*s, dtype=torch.bfloat16) * 0.05)(D, F)),
            "w_up": _w((lambda *s: torch.randn(*s, dtype=torch.bfloat16) * 0.05)(D, F)),
            "w_down": _w((lambda *s: torch.randn(*s, dtype=torch.bfloat16) * 0.05)(F, D)),
        }
    _B_in = _B_setup()

    return {
        "A_x": _A_in["x"],
        "A_wqkv": _A_in["wqkv"],
        "A_wo": _A_in["wo"],
        "B_x": _B_in["x"],
        "B_w_gate": _B_in["w_gate"],
        "B_w_up": _B_in["w_up"],
        "B_w_down": _B_in["w_down"],
    }

def reference(A_x, A_wqkv, A_wo, B_x, B_w_gate, B_w_up, B_w_down):
    def _A_ref(x, wqkv, wo):
        B, S, D = x.shape
        H = 32; Dh = D // H
        qkv = (lambda x, *d: x.permute(*d))((x @ wqkv).reshape(B, S, 3, H, Dh), 2, 0, 3, 1, 4)
        q, k, v = qkv[0], qkv[1], qkv[2]
        phi = lambda t: torch.where(t > 0, t + 1, torch.exp(t)) + 1e-6
        pq, pk = phi(q), phi(k)
        kv = torch.einsum("bhsd,bhse->bhde", pk, v)
        z = torch.einsum("bhsd->bhd", pk)
        num = torch.einsum("bhqd,bhde->bhqe", pq, kv)
        den = torch.einsum("bhqd,bhd->bhq", pq, z)[..., None] + 1e-6
        o = num / den
        return (lambda x, *d: x.permute(*d))(o, 0, 2, 1, 3).reshape(B, S, D) @ wo

    def _B_ref(x, w_gate, w_up, w_down):
        g = _silu(x @ w_gate)
        u = x @ w_up
        return (g * u) @ w_down

    x = A_x
    h = x + _A_ref(_layernorm(x, axis=-1), A_wqkv, A_wo)
    h2 = h + _B_ref(_layernorm(h, axis=-1), B_w_gate, B_w_up, B_w_down)
    return h2
```

```cpp
#include <hip/hip_runtime.h>
#include <hip/hip_bf16.h>
#include <stdint.h>

namespace nv {
typedef unsigned short u16;
__device__ __forceinline__ float bf2f(u16 v) { return __uint_as_float(((unsigned)v) << 16); }
__device__ __forceinline__ u16 f2bf(float f) { __hip_bfloat16 b = __float2bfloat16(f); return *(u16*)&b; }
__device__ __forceinline__ float rbf(float f) { return bf2f(f2bf(f)); }

constexpr int S = 2048, D = 576, H = 32, DH = 18, F = 4608;

__global__ void ln_k(const u16* x, u16* y) {
  int row = blockIdx.x; int t = threadIdx.x;
  float s = 0.f;
  for (int i = t; i < D; i += 64) s += bf2f(x[row * D + i]);
  for (int o = 32; o; o >>= 1) s += __shfl_xor(s, o);
  float mean = s / D;
  float q = 0.f;
  for (int i = t; i < D; i += 64) { float d = bf2f(x[row * D + i]) - mean; q += d * d; }
  for (int o = 32; o; o >>= 1) q += __shfl_xor(q, o);
  float var = rbf(q / D); float m = rbf(mean);
  float den = rbf(sqrtf(rbf(var + 1e-5f)));
  for (int i = t; i < D; i += 64) y[row * D + i] = f2bf(rbf(bf2f(x[row * D + i]) - m) / den);
}
__global__ void gemm_k(const u16* A, const u16* B, u16* C, int M, int N, int K) {
  __shared__ float As[16][17], Bs[16][17];
  int tx = threadIdx.x & 15, ty = threadIdx.x >> 4;
  int row = blockIdx.y * 16 + ty, col = blockIdx.x * 16 + tx;
  float acc = 0.f;
  for (int k0 = 0; k0 < K; k0 += 16) {
    As[ty][tx] = bf2f(A[row * K + k0 + tx]);
    Bs[ty][tx] = bf2f(B[(k0 + ty) * N + col]);
    __syncthreads();
    for (int k = 0; k < 16; ++k) acc += As[ty][k] * Bs[k][tx];
    __syncthreads();
  }
  C[row * N + col] = f2bf(acc);
}
__device__ __forceinline__ float phi(float t) { float r = t > 0.f ? rbf(t + 1.f) : rbf(expf(t)); return rbf(r + 1e-6f); }
__global__ void kv_k(const u16* qkv, float* kv, float* z) {
  int h = blockIdx.x; int t = threadIdx.x;
  if (t < DH * DH) {
    int d = t / DH, e = t % DH; float acc = 0.f;
    for (int s = 0; s < S; ++s) acc += phi(bf2f(qkv[s * 3 * D + D + h * DH + d])) * bf2f(qkv[s * 3 * D + 2 * D + h * DH + e]);
    kv[(h * DH + d) * DH + e] = rbf(acc);
  } else if (t < DH * DH + DH) {
    int d = t - DH * DH; float acc = 0.f;
    for (int s = 0; s < S; ++s) acc += phi(bf2f(qkv[s * 3 * D + D + h * DH + d]));
    z[h * DH + d] = rbf(acc);
  }
}
__global__ void o_k(const u16* qkv, const float* kv, const float* z, u16* o) {
  int s = blockIdx.x; int c = threadIdx.x;
  int h = c / DH, e = c % DH;
  float num = 0.f, den = 0.f;
  for (int d = 0; d < DH; ++d) { float p = phi(bf2f(qkv[s * 3 * D + h * DH + d])); num += p * kv[(h * DH + d) * DH + e]; den += p * z[h * DH + d]; }
  num = rbf(num); den = rbf(rbf(den) + 1e-6f);
  o[s * D + c] = f2bf(num / den);
}
__global__ void add_k(const u16* a, const u16* b, u16* c, int n) { int i = blockIdx.x * 256 + threadIdx.x; if (i < n) c[i] = f2bf(bf2f(a[i]) + bf2f(b[i])); }
__global__ void act_k(const u16* g, const u16* u, u16* a, int n) {
  int i = blockIdx.x * 256 + threadIdx.x; if (i < n) { float gv = bf2f(g[i]); float sg = rbf(gv / (1.f + expf(-gv))); a[i] = f2bf(sg * bf2f(u[i])); }
}

struct WS { u16 *xn, *qkv, *o, *ao, *h, *hn, *g, *u, *a, *dn; float *kv, *z; };
inline WS carve(void* ws) {
  WS w; char* p = (char*)ws;
  auto take = [&](size_t b) { char* r = p; p += (b + 255) / 256 * 256; return (void*)r; };
  w.xn = (u16*)take(S * D * 2); w.qkv = (u16*)take(S * 3 * D * 2); w.o = (u16*)take(S * D * 2); w.ao = (u16*)take(S * D * 2);
  w.h = (u16*)take(S * D * 2); w.hn = (u16*)take(S * D * 2); w.g = (u16*)take((size_t)S * F * 2); w.u = (u16*)take((size_t)S * F * 2);
  w.a = (u16*)take((size_t)S * F * 2); w.dn = (u16*)take(S * D * 2); w.kv = (float*)take(H * DH * DH * 4); w.z = (float*)take(H * DH * 4);
  return w;
}
inline void run(void* const* d_in, void* d_out, void* d_ws, hipStream_t st) {
  const u16* x = (const u16*)d_in[0]; const u16* wqkv = (const u16*)d_in[1]; const u16* wo = (const u16*)d_in[2];
  const u16* wg = (const u16*)d_in[4]; const u16* wu = (const u16*)d_in[5]; const u16* wd = (const u16*)d_in[6];
  WS w = carve(d_ws);
  ln_k<<<S, 64, 0, st>>>(x, w.xn);
  gemm_k<<<dim3(3 * D / 16, S / 16), 256, 0, st>>>(w.xn, wqkv, w.qkv, S, 3 * D, D);
  kv_k<<<H, 384, 0, st>>>(w.qkv, w.kv, w.z);
  o_k<<<S, D, 0, st>>>(w.qkv, w.kv, w.z, w.o);
  gemm_k<<<dim3(D / 16, S / 16), 256, 0, st>>>(w.o, wo, w.ao, S, D, D);
  add_k<<<S * D / 256, 256, 0, st>>>(x, w.ao, w.h, S * D);
  ln_k<<<S, 64, 0, st>>>(w.h, w.hn);
  gemm_k<<<dim3(F / 16, S / 16), 256, 0, st>>>(w.hn, wg, w.g, S, F, D);
  gemm_k<<<dim3(F / 16, S / 16), 256, 0, st>>>(w.hn, wu, w.u, S, F, D);
  act_k<<<S * F / 256, 256, 0, st>>>(w.g, w.u, w.a, S * F);
  gemm_k<<<dim3(D / 16, S / 16), 256, 0, st>>>(w.a, wd, w.dn, S, D, F);
  add_k<<<S * D / 256, 256, 0, st>>>(w.h, w.dn, (u16*)d_out, S * D);
}
}

#ifndef NV_NO_ENTRY
extern "C" void kernel_launch(void* const* d_in, const int* in_sizes, int n_in,
                              void* d_out, int out_size, void* d_ws, size_t ws_size,
                              hipStream_t stream) {
  nv::run(d_in, d_out, d_ws, stream);
}
#endif
```

```cpp
#include <hip/hip_runtime.h>
#include <hip/hip_bf16.h>
#include <stdint.h>
#include <type_traits>
#include <utility>

namespace fk {
typedef unsigned short u16;
typedef short bf16x8 __attribute__((ext_vector_type(8)));
typedef short s16x4 __attribute__((ext_vector_type(4)));
typedef float f32x4 __attribute__((ext_vector_type(4)));
typedef float f32x2 __attribute__((ext_vector_type(2)));
typedef float f32x16 __attribute__((ext_vector_type(16)));
typedef unsigned u32x4 __attribute__((ext_vector_type(4)));
typedef unsigned u32x2 __attribute__((ext_vector_type(2)));
typedef __bf16 bf16x2v __attribute__((ext_vector_type(2)));
#define LAS __attribute__((address_space(3)))
#define DI __device__ __forceinline__

constexpr int S_ = 2048, D_ = 576, H_ = 32, DH_ = 18, F_ = 4608;

template <int N, class Fn, int... I> DI void sfor_impl(Fn&& f, std::integer_sequence<int, I...>) { (f(std::integral_constant<int, I>{}), ...); }
template <int N, class Fn> DI void sfor(Fn&& f) { sfor_impl<N>(f, std::make_integer_sequence<int, N>{}); }

template <int N> DI void wait_vm() { asm volatile("s_waitcnt vmcnt(%0)" ::"i"(N) : "memory"); }
DI void wait_lgkm0() { asm volatile("s_waitcnt lgkmcnt(0)" ::: "memory"); }
DI void bar() { asm volatile("" ::: "memory"); __builtin_amdgcn_s_barrier(); asm volatile("" ::: "memory"); }
template <class T> DI void pin(T& v) { asm volatile("" : "+v"(v)); }

DI void glds16(const void* sbase, unsigned voff, unsigned ldsdst) {
  asm volatile("s_mov_b32 m0, %2\n\ts_nop 0\n\tglobal_load_lds_dwordx4 %0, %1" :: "v"(voff), "s"(sbase), "s"(ldsdst) : "memory");
}
DI void glds16x2(const void* sbase, unsigned voff, unsigned ldsdst) {
  asm volatile("s_mov_b32 m0, %2\n\ts_nop 0\n\tglobal_load_lds_dwordx4 %0, %1\n\tglobal_load_lds_dwordx4 %0, %1 offset:1024" :: "v"(voff), "s"(sbase), "s"(ldsdst) : "memory");
}
DI void glds16v(const void* gptr, unsigned ldsdst) {
  asm volatile("s_mov_b32 m0, %1\n\ts_nop 0\n\tglobal_load_lds_dwordx4 %0, off" :: "v"(gptr), "s"(ldsdst) : "memory");
}
DI void sb0() { __builtin_amdgcn_sched_barrier(0); }
template <int OFF> DI void gload16(u32x4& dst, const void* sbase, unsigned voff) {
  asm volatile("global_load_dwordx4 %0, %1, %2 offset:%3" : "=v"(dst) : "v"(voff), "s"(sbase), "i"(OFF) : "memory");
}
template <int OFF> DI void gload16a(u32x4& dst, const void* sbase, unsigned voff) {
  asm volatile("global_load_dwordx4 %0, %1, %2 offset:%3" : "=a"(dst) : "v"(voff), "s"(sbase), "i"(OFF) : "memory");
}
template <class T> DI void pina(T& v) { asm volatile("" : "+a"(v)); }
template <int MODE> DI void store16(void* p, u32x4 v) {
  if constexpr (MODE == 0) *(u32x4*)p = v;
  else if constexpr (MODE == 1) __builtin_nontemporal_store(v, (u32x4*)p);
  else if constexpr (MODE == 2) asm volatile("global_store_dwordx4 %0, %1, off sc1\n\ts_nop 1" :: "v"(p), "v"(v) : "memory");
  else if constexpr (MODE == 3) asm volatile("global_store_dwordx4 %0, %1, off sc0 sc1\n\ts_nop 1" :: "v"(p), "v"(v) : "memory");
  else if constexpr (MODE == 4) asm volatile("global_store_dwordx4 %0, %1, off sc1 nt\n\ts_nop 1" :: "v"(p), "v"(v) : "memory");
  else asm volatile("" :: "v"(v));
}
DI float bf2f(u16 v) { return __uint_as_float(((unsigned)v) << 16); }
DI float bflo(unsigned v) { return __uint_as_float(v << 16); }
DI float bfhi(unsigned v) { return __uint_as_float(v & 0xffff0000u); }
DI unsigned pk2(float lo, float hi) {
  bf16x2v r; r[0] = (__bf16)lo; r[1] = (__bf16)hi; return __builtin_bit_cast(unsigned, r);
}
DI s16x4 tr_read(unsigned lds_addr) {
  return __builtin_amdgcn_ds_read_tr16_b64_v4i16((LAS s16x4*)(size_t)lds_addr);
}
DI f32x16 mfma32(u32x4 a, u32x4 b, f32x16 c) { return __builtin_amdgcn_mfma_f32_32x32x16_bf16(__builtin_bit_cast(bf16x8, a), __builtin_bit_cast(bf16x8, b), c, 0, 0, 0); }
DI f32x4 mfma16(u32x4 a, u32x4 b, f32x4 c) { return __builtin_amdgcn_mfma_f32_16x16x32_bf16(__builtin_bit_cast(bf16x8, a), __builtin_bit_cast(bf16x8, b), c, 0, 0, 0); }
DI int uni(int v) { return __builtin_amdgcn_readfirstlane(v); }
}
namespace fk {
DI bf16x2v as_bf2(unsigned v) { return __builtin_bit_cast(bf16x2v, v); }
DI float dot2_ones(unsigned v, float c) { return __builtin_amdgcn_fdot2_f32_bf16(as_bf2(v), as_bf2(0x3f803f80u), c, false); }

constexpr size_t WS_HF   = 0;
constexpr size_t SZ_HF   = (size_t)S_ * D_ * 2;
constexpr size_t WS_HST  = WS_HF + SZ_HF;
constexpr size_t SZ_HST  = (size_t)S_ * 16 * 8;
constexpr int    GU_STREAM = 336;
constexpr size_t WS_WGU  = WS_HST + SZ_HST;
constexpr size_t SZ_WGU  = (size_t)32 * GU_STREAM * 1024;
constexpr int    CS_PARTS = 9;
constexpr size_t WS_CS   = WS_WGU + SZ_WGU;
constexpr size_t SZ_CS   = (size_t)CS_PARTS * 288 * 32 * 4;
constexpr size_t WS_ACT  = WS_CS + SZ_CS;
constexpr size_t SZ_ACT  = (size_t)S_ * F_ * 2;
constexpr int    WD_STREAM = 168;
constexpr size_t WS_WD   = WS_ACT + SZ_ACT;
constexpr size_t SZ_WD   = (size_t)32 * WD_STREAM * 1024;
constexpr size_t WS_PART = WS_WD + SZ_WD;
constexpr size_t SZ_PART = (size_t)4 * S_ * D_ * 4;
constexpr size_t WS_R2 = WS_PART + SZ_PART;
constexpr size_t WS_MEAN = WS_R2 + S_ * 4;
constexpr size_t WS_END_FFN = WS_MEAN + S_ * 4;

DI int gu_stream_idx(int T, int S, int mat) { return T < 3 ? (S * 3 + T) * 2 + mat : 108 + (T - 3) * 36 + S * 2 + mat; }

DI void pack_gu_unit(const u16* wg, const u16* wu, char* ws, int unit, unsigned ldsb, int lane) {
  const int lc = unit / 9, kp = unit % 9;
  {
    const int t = lane >> 4, mat = (lane >> 3) & 1, q = (lane >> 1) & 3, hf = lane & 1;
    const unsigned voff = (unsigned)(((kp * 64 + q) * F_ + lc * 64 + t * 16 + hf * 8) * 2);
    const char* gsrc = (const char*)(mat ? wu : wg) + voff;
    sfor<16>([&](auto I) {
      constexpr int kg = decltype(I)::value;
      glds16v(gsrc + (size_t)kg * 4 * F_ * 2, ldsb + kg * 1024);
    });
  }
  const int g = lane >> 4;
  const unsigned rbase = ldsb + (4 * (g >> 1) + (g & 1)) * 1024 + (lane & 15) * 8;
  sfor<2>([&](auto I) {
    constexpr int s = decltype(I)::value;
    wait_vm<8 - 8 * s>();
    sfor<8>([&](auto J) {
      constexpr int t = decltype(J)::value >> 1, mat = decltype(J)::value & 1;
      s16x4 lo = tr_read(rbase + (8 * s + 0) * 1024 + t * 256 + mat * 128);
      s16x4 hi = tr_read(rbase + (8 * s + 2) * 1024 + t * 256 + mat * 128);
      u32x2 a = __builtin_bit_cast(u32x2, lo), b = __builtin_bit_cast(u32x2, hi);
      u32x4 v = {a[0], a[1], b[0], b[1]};
      const int ft = lc * 4 + t, csl = ft / 9, T = ft % 9;
      const int idx = gu_stream_idx(T, kp * 2 + s, mat);
      *(u32x4*)(ws + WS_WGU + ((size_t)csl * GU_STREAM + idx) * 1024 + lane * 16) = v;
    });
  });
}

DI void pack_wd_unit(const u16* wd, char* ws, int unit, unsigned ldsb, int lane) {
  const int S = unit >> 2, ng = unit & 3;
  sfor<9>([&](auto I) {
    constexpr int n = decltype(I)::value;
    const int c = n * 64 + lane, row = c / 18, cc = c % 18;
    const char* gsrc = (const char*)wd + ((size_t)(S * 32 + row) * D_ + ng * 144 + cc * 8) * 2;
    glds16v(gsrc, ldsb + n * 1024);
  });
  wait_vm<0>();
  const int g = lane >> 4, q = (lane >> 2) & 3, p = lane & 3;
  const unsigned rb = ldsb + (16 * (g >> 1) + 4 * (g & 1) + q) * 288;
  const int ksl = S / 36, kh = (S % 36) / 18, ks = S % 18;
  char* dst = ws + WS_WD + ((size_t)((ng * 4 + ksl) * 2 + kh) * WD_STREAM + ks * 9) * 1024 + lane * 16;
  sfor<9>([&](auto I) {
    constexpr int t = decltype(I)::value;
    const int col = t < 8 ? 32 * (t >> 1) + 8 * p + 4 * (t & 1) : 128 + 4 * p;
    s16x4 lo = tr_read(rb + col * 2);
    s16x4 hi = tr_read(rb + 8 * 288 + col * 2);
    u32x2 a = __builtin_bit_cast(u32x2, lo), b = __builtin_bit_cast(u32x2, hi);
    u32x4 v = {a[0], a[1], b[0], b[1]};
    *(u32x4*)(dst + t * 1024) = v;
  });
}

__global__ __launch_bounds__(256) void k_pack(const u16* wg, const u16* wu, const u16* wd, char* ws) {
  extern __shared__ __attribute__((aligned(16))) char lds[];
  const int lane = threadIdx.x & 63, w = uni(threadIdx.x >> 6);
  const int gw = blockIdx.x * 4 + w;
  const unsigned ldsb = (unsigned)(size_t)(LAS char*)lds + w * 16384;
  if (gw < 648) pack_gu_unit(wg, wu, ws, gw, ldsb, lane);
  else if (gw < 648 + 576) pack_wd_unit(wd, ws, gw - 648, ldsb, lane);
}

#define ACT_ST 2
#define K3_TG 1
#define K3_LA 4
#define K3_LW0 2
#define K3_DD 4
constexpr int K3_NW = 8 / K3_TG;
constexpr int K3_NAG = 15;
constexpr int K3_R = 8;
constexpr int K3_D = K3_DD;
constexpr int K3_LDS = K3_R * 16384;
constexpr int K3_NST = 21;
constexpr int K3_NSTAT = 4 * K3_TG;
constexpr int K3_GPS = 2 * K3_TG;
constexpr int K3_APS = 2 * K3_TG;
struct K3Sched {
  int aslot[18];
  int sslot[K3_NST];
  int posA[18], posS[K3_NST];
  int cntBar[K3_NST];
  int cntSlot[324];
  constexpr K3Sched() : aslot{}, sslot{}, posA{}, posS{}, cntBar{}, cntSlot{} {
    for (int a = 0; a < 18; ++a) aslot[a] = a < K3_LA ? -1 : 6 * (a - K3_LA);
    for (int s = 0; s < K3_NST; ++s) {
      int j;
      if (s < K3_LW0) j = -1;
      else if (s <= 6 + K3_LW0) j = s - K3_LW0;
      else { j = 7 + (s - 7 - K3_LW0) / 2; if (j < s - (K3_R - 2)) j = s - (K3_R - 2); }
      sslot[s] = j;
    }
    int c = 0;
    int a = 0;
    for (int s = 0; s < K3_LW0; ++s) {
      c += K3_GPS; posS[s] = c;
      int aend = (s == K3_LW0 - 1) ? K3_LA : (s + 1);
      for (; a < aend; ++a) { c += K3_APS; posA[a] = c; }
      if (s == 0) c += K3_NSTAT;
    }
    for (int n = -K3_D; n < 324; ++n) {
      if ((n + K3_D) % 16 == 0 && (n + K3_D) < 324) {
        int i = (n + K3_D) / 16; cntBar[i] = c;
        for (int s = 0; s < K3_NST; ++s) if (sslot[s] == i) { if (s < K3_NST - 1) c += K3_GPS; posS[s] = c; }
      }
      if (n >= 0) {
        for (int q = 0; q < 18; ++q) if (aslot[q] == n) { c += K3_APS; posA[q] = c; }
        cntSlot[n] = c;
      }
    }
  }
};
constexpr K3Sched k3s{};

__global__ __launch_bounds__(64 * K3_NW, K3_NW / 4) void k3_gateup(const char* __restrict__ ws_in, char* __restrict__ ws_out) {
  extern __shared__ __attribute__((aligned(16))) char lds[];
  const int tid = threadIdx.x, lane = tid & 63, w = uni(tid >> 6);
  const int b = blockIdx.x, xcd = b & 7, idx = b >> 3;
  const int rt = (xcd & 1) * 4 + (idx & 3), cs = (xcd >> 1) * 8 + (idx >> 2);
  const int tt0 = rt * 8 + w * K3_TG;
  const unsigned ldsb = (unsigned)(size_t)(LAS char*)lds;
  const char* wsrc = ws_in + WS_WGU + (size_t)cs * GU_STREAM * 1024 + w * K3_GPS * 1024;
  const char* asrc = ws_in + WS_HF + (size_t)tt0 * 36 * 1024;
  const unsigned voff = lane * 16;
  const int g = lane >> 4;
  const unsigned avoff = (g >> 1) * 1024 + (g & 1) * 512 + (lane & 15) * 16;
  u32x4 hf[K3_TG][18][2];
  u32x4 stv[K3_TG][2][2];
  auto issue_stage = [&](auto SI) {
    constexpr int s = decltype(SI)::value;
    constexpr int slot = s % K3_R;
    if (s < K3_NST - 1 || w * K3_GPS < 4) {
      sfor<K3_TG>([&](auto I) { constexpr int i = decltype(I)::value;
        glds16x2(wsrc + (size_t)s * 16384 + i * 2048, voff, ldsb + slot * 16384 + w * K3_GPS * 1024 + i * 2048); });
    }
  };
  auto issue_a = [&](auto AI) {
    constexpr int a = decltype(AI)::value;
    sfor<K3_TG>([&](auto I) { constexpr int i = decltype(I)::value;
      if constexpr (i == 0 || a >= K3_NAG) {
        gload16<(a % 2) * 2048>(hf[i][a][0], asrc + i * 36 * 1024 + (a / 2) * 4096, avoff);
        gload16<(a % 2) * 2048 + 256>(hf[i][a][1], asrc + i * 36 * 1024 + (a / 2) * 4096, avoff);
      } else {
        gload16a<(a % 2) * 2048>(hf[i][a][0], asrc + i * 36 * 1024 + (a / 2) * 4096, avoff);
        gload16a<(a % 2) * 2048 + 256>(hf[i][a][1], asrc + i * 36 * 1024 + (a / 2) * 4096, avoff);
      } });
  };
  sfor<K3_LW0>([&](auto SI) {
    constexpr int s = decltype(SI)::value;
    issue_stage(SI);
    constexpr int a0 = s, a1 = (s == K3_LW0 - 1) ? K3_LA : (s + 1);
    sfor<a1 - a0>([&](auto AI) { issue_a(std::integral_constant<int, a0 + decltype(AI)::value>{}); });
    if constexpr (s == 0) {
      sfor<K3_TG>([&](auto I) { constexpr int i = decltype(I)::value;
        const char* sp = ws_in + WS_HST + (size_t)(tt0 + i) * 32 * 128;
        const unsigned so = (lane & 15) * 128 + g * 32;
        gload16<0>(stv[i][0][0], sp, so); gload16<16>(stv[i][0][1], sp, so); gload16<2048>(stv[i][1][0], sp, so); gload16<2064>(stv[i][1][1], sp, so); });
    }
  });
  float c1[K3_TG][2];

  f32x4 acc[4][K3_TG][2][2];
  f32x16 acc32[4][K3_TG];
  u32x4 wf[K3_D + 1];
  unsigned lrd0 = ldsb + lane * 16, lrd1 = lrd0 + 65536;
  asm volatile("" : "+v"(lrd0), "+v"(lrd1));
  unsigned ov[4];
  char* actp = ws_out + WS_ACT + ((size_t)tt0 * 288 + cs * 9) * 1024 + ((g & 1) * 32 + (g >> 1) * 16 + (lane & 15)) * 16;

  auto rd = [&](auto NI) {
    constexpr int n = decltype(NI)::value;
    if constexpr (n < 324) {
      constexpr int st = n / 16;
      if constexpr (n % 16 == 0) {
        wait_vm<(st < K3_NST - 1 ? k3s.cntBar[st] - k3s.posS[st] : 0)>();
        bar();
        sfor<K3_NST>([&](auto SI) { if constexpr (k3s.sslot[decltype(SI)::value] == st) issue_stage(SI); });
      }
      constexpr int off = (st % K3_R) * 16384 + (n % 16) * 1024;
      wf[n % (K3_D + 1)] = *(const LAS u32x4*)(size_t)((off < 65536 ? lrd0 : lrd1) + (off & 65535));
    }
  };
  float et[8], ep[8];
  auto epi_half = [&](auto TI, auto GI, auto UI) {
    constexpr int T = decltype(TI)::value, tg = decltype(GI)::value, u = decltype(UI)::value, q = u / 2;
    constexpr int ai = T % 4;
    if constexpr (u % 2 == 0) {
      if constexpr (q < 8) { float a = acc[ai][tg][0][q >> 2][q & 3]; et[q] = a * c1[tg][q >> 2]; pin(et[q]); ep[q] = a * acc[ai][tg][1][q >> 2][q & 3]; pin(ep[q]); }
      if constexpr (q >= 1 && q <= 8) et[q - 1] = __builtin_amdgcn_exp2f(et[q - 1]);
    } else {
      if constexpr (q >= 1 && q <= 8) et[q - 1] = __builtin_amdgcn_rcpf(1.f + et[q - 1]);
      if constexpr (q >= 2 && q <= 9) { ep[q - 2] = ep[q - 2] * et[q - 2]; pin(ep[q - 2]); }
      if constexpr (q >= 3 && q <= 9 && (q % 2 == 1)) ov[(q - 3) / 2] = pk2(ep[q - 3], ep[q - 2]);
      if constexpr (q == 10) {
        auto r0 = __builtin_amdgcn_permlane32_swap(ov[0], ov[2], false, false);
        auto r1 = __builtin_amdgcn_permlane32_swap(ov[1], ov[3], false, false);
        u32x4 o = {r0[0], r1[0], r0[1], r1[1]};
        store16<ACT_ST>(actp + (tg * 288 + T) * 1024, o);
      }
    }
  };

  sfor<K3_D>([&](auto I) { rd(I); });
  sfor<324>([&](auto NI) {
    constexpr int n = decltype(NI)::value;
    constexpr int T = n < 108 ? (n % 6) / 2 : 3 + (n - 108) / 36;
    constexpr int ks = n < 108 ? n / 6 : ((n - 108) % 36) / 2;
    constexpr int m = n & 1;
    constexpr int ai = T % 4;
    rd(std::integral_constant<int, n + K3_D>{});
    sfor<18>([&](auto AI) { if constexpr (k3s.aslot[decltype(AI)::value] == n) issue_a(AI); });
    if constexpr (n < 108 && n % 6 == 0) {
      wait_vm<k3s.cntSlot[n] - k3s.posA[ks]>();
      sfor<K3_TG>([&](auto I) { if constexpr (decltype(I)::value == 0 || ks >= K3_NAG) { pin(hf[decltype(I)::value][ks][0]); pin(hf[decltype(I)::value][ks][1]); } else { pina(hf[decltype(I)::value][ks][0]); pina(hf[decltype(I)::value][ks][1]); } });
    }
    if constexpr (n == 7) {
      sfor<K3_TG>([&](auto I) { constexpr int i = decltype(I)::value;
        sfor<2>([&](auto J) { constexpr int th = decltype(J)::value;
          pin(stv[i][th][0]); pin(stv[i][th][1]);
          float sm = (__uint_as_float(stv[i][th][0][0]) + __uint_as_float(stv[i][th][0][2])) + (__uint_as_float(stv[i][th][1][0]) + __uint_as_float(stv[i][th][1][2]));
          float sq = (__uint_as_float(stv[i][th][0][1]) + __uint_as_float(stv[i][th][0][3])) + (__uint_as_float(stv[i][th][1][1]) + __uint_as_float(stv[i][th][1][3]));
          sm += __shfl_xor(sm, 16); sq += __shfl_xor(sq, 16);
          sm += __shfl_xor(sm, 32); sq += __shfl_xor(sq, 32);
          float mean = sm * (1.f / D_);
          float var = fmaxf(sq * (1.f / D_) - mean * mean, 0.f);
          float rstd = __builtin_amdgcn_rsqf(var + 1e-5f);
          c1[i][th] = -1.4426950408889634f * rstd;
          if (cs == 0 && lane < 16) ((float*)(ws_out + WS_R2))[(tt0 + i) * 32 + th * 16 + lane] = rstd * rstd;
        }); });
    }
    sfor<K3_TG>([&](auto I) { constexpr int i = decltype(I)::value;
      sfor<2>([&](auto J) { constexpr int th = decltype(J)::value;
        if constexpr (ks == 0) acc[ai][i][m][th] = mfma16(wf[n % (K3_D + 1)], hf[i][ks][th], (f32x4){});
        else acc[ai][i][m][th] = mfma16(wf[n % (K3_D + 1)], hf[i][ks][th], acc[ai][i][m][th]);
      });
      if constexpr (n >= 108) {
        constexpr int j = (n - 108) / 27, u0 = ((n - 108) % 27 - 2);
        if constexpr (K3_TG == 1) {
          if constexpr (u0 >= 0 && u0 < 24) epi_half(std::integral_constant<int, j>{}, I, std::integral_constant<int, u0>{});
        } else {
          if constexpr (u0 >= 0 && u0 < 24) { constexpr int uu = u0 * 2 + i; epi_half(std::integral_constant<int, j>{}, std::integral_constant<int, uu / 24>{}, std::integral_constant<int, uu % 24>{}); }
        }
      }
      sb0();
    });
  });
  sfor<K3_TG>([&](auto I) { sfor<24>([&](auto UI) { epi_half(std::integral_constant<int, 8>{}, I, UI); }); });
}

#define K4_LA 4
#define K4_LW 4
#define K4_DD 4
#define PART_ST 2
constexpr int K4_R = 8;
constexpr int K4_D = K4_DD;
constexpr int K4_LDS = K4_R * 16384;
constexpr int K4_NST = 21;
struct K4Sched {
  int aslot[18], sslot[K4_NST], posA[18], posS[K4_NST], cntBar[K4_NST], cntSlot[162];
  constexpr K4Sched() : aslot{}, sslot{}, posA{}, posS{}, cntBar{}, cntSlot{} {
    for (int a = 0; a < 18; ++a) aslot[a] = a < K4_LA ? -1 : 9 * (a - K4_LA) + 4;
    for (int s = 0; s < K4_NST; ++s) sslot[s] = s < K4_LW ? -1 : s - K4_LW;
    int c = 0;
    for (int s = 0; s < (K4_LW > K4_LA ? K4_LW : K4_LA); ++s) {
      if (s < K4_LW) { c += 2; posS[s] = c; }
      if (s < K4_LA) { c += 2; posA[s] = c; }
    }
    for (int n = -K4_D; n < 162; ++n) {
      if ((n + K4_D) % 8 == 0 && (n + K4_D) < 162) {
        int i = (n + K4_D) / 8; cntBar[i] = c;
        for (int s = 0; s < K4_NST; ++s) if (sslot[s] == i) { if (s < K4_NST - 1) c += 2; posS[s] = c; }
      }
      if (n >= 0) {
        for (int q = 0; q < 18; ++q) if (aslot[q] == n) { c += 2; posA[q] = c; }
        cntSlot[n] = c;
      }
    }
  }
};
constexpr K4Sched k4s{};
static_assert(K4_LW <= K4_R - 2, "ring");

__global__ __launch_bounds__(512, 2) void k4_down(const char* __restrict__ ws_in, char* __restrict__ ws_out) {
  extern __shared__ __attribute__((aligned(16))) char lds[];
  const int tid = threadIdx.x, lane = tid & 63, w = uni(tid >> 6);
  const int b = blockIdx.x, xcd = b & 7, idx = b >> 3;
  const int ksl = xcd >> 1, mt = (xcd & 1) * 8 + (idx & 7), ng = idx >> 3;
  const int tg = w & 3, kh = w >> 2;
  const int tt = mt * 4 + tg;
  const unsigned ldsb = (unsigned)(size_t)(LAS char*)lds;
  const char* wsrc = ws_in + WS_WD + (size_t)((ng * 4 + ksl) * 2 + kh) * WD_STREAM * 1024 + tg * 2048;
  const char* asrc = ws_in + WS_ACT + ((size_t)tt * 288 + ksl * 72 + kh * 36) * 1024;
  const unsigned voff = lane * 16;
  const int g = lane >> 4;
  const unsigned avoff = (g >> 1) * 1024 + (g & 1) * 512 + (lane & 15) * 16;
  u32x4 af[K4_LA + 1][2];
  auto issue_stage = [&](auto SI) {
    constexpr int s = decltype(SI)::value;
    constexpr int slot = s % K4_R;
    if (s < K4_NST - 1 || tg == 0) glds16x2(wsrc + (size_t)s * 8192, voff, ldsb + slot * 16384 + kh * 8192 + tg * 2048);
  };
  auto issue_a = [&](auto AI) {
    constexpr int a = decltype(AI)::value;
    gload16<(a % 2) * 2048>(af[a % (K4_LA + 1)][0], asrc + (a / 2) * 4096, avoff);
    gload16<(a % 2) * 2048 + 256>(af[a % (K4_LA + 1)][1], asrc + (a / 2) * 4096, avoff);
  };
  sfor<(K4_LW > K4_LA ? K4_LW : K4_LA)>([&](auto SI) {
    constexpr int s = decltype(SI)::value;
    if constexpr (s < K4_LW) issue_stage(SI);
    if constexpr (s < K4_LA) issue_a(SI);
  });
  f32x4 acc[9][2];
  u32x4 wf[K4_D + 1];
  unsigned lrd0 = ldsb + kh * 8192 + lane * 16, lrd1 = lrd0 + 65536;
  asm volatile("" : "+v"(lrd0), "+v"(lrd1));
  auto rd = [&](auto NI) {
    constexpr int n = decltype(NI)::value;
    if constexpr (n < 162) {
      constexpr int st = n / 8;
      if constexpr (n % 8 == 0) {
        wait_vm<(st < K4_NST - 1 ? k4s.cntBar[st] - k4s.posS[st] : 0)>();
        bar();
        sfor<K4_NST>([&](auto SI) { if constexpr (k4s.sslot[decltype(SI)::value] == st) issue_stage(SI); });
      }
      constexpr int off = (st % K4_R) * 16384 + (n % 8) * 1024;
      wf[n % (K4_D + 1)] = *(const LAS u32x4*)(size_t)((off < 65536 ? lrd0 : lrd1) + (off & 65535));
    }
  };
  sfor<K4_D>([&](auto I) { rd(I); });
  sfor<162>([&](auto NI) {
    constexpr int n = decltype(NI)::value;
    constexpr int ks = n / 9, t = n % 9, ab = ks % (K4_LA + 1);
    rd(std::integral_constant<int, n + K4_D>{});
    sfor<18>([&](auto AI) { if constexpr (k4s.aslot[decltype(AI)::value] == n) issue_a(AI); });
    if constexpr (t == 0) {
      wait_vm<k4s.cntSlot[n] - k4s.posA[ks]>();
      pin(af[ab][0]); pin(af[ab][1]);
    }
    if constexpr (ks == 0) {
      acc[t][0] = mfma16(wf[n % (K4_D + 1)], af[ab][0], (f32x4){});
      acc[t][1] = mfma16(wf[n % (K4_D + 1)], af[ab][1], (f32x4){});
    } else {
      acc[t][0] = mfma16(wf[n % (K4_D + 1)], af[ab][0], acc[t][0]);
      acc[t][1] = mfma16(wf[n % (K4_D + 1)], af[ab][1], acc[t][1]);
    }
    sb0();
  });
  wait_lgkm0(); bar();
  f32x4* xb = (f32x4*)lds;
#pragma unroll
  for (int t = 0; t < 9; ++t) xb[(w * 9 + t) * 64 + lane] = kh ? acc[t][0] : acc[t][1];
  wait_lgkm0(); bar();
  char* pp = ws_out + WS_PART + (((((size_t)ksl * 64 + tt) * 4 + ng) * 18) * 64 + lane) * 16;
#pragma unroll
  for (int t = 0; t < 9; ++t) {
    f32x4 o = xb[((w ^ 4) * 9 + t) * 64 + lane];
    f32x4 m = kh ? acc[t][1] : acc[t][0];
    store16<PART_ST>(pp + (t * 2 + kh) * 1024, __builtin_bit_cast(u32x4, o + m));
  }
}

__global__ __launch_bounds__(256) void k5_reduce(const char* __restrict__ ws, u16* __restrict__ out) {
  const int lane = threadIdx.x & 63;
  const int gw = blockIdx.x * 4 + (threadIdx.x >> 6);
  const int a = gw % 5, th = (gw / 5) & 1, ng = (gw / 10) & 3, tt = gw / 40;
  const int g = lane >> 4, tok = th * 16 + (lane & 15);
  const f32x4* pp = (const f32x4*)(ws + WS_PART) + (((size_t)tt * 4 + ng) * 18) * 64 + lane;
  constexpr size_t KS = (size_t)64 * 4 * 18 * 64;
  const char* hf = ws + WS_HF + (size_t)tt * 36 * 1024;
  auto hload = [&](int col) {
    const int ks = col >> 4, r = (col >> 3) & 1, h = (col >> 2) & 1;
    return *(const u32x2*)(hf + ks * 1024 + (h * 32 + tok) * 16 + r * 8);
  };
  u16* orow = out + (size_t)(tt * 32 + tok) * D_ + ng * 144;
  const float r2 = ((const float*)(ws + WS_R2))[tt * 32 + tok], mu = ((const float*)(ws + WS_MEAN))[tt * 32 + tok];
  if (a < 4) {
    f32x4 s0 = {}, s1 = {};
#pragma unroll
    for (int k = 0; k < 4; ++k) { s0 += pp[k * KS + ((2 * a) * 2 + th) * 64]; s1 += pp[k * KS + ((2 * a + 1) * 2 + th) * 64]; }
    s0 = s0 * r2 + mu; s1 = s1 * r2 + mu;
    const int col = 32 * a + 8 * g;
    u32x2 h0 = hload(ng * 144 + col), h1 = hload(ng * 144 + col + 4);
    u32x4 o;
    o[0] = pk2(s0[0] + bflo(h0[0]), s0[1] + bfhi(h0[0])); o[1] = pk2(s0[2] + bflo(h0[1]), s0[3] + bfhi(h0[1]));
    o[2] = pk2(s1[0] + bflo(h1[0]), s1[1] + bfhi(h1[0])); o[3] = pk2(s1[2] + bflo(h1[1]), s1[3] + bfhi(h1[1]));
    *(u32x4*)(orow + col) = o;
  } else {
    f32x4 s0 = {};
#pragma unroll
    for (int k = 0; k < 4; ++k) s0 += pp[k * KS + (8 * 2 + th) * 64];
    s0 = s0 * r2 + mu;
    const int col = 128 + 4 * g;
    u32x2 h0 = hload(ng * 144 + col);
    u32x2 o; o[0] = pk2(s0[0] + bflo(h0[0]), s0[1] + bfhi(h0[0])); o[1] = pk2(s0[2] + bflo(h0[1]), s0[3] + bfhi(h0[1]));
    *(u32x2*)(orow + col) = o;
  }
}
}
namespace fk {
constexpr size_t WS_PQ   = (WS_END_FFN + 4095) / 4096 * 4096;
constexpr size_t WS_KVP  = WS_PQ + SZ_HF;
constexpr size_t SZ_KVP  = (size_t)16 * 8 * 16 * 1024;
constexpr size_t WS_KVF  = WS_KVP + SZ_KVP;
constexpr size_t SZ_KVF  = 64 * 1024;
constexpr size_t WS_WOP  = WS_KVF + SZ_KVF;
constexpr size_t SZ_WOP  = (size_t)4 * 4 * 9 * 5 * 1024;
constexpr size_t WS_MX   = WS_WOP + SZ_WOP;
constexpr size_t WS_END  = WS_MX + S_ * 4;

DI float phi_f(float t) { return (t > 0.f ? t + 1.f : __builtin_amdgcn_exp2f(t * 1.4426950408889634f)) + 1e-6f; }

DI void pack_wo_task(const u16* wo, char* ws, int id) {
  const int lane = id & 63, fi = id >> 6, i = fi % 5, u = (fi / 5) % 9, w = (fi / 45) & 3, ng = fi / 180;
  const int row = lane & 31, h = lane >> 5;
  if (i == 4 && row >= 16) {
    if (row >= 18) *(u32x4*)(ws + WS_WOP + (size_t)id * 16) = (u32x4){0u, 0u, 0u, 0u};
    return;
  }
  const int n = ng * 144 + i * 32 + row;
  unsigned v[8];
#pragma unroll
  for (int j = 0; j < 8; ++j) {
    const int e = u < 8 ? 18 * (8 * w + u) + 8 * (j >> 2) + 4 * h + (j & 3) : 18 * (8 * w + 4 * h + (j >> 1)) + 16 + (j & 1);
    v[j] = wo[e * D_ + n];
  }
  *(u32x4*)(ws + WS_WOP + (size_t)id * 16) = (u32x4){v[0] | (v[1] << 16), v[2] | (v[3] << 16), v[4] | (v[5] << 16), v[6] | (v[7] << 16)};
}
DI void pack_worow_task(const u16* wo, char* ws, int e, int lane) {
  float s = 0.f;
#pragma unroll
  for (int i = 0; i < 9; ++i) s += bf2f(wo[e * D_ + i * 64 + lane]);
#pragma unroll
  for (int o = 32; o; o >>= 1) s += __shfl_xor(s, o);
  const unsigned hi = pk2(s, 0.f) & 0xffffu; const unsigned lo = pk2(s - bflo(hi), 0.f) & 0xffffu;
  const int hh = e / 18, el = e % 18, w = hh >> 3, ul = hh & 7;
  int u, h, j;
  if (el < 16) { u = ul; h = (el >> 2) & 1; j = (el >> 3) * 4 + (el & 3); } else { u = 8; h = ul >> 2; j = (ul & 3) * 2 + (el - 16); }
  if (lane < 8) {
    const int ng = lane >> 1, part = lane & 1;
    u16* p = (u16*)(ws + WS_WOP) + ((((size_t)(ng * 4 + w) * 9 + u) * 5 + 4) * 64 + h * 32 + 16 + part) * 8 + j;
    *p = (u16)(part ? lo : hi);
  }
}

#define K1_L 4
constexpr int K1_SLOT = 9216, K1_NSLOT = 16;
constexpr int K1_TAB = K1_SLOT * K1_NSLOT;
constexpr int K1_LDS = K1_TAB + 1024;
__global__ __launch_bounds__(512, 2) void k1_qkv(const u16* __restrict__ x, const u16* __restrict__ wqkv, const u16* __restrict__ wo,
                                                  const u16* __restrict__ wg, const u16* __restrict__ wu, char* __restrict__ ws) {
  extern __shared__ __attribute__((aligned(16))) char lds[];
  const int tid = threadIdx.x, lane = tid & 63, w = uni(tid >> 6);
  const int b = blockIdx.x, xcd = b & 7, idx = b >> 3;
  const unsigned ldsb = (unsigned)(size_t)(LAS char*)lds;
  if (idx >= 24) {
    const int pw = ((idx - 24) * 8 + xcd) * 8 + w;
    pack_gu_unit(wg, wu, ws, pw, ldsb + w * 16384, lane);
    for (int id = pw * 64 + lane; id < 4 * 4 * 9 * 5 * 64; id += 512 * 64) pack_wo_task(wo, ws, id);
    for (int e = pw; e < D_; e += 512) pack_worow_task(wo, ws, e, lane);
    return;
  }
  const int rt = (xcd & 3) * 4 + (idx & 3), cgl = idx >> 2;
  const int ch = xcd >> 2;
  const bool isq = cgl < 2;
  const int qg = ch * 2 + cgl, G = ch * 4 + (cgl - 2);
  const int g = lane >> 4, l15 = lane & 15;
  auto wsrc_off = [&](int n) -> unsigned {
    const int c = n * 64 + lane, p = c / 72, rem = c % 72, tile = rem >> 3, q = (rem >> 1) & 3, hf = rem & 1;
    const int kg = 4 * (p >> 2) + 2 * (p & 1) + ((p >> 1) & 1);
    int col;
    if (isq) col = 144 * qg + 16 * tile + 8 * hf;
    else if (tile < 4) col = 576 + 72 * G + 16 * tile + 8 * hf;
    else if (tile == 4) col = (hf ? 1152 : 576) + 72 * G + 64;
    else col = 1152 + 72 * G + 16 * (tile - 5) + 8 * hf;
    return (unsigned)(((4 * kg + q) * 1728 + col) * 2);
  };
  const unsigned woffA = wsrc_off(w), woffB = wsrc_off(8);
  const char* xsrc = (const char*)x + (size_t)(rt * 128 + w * 16) * 1152;
  const unsigned xoff = l15 * 1152 + g * 16;
  u32x4 xf[18];
  auto issue = [&](auto KI) {
    constexpr int ks = decltype(KI)::value, slot = ks % K1_NSLOT;
    const char* wb = (const char*)wqkv + (size_t)ks * 32 * 3456;
    if (w == (ks & 7)) glds16(wb, woffB, ldsb + slot * K1_SLOT + 8192);
    glds16(wb, woffA, ldsb + slot * K1_SLOT + w * 1024);
    gload16<ks * 64>(xf[ks], xsrc, xoff);
  };
  float* tab = (float*)(lds + K1_TAB);
  if (tid < 144) tab[tid] = 0.f;
  sfor<K1_L>([&](auto I) { issue(I); });
  f32x4 acc[9], accs[9];
#pragma unroll
  for (int t = 0; t < 9; ++t) { acc[t] = (f32x4){}; accs[t] = (f32x4){}; }
  float sm = 0.f, sq = 0.f;
  unsigned rb[3];
  rb[0] = ldsb + (4 * (g >> 1) + (g & 1)) * 1152 + l15 * 8; rb[1] = rb[0] + 7 * K1_SLOT; rb[2] = rb[0] + 14 * K1_SLOT;
  asm volatile("" : "+v"(rb[0]), "+v"(rb[1]), "+v"(rb[2]));
  const u32x4 ones = {0x3f803f80u, 0x3f803f80u, 0x3f803f80u, 0x3f803f80u};
  sfor<18>([&](auto KI) {
    constexpr int ks = decltype(KI)::value, slot = ks % K1_NSLOT;
    constexpr int later = (ks + K1_L < 18 ? K1_L - 1 : 17 - ks);
    wait_vm<2 * later>();
    pin(xf[ks]);
    if constexpr (ks == 0) wait_lgkm0();
    bar();
    if constexpr (ks + K1_L < 18) issue(std::integral_constant<int, ks + K1_L>{});
    u32x4 wfr[9];
#pragma unroll
    for (int t = 0; t < 9; ++t) {
      const unsigned a = rb[slot / 7] + (slot % 7) * K1_SLOT + t * 128;
      u32x2 lo = __builtin_bit_cast(u32x2, tr_read(a)), hi = __builtin_bit_cast(u32x2, tr_read(a + 2304));
      wfr[t] = (u32x4){lo[0], lo[1], hi[0], hi[1]};
    }
#pragma unroll
    for (int i = 0; i < 4; ++i) { const unsigned xv = xf[ks][i]; sm = dot2_ones(xv, sm); sq = __builtin_amdgcn_fdot2_f32_bf16(as_bf2(xv), as_bf2(xv), sq, false); }
    if (isq) {
#pragma unroll
      for (int t = 0; t < 9; ++t) acc[t] = mfma16(wfr[t], xf[ks], acc[t]);
    } else {
#pragma unroll
      for (int t = 0; t < 9; ++t) acc[t] = mfma16(xf[ks], wfr[t], acc[t]);
    }
    if (w == (ks & 7)) {
#pragma unroll
      for (int t = 0; t < 9; ++t) accs[t] = mfma16(ones, wfr[t], accs[t]);
    }
  });
  if (g == 0) {
#pragma unroll
    for (int t = 0; t < 9; ++t) atomicAdd(&tab[t * 16 + l15], accs[t][0]);
  }
  sm += __shfl_xor(sm, 16); sq += __shfl_xor(sq, 16);
  sm += __shfl_xor(sm, 32); sq += __shfl_xor(sq, 32);
  const float mean = sm * (1.f / D_);
  const float rstd = __builtin_amdgcn_rsqf(fmaxf(sq * (1.f / D_) - mean * mean, 0.f) + 1e-5f);
  wait_lgkm0(); bar();
  if (isq) {
    const int tok128 = w * 16 + l15, tt = rt * 4 + (tok128 >> 5);
    if (qg == 0 && g == 0) ((float*)(ws + WS_MX))[rt * 128 + tok128] = mean;
    const float nmr = -mean * rstd;
    unsigned pk[9][2];
#pragma unroll
    for (int t = 0; t < 9; ++t) {
      const f32x4 cs = *(const f32x4*)(tab + t * 16 + g * 4);
      float v[4];
#pragma unroll
      for (int j = 0; j < 4; ++j) v[j] = phi_f(fmaf(acc[t][j], rstd, nmr * cs[j]));
      pk[t][0] = pk2(v[0], v[1]); pk[t][1] = pk2(v[2], v[3]);
    }
    char* pq = ws + WS_PQ + ((size_t)tt * 36 + qg * 9) * 1024 + ((g & 1) * 32 + (tok128 & 31)) * 16;
#pragma unroll
    for (int a = 0; a < 4; ++a) {
      auto r0 = __builtin_amdgcn_permlane32_swap(pk[2 * a][0], pk[2 * a + 1][0], false, false);
      auto r1 = __builtin_amdgcn_permlane32_swap(pk[2 * a][1], pk[2 * a + 1][1], false, false);
      *(u32x4*)(pq + (2 * a + (g >> 1)) * 1024) = (u32x4){r0[0], r1[0], r0[1], r1[1]};
    }
    *(u32x2*)(pq + 8 * 1024 + (g >> 1) * 8) = (u32x2){pk[8][0], pk[8][1]};
  } else {
    float rs[4], nm[4];
#pragma unroll
    for (int j = 0; j < 4; ++j) { rs[j] = __shfl(rstd, 4 * g + j); nm[j] = -__shfl(mean, 4 * g + j) * rs[j]; }
    char* ob = lds + ((w >> 1) * 64 + lane) * 16 + (w & 1) * 8;
#pragma unroll
    for (int t = 0; t < 9; ++t) {
      const float cs = tab[t * 16 + l15];
      float v[4];
#pragma unroll
      for (int j = 0; j < 4; ++j) {
        const float val = fmaf(acc[t][j], rs[j], nm[j] * cs);
        v[j] = (t < 4) ? phi_f(val) : (t > 4 ? val : (l15 < 8 ? phi_f(val) : val));
      }
      const u32x2 pv = {pk2(v[0], v[1]), pk2(v[2], v[3])};
      *(u32x2*)(ob + t * 4096) = pv;
      if (t == 4) *(u32x2*)(ob + 9 * 4096) = l15 < 8 ? (u32x2){0x3f803f80u, 0x3f803f80u} : pv;
    }
    wait_lgkm0(); bar();
#pragma unroll
    for (int pi = 0; pi < 2; ++pi) {
      const int p = 2 * w + pi;
      int a, bt;
      if (p < 10) { a = (p + 1) / 3; bt = 5 + a + ((p + 1) % 3) - 1; } else if (p == 10) { a = 4; bt = 8; } else { a = p - 11; bt = 9; }
      f32x4 c = {};
#pragma unroll
      for (int i = 0; i < 4; ++i) {
        const u32x4 av = *(const u32x4*)(lds + a * 4096 + (i * 64 + lane) * 16);
        const u32x4 bv = *(const u32x4*)(lds + bt * 4096 + (i * 64 + lane) * 16);
        c = mfma16(av, bv, c);
      }
      *(f32x4*)(ws + WS_KVP + (((size_t)rt * 8 + G) * 16 + p) * 1024 + lane * 16) = c;
    }
  }
}

__global__ __launch_bounds__(256) void k1b_reduce(const u16* __restrict__ wg, const u16* __restrict__ wu, const u16* __restrict__ wd, char* __restrict__ ws) {
  extern __shared__ __attribute__((aligned(16))) char lds[];
  const int tid = threadIdx.x, lane = tid & 63, w = uni(tid >> 6);
  const int b = blockIdx.x;
  if (b >= 32) {
    const int gw = (b - 32) * 4 + w;
    const unsigned ldsb = (unsigned)(size_t)(LAS char*)lds + w * 16384;
    if (gw < 136) pack_gu_unit(wg, wu, ws, 512 + gw, ldsb, lane);
    else if (gw < 136 + 576) pack_wd_unit(wd, ws, gw - 136, ldsb, lane);
    return;
  }
  const int hh = b, s = tid >> 7, l = (tid >> 1) & 63, r = tid & 1;
  const int row = l & 31, h = l >> 5;
  const int T = (18 * hh) / 16 + s;
  const int d0 = 16 * T + 8 * r + 4 * h;
  const int G = hh >> 2, hl = hh & 3;
  int el = -1; bool isz = false;
  if (row < 16) el = row;
  else if (row == 18 || row == 22) isz = true;
  else if ((hh & 7) < 4 ? (row == 16 || row == 17) : (row == 20 || row == 21)) el = 16 + (row & 1);
  const int dlo = 18 * hh, dhi = dlo + 18;
  f32x4 sum = {};
  if ((el >= 0 || isz) && d0 + 3 >= dlo && d0 < dhi) {
    const int dk = d0 - 72 * G, a = dk >> 4, dr = dk & 15;
    int p, col;
    if (isz) { p = 11 + a; col = 0; }
    else {
      const int ev = 18 * hl + el;
      if (ev >= 64) { p = 11 + a; col = 8 + ev - 64; }
      else { const int vt = ev >> 4; col = ev & 15; p = a == 4 ? 10 : 3 * a + (vt - a); }
    }
    const char* src = ws + WS_KVP + ((size_t)G * 16 + p) * 1024 + ((dr >> 2) * 16 + col) * 16;
    f32x4 v[16];
#pragma unroll
    for (int i = 0; i < 16; ++i) v[i] = *(const f32x4*)(src + (size_t)i * 8 * 16 * 1024);
#pragma unroll
    for (int i = 0; i < 16; ++i) sum += v[i];
#pragma unroll
    for (int j = 0; j < 4; ++j) if (d0 + j < dlo || d0 + j >= dhi) sum[j] = 0.f;
  }
  *(u32x2*)(ws + WS_KVF + ((size_t)(hh * 2 + s) * 64 + l) * 16 + r * 8) = (u32x2){pk2(sum[0], sum[1]), pk2(sum[2], sum[3])};
}

constexpr int K2_LDS = 4 * 5 * 16 * 64 * 4;
__global__ __launch_bounds__(256, 1) void k2_attn_out(const u16* __restrict__ x, const char* __restrict__ ws_in, char* __restrict__ ws) {
  extern __shared__ __attribute__((aligned(16))) char lds[];
  const int tid = threadIdx.x, lane = tid & 63, w = uni(tid >> 6);
  const int b = blockIdx.x, xcd = b & 7, idx = b >> 3;
  const int tt = xcd * 8 + (idx & 7), ng = idx >> 3;
  const int tok = lane & 31, h = lane >> 5;
  const u32x4* pqp = (const u32x4*)(ws_in + WS_PQ + ((size_t)tt * 36 + w * 9) * 1024) + lane;
  const u32x4* kvp = (const u32x4*)(ws_in + WS_KVF + (size_t)w * 16 * 1024) + lane;
  const u32x4* wop = (const u32x4*)(ws_in + WS_WOP + (size_t)(ng * 4 + w) * 45 * 1024) + lane;
  u32x4 pq[9], kvf[16], wf[45];
#pragma unroll
  for (int i = 0; i < 9; ++i) pq[i] = pqp[i * 64];
#pragma unroll
  for (int i = 0; i < 16; ++i) kvf[i] = kvp[i * 64];
#pragma unroll
  for (int i = 0; i < 45; ++i) wf[i] = wop[i * 64];
  const int u0 = w == 0 ? 0 : 2 * w + 1;
  u32x2 xr[3][2];
  const char* xrow = (const char*)x + (size_t)(tt * 32 + tok) * 1152 + (ng * 144 + 4 * h) * 2;
#pragma unroll
  for (int i = 0; i < 3; ++i) {
    const int u = (i < 2 || w == 0) ? u0 + i : u0;
    xr[i][0] = *(const u32x2*)(xrow + u * 32); xr[i][1] = *(const u32x2*)(xrow + u * 32 + 16);
  }
  const float mx = ((const float*)(ws_in + WS_MX))[tt * 32 + tok];
  u32x4 of[9]; unsigned lo[8];
#pragma unroll
  for (int u = 0; u < 8; ++u) {
    const int T0 = (18 * u) / 16;
    f32x16 c = mfma32(kvf[2 * u], pq[T0], (f32x16){});
    c = mfma32(kvf[2 * u + 1], pq[T0 + 1], c);
    const float r = 1.f / (c[10] + 1e-6f);
    of[u] = (u32x4){pk2(c[0] * r, c[1] * r), pk2(c[2] * r, c[3] * r), pk2(c[4] * r, c[5] * r), pk2(c[6] * r, c[7] * r)};
    lo[u] = pk2(c[8] * r, c[9] * r);
  }
  of[8] = (u32x4){h ? lo[4] : lo[0], h ? lo[5] : lo[1], h ? lo[6] : lo[2], h ? lo[7] : lo[3]};
  f32x16 acc[5];
#pragma unroll
  for (int i = 0; i < 5; ++i) acc[i] = (f32x16){};
#pragma unroll
  for (int u = 0; u < 9; ++u)
#pragma unroll
    for (int i = 0; i < 5; ++i) acc[i] = mfma32(wf[u * 5 + i], of[u], acc[i]);
  f32x4* xb = (f32x4*)lds;
#pragma unroll
  for (int u = 0; u < 10; ++u) {
    const int i = u >> 1, rb = (u & 1) * 8;
    xb[((w * 10 + u) * 2 + 0) * 64 + lane] = (f32x4){acc[i][rb], acc[i][rb + 1], acc[i][rb + 2], acc[i][rb + 3]};
    if (u < 9) xb[((w * 10 + u) * 2 + 1) * 64 + lane] = (f32x4){acc[i][rb + 4], acc[i][rb + 5], acc[i][rb + 6], acc[i][rb + 7]};
  }
  __syncthreads();
  float ms = 0.f;
#pragma unroll
  for (int k = 0; k < 4; ++k) { const f32x4 v = xb[((k * 10 + 9) * 2) * 64 + tok]; ms += v[0] + v[1]; }
  const float mean = mx + ms * (1.f / D_);
  if (ng == 0 && w == 0 && h == 0) ((float*)(ws + WS_MEAN))[tt * 32 + tok] = mean;
  float s1 = 0.f, s2 = 0.f;
#pragma unroll
  for (int i = 0; i < 3; ++i) {
    if (i < 2 || w == 0) {
      const int u = u0 + i;
      f32x4 a = {}, c = {};
#pragma unroll
      for (int k = 0; k < 4; ++k) { a += xb[((k * 10 + u) * 2 + 0) * 64 + lane]; c += xb[((k * 10 + u) * 2 + 1) * 64 + lane]; }
      u32x4 o;
      o[0] = pk2(a[0] + bflo(xr[i][0][0]) - mean, a[1] + bfhi(xr[i][0][0]) - mean);
      o[1] = pk2(a[2] + bflo(xr[i][0][1]) - mean, a[3] + bfhi(xr[i][0][1]) - mean);
      o[2] = pk2(c[0] + bflo(xr[i][1][0]) - mean, c[1] + bfhi(xr[i][1][0]) - mean);
      o[3] = pk2(c[2] + bflo(xr[i][1][1]) - mean, c[3] + bfhi(xr[i][1][1]) - mean);
#pragma unroll
      for (int j = 0; j < 4; ++j) { const unsigned ov = o[j]; s1 = dot2_ones(ov, s1); s2 = __builtin_amdgcn_fdot2_f32_bf16(as_bf2(ov), as_bf2(ov), s2, false); }
      *(u32x4*)(ws + WS_HF + (((size_t)tt * 36 + ng * 9 + u) * 64 + lane) * 16) = o;
    }
  }
  s1 += __shfl_xor(s1, 32); s2 += __shfl_xor(s2, 32);
  if (h == 0) *(f32x2*)(ws + WS_HST + ((size_t)(tt * 32 + tok) * 16 + ng * 4 + w) * 8) = (f32x2){s1, s2};
}
}
extern "C" void kernel_launch(void* const* d_in, const int* in_sizes, int n_in, void* d_out, int out_size, void* d_ws, size_t ws_size, hipStream_t st) {
  using namespace fk;
  const u16* x = (const u16*)d_in[0]; const u16* wqkv = (const u16*)d_in[1]; const u16* wo = (const u16*)d_in[2];
  const u16* wg = (const u16*)d_in[4]; const u16* wu = (const u16*)d_in[5]; const u16* wd = (const u16*)d_in[6];
  char* ws = (char*)d_ws;
  (void)hipFuncSetAttribute((const void*)k1_qkv, hipFuncAttributeMaxDynamicSharedMemorySize, K1_LDS);
  (void)hipFuncSetAttribute((const void*)k1b_reduce, hipFuncAttributeMaxDynamicSharedMemorySize, 65536);
  (void)hipFuncSetAttribute((const void*)k2_attn_out, hipFuncAttributeMaxDynamicSharedMemorySize, K2_LDS);
  (void)hipFuncSetAttribute((const void*)k3_gateup, hipFuncAttributeMaxDynamicSharedMemorySize, K3_LDS);
  (void)hipFuncSetAttribute((const void*)k4_down, hipFuncAttributeMaxDynamicSharedMemorySize, K4_LDS);
  k1_qkv<<<256, 512, K1_LDS, st>>>(x, wqkv, wo, wg, wu, ws);
  k1b_reduce<<<32 + (136 + 576) / 4, 256, 65536, st>>>(wg, wu, wd, ws);
  k2_attn_out<<<256, 256, K2_LDS, st>>>(x, ws, ws);
  k3_gateup<<<256, 64 * K3_NW, K3_LDS, st>>>(ws, ws);
  k4_down<<<256, 512, K4_LDS, st>>>(ws, ws);
  k5_reduce<<<640, 256, 0, st>>>(ws, (u16*)d_out);
}
```

```cpp
#include <hip/hip_runtime.h>
#include <hip/hip_bf16.h>
#include <stdint.h>
#include <type_traits>
#include <utility>

namespace fk {
typedef unsigned short u16;
typedef short bf16x8 __attribute__((ext_vector_type(8)));
typedef short s16x4 __attribute__((ext_vector_type(4)));
typedef float f32x4 __attribute__((ext_vector_type(4)));
typedef float f32x2 __attribute__((ext_vector_type(2)));
typedef float f32x16 __attribute__((ext_vector_type(16)));
typedef unsigned u32x4 __attribute__((ext_vector_type(4)));
typedef unsigned u32x2 __attribute__((ext_vector_type(2)));
typedef __bf16 bf16x2v __attribute__((ext_vector_type(2)));
#define LAS __attribute__((address_space(3)))
#define DI __device__ __forceinline__

constexpr int S_ = 2048, D_ = 576, H_ = 32, DH_ = 18, F_ = 4608;

template <int N, class Fn, int... I> DI void sfor_impl(Fn&& f, std::integer_sequence<int, I...>) { (f(std::integral_constant<int, I>{}), ...); }
template <int N, class Fn> DI void sfor(Fn&& f) { sfor_impl<N>(f, std::make_integer_sequence<int, N>{}); }

template <int N> DI void wait_vm() { asm volatile("s_waitcnt vmcnt(%0)" ::"i"(N) : "memory"); }
DI void wait_lgkm0() { asm volatile("s_waitcnt lgkmcnt(0)" ::: "memory"); }
DI void bar() { asm volatile("" ::: "memory"); __builtin_amdgcn_s_barrier(); asm volatile("" ::: "memory"); }
template <class T> DI void pin(T& v) { asm volatile("" : "+v"(v)); }

DI void glds16(const void* sbase, unsigned voff, unsigned ldsdst) {
  asm volatile("s_mov_b32 m0, %2\n\ts_nop 0\n\tglobal_load_lds_dwordx4 %0, %1" :: "v"(voff), "s"(sbase), "s"(ldsdst) : "memory");
}
DI void glds16x2(const void* sbase, unsigned voff, unsigned ldsdst) {
  asm volatile("s_mov_b32 m0, %2\n\ts_nop 0\n\tglobal_load_lds_dwordx4 %0, %1\n\tglobal_load_lds_dwordx4 %0, %1 offset:1024" :: "v"(voff), "s"(sbase), "s"(ldsdst) : "memory");
}
DI void glds16v(const void* gptr, unsigned ldsdst) {
  asm volatile("s_mov_b32 m0, %1\n\ts_nop 0\n\tglobal_load_lds_dwordx4 %0, off" :: "v"(gptr), "s"(ldsdst) : "memory");
}
DI void sb0() { __builtin_amdgcn_sched_barrier(0); }
template <int OFF> DI void gload16(u32x4& dst, const void* sbase, unsigned voff) {
  asm volatile("global_load_dwordx4 %0, %1, %2 offset:%3" : "=v"(dst) : "v"(voff), "s"(sbase), "i"(OFF) : "memory");
}
template <int OFF> DI void gload8(u32x2& dst, const void* sbase, unsigned voff) {
  asm volatile("global_load_dwordx2 %0, %1, %2 offset:%3" : "=v"(dst) : "v"(voff), "s"(sbase), "i"(OFF) : "memory");
}
template <int OFF> DI void gload4(unsigned& dst, const void* sbase, unsigned voff) {
  asm volatile("global_load_dword %0, %1, %2 offset:%3" : "=v"(dst) : "v"(voff), "s"(sbase), "i"(OFF) : "memory");
}
template <int OFF> DI void gload16a(u32x4& dst, const void* sbase, unsigned voff) {
  asm volatile("global_load_dwordx4 %0, %1, %2 offset:%3" : "=a"(dst) : "v"(voff), "s"(sbase), "i"(OFF) : "memory");
}
DI void l2touch(const void* sbase, unsigned voff, unsigned ldsdummy) {
  asm volatile("s_mov_b32 m0, %2\n\ts_nop 0\n\tglobal_load_lds_dword %0, %1" :: "v"(voff), "s"(sbase), "s"(ldsdummy) : "memory");
}
template <class T> DI void pina(T& v) { asm volatile("" : "+a"(v)); }
template <int MODE> DI void store16(void* p, u32x4 v) {
  if constexpr (MODE == 0) *(u32x4*)p = v;
  else if constexpr (MODE == 1) __builtin_nontemporal_store(v, (u32x4*)p);
  else if constexpr (MODE == 2) asm volatile("global_store_dwordx4 %0, %1, off sc1\n\ts_nop 1" :: "v"(p), "v"(v) : "memory");
  else if constexpr (MODE == 3) asm volatile("global_store_dwordx4 %0, %1, off sc0 sc1\n\ts_nop 1" :: "v"(p), "v"(v) : "memory");
  else if constexpr (MODE == 4) asm volatile("global_store_dwordx4 %0, %1, off sc1 nt\n\ts_nop 1" :: "v"(p), "v"(v) : "memory");
  else asm volatile("" :: "v"(v));
}
DI float bf2f(u16 v) { return __uint_as_float(((unsigned)v) << 16); }
DI float bflo(unsigned v) { return __uint_as_float(v << 16); }
DI float bfhi(unsigned v) { return __uint_as_float(v & 0xffff0000u); }
DI unsigned pk2(float lo, float hi) {
  bf16x2v r; r[0] = (__bf16)lo; r[1] = (__bf16)hi; return __builtin_bit_cast(unsigned, r);
}
DI float rbf(float v) { return bflo(pk2(v, 0.f)); }
DI float ln_rstd(float var) { return __builtin_amdgcn_rcpf(rbf(__builtin_amdgcn_sqrtf(rbf(rbf(fmaxf(var, 0.f)) + 1e-5f)))); }
DI s16x4 tr_read(unsigned lds_addr) {
  return __builtin_amdgcn_ds_read_tr16_b64_v4i16((LAS s16x4*)(size_t)lds_addr);
}
DI f32x16 mfma32(u32x4 a, u32x4 b, f32x16 c) { return __builtin_amdgcn_mfma_f32_32x32x16_bf16(__builtin_bit_cast(bf16x8, a), __builtin_bit_cast(bf16x8, b), c, 0, 0, 0); }
DI f32x4 mfma16(u32x4 a, u32x4 b, f32x4 c) { return __builtin_amdgcn_mfma_f32_16x16x32_bf16(__builtin_bit_cast(bf16x8, a), __builtin_bit_cast(bf16x8, b), c, 0, 0, 0); }
#define TLSCOPE(k)
DI void tlmark(int) {}
DI void ckmark(int) {}
DI int uni(int v) { return __builtin_amdgcn_readfirstlane(v); }
}
namespace fk {
DI bf16x2v as_bf2(unsigned v) { return __builtin_bit_cast(bf16x2v, v); }
DI float dot2_ones(unsigned v, float c) { return __builtin_amdgcn_fdot2_f32_bf16(as_bf2(v), as_bf2(0x3f803f80u), c, false); }

constexpr size_t WS_HF   = 0;
constexpr size_t SZ_HF   = (size_t)S_ * D_ * 2;
constexpr size_t WS_HST  = WS_HF + SZ_HF;
constexpr size_t SZ_HST  = (size_t)S_ * 16 * 8;
constexpr size_t WS_WGU  = WS_HST + SZ_HST;
constexpr size_t SZ_WGU  = (size_t)288 * 36 * 1024;
constexpr int    WD_STREAM = 168;
constexpr size_t WS_WD   = WS_WGU + SZ_WGU + 16384;
constexpr size_t SZ_WD   = (size_t)32 * WD_STREAM * 1024;
constexpr size_t WS_ACT  = WS_WD + SZ_WD;
constexpr size_t SZ_ACT  = (size_t)S_ * F_ * 2;
constexpr size_t WS_PART = WS_ACT + SZ_ACT;
constexpr size_t SZ_PART = (size_t)4 * S_ * D_ * 4;
constexpr size_t WS_R2   = WS_PART + SZ_PART;
constexpr size_t WS_MEAN = WS_R2 + S_ * 4;
constexpr size_t WS_CNT  = WS_MEAN + S_ * 4;
constexpr size_t WS_END_FFN = WS_CNT + 65 * 128;

template <int ST> DI void pack_gu_unit(const u16* wg, const u16* wu, char* ws, int lc, int kp, unsigned ldsb, int lane) {
  const unsigned voff = (unsigned)(((kp * 64 + (lane >> 3)) * F_ + lc * 64 + (lane & 7) * 8) * 2);
  sfor<16>([&](auto I) {
    constexpr int i = decltype(I)::value, s = i >> 3, mat = (i >> 2) & 1, r8 = s * 4 + (i & 3);
    glds16((const char*)(mat ? wu : wg) + (size_t)r8 * 8 * F_ * 2, voff, ldsb + mat * 8192 + r8 * 1024);
  });
  const int g = lane >> 4;
  const unsigned rbase = ldsb + (16 * (g >> 1) + 4 * (g & 1) + ((lane >> 2) & 3)) * 128 + (lane & 3) * 8;
  char* dst = ws + WS_WGU + ((size_t)(lc * 4) * 18 + kp * 2) * 2048 + lane * 16;
  sfor<2>([&](auto I) {
    constexpr int s = decltype(I)::value;
    wait_vm<8>();
    sfor<8>([&](auto J) {
      constexpr int t = decltype(J)::value >> 1, mat = decltype(J)::value & 1;
      s16x4 lo = tr_read(rbase + s * 4096 + mat * 8192 + t * 32);
      s16x4 hi = tr_read(rbase + s * 4096 + mat * 8192 + t * 32 + 1024);
      u32x2 a = __builtin_bit_cast(u32x2, lo), b = __builtin_bit_cast(u32x2, hi);
      u32x4 v = {a[0], a[1], b[0], b[1]};
      store16<ST>(dst + (t * 18 + s) * 2048 + mat * 1024, v);
    });
  });
}
template <int ST> DI void pack_wd_unit(const u16* wd, char* ws, int S, int c2, unsigned ldsb, int lane) {
  const char* base = (const char*)wd + ((size_t)S * 32 * D_ + c2 * 288) * 2;
  sfor<9>([&](auto I) {
    constexpr int n = decltype(I)::value;
    const int c = n * 64 + lane, row = c / 36, cc = c % 36;
    const unsigned vo = (unsigned)(row * D_ * 2 + cc * 16);
    glds16(base, vo, ldsb + n * 1024);
    glds16(base + 16 * D_ * 2, vo, ldsb + 9216 + n * 1024);
  });
  wait_vm<0>();
  const int g = lane >> 4, q = (lane >> 2) & 3, p = lane & 3;
  const unsigned rb = ldsb + (16 * (g >> 1) + 4 * (g & 1) + q) * 576;
  const int ksl = S / 36, kh = (S % 36) / 18, ks = S % 18;
  char* dst = ws + WS_WD + ((size_t)((c2 * 2 * 4 + ksl) * 2 + kh) * WD_STREAM + ks * 9) * 1024 + lane * 16;
  sfor<18>([&](auto I) {
    constexpr int ngl = decltype(I)::value / 9, t = decltype(I)::value % 9;
    const unsigned a0 = rb + ngl * 288 + (t < 8 ? 64 * (t >> 1) + 16 * p + 8 * (t & 1) : 256 + 8 * p);
    s16x4 lo = tr_read(a0);
    s16x4 hi = tr_read(a0 + 8 * 576);
    u32x2 a = __builtin_bit_cast(u32x2, lo), b = __builtin_bit_cast(u32x2, hi);
    u32x4 v = {a[0], a[1], b[0], b[1]};
    store16<ST>(dst + (size_t)ngl * 8 * WD_STREAM * 1024 + t * 1024, v);
  });
}

constexpr int K3_LDS = 144 * 1024 + 512;
#define K3_P 2
constexpr int K3_Q = K3_P + 1;
DI void k3_wload(u32x4 (&dst)[4], const char* base, int s, int rot, unsigned voff) {
  const int sa = s + rot >= 9 ? s + rot - 9 : s + rot;
  sfor<4>([&](auto FI) { constexpr int f = decltype(FI)::value; gload16<f * 1024>(dst[f], base + sa * 4096, voff); });
}
template <int NTK, bool FIRST, bool NEXT, int QB, int NST, class HL>
DI void k3_unit(f32x4 (&acc)[2][8], u32x4 (&wq)[K3_Q][4], const char* cur, const char* nxt, const unsigned (&lb)[3], int rot, unsigned voff, HL&& hload) {
  constexpr int NG = NTK / 4;
  u32x4 af[2][4];
  auto aread = [&](auto II) {
    constexpr int it = decltype(II)::value, ks = it / NG, gp = it % NG;
#pragma unroll
    for (int tk = 0; tk < 4; ++tk) af[it & 1][tk] = *(const LAS u32x4*)(size_t)(lb[ks / 6] + ((ks % 6) * 8 + gp * 4 + tk) * 1024);
  };
  aread(std::integral_constant<int, 0>{});
  sfor<9>([&](auto SI) {
    constexpr int s = decltype(SI)::value;
    if constexpr (FIRST) {
      if constexpr (s + 2 < 9) k3_wload(wq[(QB + s + 2) % K3_Q], cur, s + 2, rot, voff);
      if constexpr (s < 6) { hload(std::integral_constant<int, 6 + 2 * s>{}); hload(std::integral_constant<int, 7 + 2 * s>{}); }
      constexpr int n0 = s < 6 ? 0 : (K3_P * (s - 6)) / 3, n1 = s < 6 ? 0 : (K3_P * (s - 5)) / 3;
      constexpr int m0 = K3_P == 2 ? (s < 7 ? 0 : s - 7) : n0, m1 = K3_P == 2 ? (s < 7 ? 0 : s - 6) : n1;
      sfor<m1 - m0>([&](auto JI) { constexpr int j = m0 + decltype(JI)::value; static_assert(9 + j - K3_Q < s, "slot busy"); k3_wload(wq[(QB + 9 + j) % K3_Q], nxt, j, rot, voff); });
      constexpr int y = s <= 5 ? 12 : (s == 6 ? 4 + 2 + 4 : (s == 7 ? 4 : 0));
      wait_vm<y + 4 * m1>();
    } else {
      if constexpr (s + K3_P < 9) k3_wload(wq[(QB + s + K3_P) % K3_Q], cur, s + K3_P, rot, voff);
      else if constexpr (NEXT) k3_wload(wq[(QB + s + K3_P) % K3_Q], nxt, s + K3_P - 9, rot, voff);
      constexpr int ny = NEXT ? K3_P : ((8 - s) < K3_P ? (8 - s) : K3_P);
      wait_vm<4 * ny + (s < K3_P ? NST : 0)>();
    }
    sfor<4>([&](auto FI) { pin(wq[(QB + s) % K3_Q][decltype(FI)::value]); });
    if constexpr (FIRST && s < 8) bar();
    sfor<2 * NG>([&](auto QI) {
      constexpr int q = decltype(QI)::value, it = s * 2 * NG + q, ks = it / NG, gp = it % NG, kq = ks & 1;
      if constexpr (it + 1 < 18 * NG) aread(std::integral_constant<int, it + 1>{});
      sb0();
#pragma unroll
      for (int f = 0; f < 2; ++f)
#pragma unroll
        for (int tk = 0; tk < 4; ++tk)
          acc[f][gp * 4 + tk] = mfma16(wq[(QB + s) % K3_Q][kq * 2 + f], af[it & 1][tk], ks == 0 ? (f32x4){} : acc[f][gp * 4 + tk]);
      sb0();
    });
  });
}

template <int HALF>
DI void k3_body(const char* __restrict__ ws_in, char* __restrict__ ws_out, char* lds, int w) {
  TLSCOPE(3);
  const int tid = threadIdx.x, lane = tid & 63;
  const int b = blockIdx.x, xcd = b & 7, idx = b >> 3;
  const int rt = (xcd & 1) * 8 + (idx & 7), fgp = (xcd >> 1) * 4 + (idx >> 3);
  const unsigned ldsb = (unsigned)(size_t)(LAS char*)lds;
  const unsigned voff = lane * 16;
  const int g = lane >> 4, l15 = lane & 15;
  const unsigned avoff = (g >> 1) * 1024 + (g & 1) * 512 + l15 * 16;
  const char* hsrc = ws_in + WS_HF + (size_t)(rt * 4 + (w >> 1)) * 36 * 1024 + (w & 1) * 256;
#define K3_ROT 0
  const int rot = K3_ROT;
  auto hload = [&](auto I) { constexpr int i = decltype(I)::value; const int ia = i + 2 * rot >= 18 ? i + 2 * rot - 18 : i + 2 * rot;
    glds16(hsrc + ia * 2048, avoff, ldsb + (i * 8 + w) * 1024); };
  auto nohl = [&](auto) {};
  const int t0 = w, t1 = w + 8, t2 = 16 + ((w >> 1) & 1);
  const char* wgu = ws_in + WS_WGU + (size_t)fgp * 18 * 36 * 1024;
  const char* sA = wgu + (size_t)t0 * 36 * 1024;
  const char* sB = wgu + (size_t)t1 * 36 * 1024;
  const char* sC = wgu + (size_t)t2 * 36 * 1024;
  u32x4 wq[K3_Q][4];
  u32x4 stv[2];
  {
    const char* sp = ws_in + WS_HST + (size_t)(rt * 128 + w * 16) * 128;
    gload16<0>(stv[0], sp, l15 * 128 + g * 32); gload16<16>(stv[1], sp, l15 * 128 + g * 32);
  }
  hload(std::integral_constant<int, 0>{}); hload(std::integral_constant<int, 1>{});
  k3_wload(wq[0], sA, 0, rot, voff);
  hload(std::integral_constant<int, 2>{}); hload(std::integral_constant<int, 3>{});
  k3_wload(wq[1], sA, 1, rot, voff);
  hload(std::integral_constant<int, 4>{}); hload(std::integral_constant<int, 5>{});
  wait_vm<12>();
  float* ctab = (float*)(lds + 144 * 1024);
  {
    pin(stv[0]); pin(stv[1]);
    float sm = (__uint_as_float(stv[0][0]) + __uint_as_float(stv[0][2])) + (__uint_as_float(stv[1][0]) + __uint_as_float(stv[1][2]));
    float sq = (__uint_as_float(stv[0][1]) + __uint_as_float(stv[0][3])) + (__uint_as_float(stv[1][1]) + __uint_as_float(stv[1][3]));
    sm += __shfl_xor(sm, 16); sq += __shfl_xor(sq, 16);
    sm += __shfl_xor(sm, 32); sq += __shfl_xor(sq, 32);
    const float mean = sm * (1.f / D_);
    const float rstd = ln_rstd(sq * (1.f / D_) - mean * mean);
    if (lane < 16) {
      ctab[w * 16 + lane] = -1.4426950408889634f * rstd;
      if (fgp == 0) ((float*)(ws_out + WS_R2))[rt * 128 + w * 16 + lane] = rstd * rstd;
    }
  }
  wait_lgkm0();
  bar();
  float c1[8];
#pragma unroll
  for (int tk = 0; tk < 8; ++tk) c1[tk] = ctab[tk * 16 + l15];
  tlmark(24); ckmark(0);
  unsigned lb[3] = {ldsb + voff, ldsb + voff + 49152, ldsb + voff + 98304};
  unsigned lh[3] = {lb[0] + (w & 1) * 4096, lb[1] + (w & 1) * 4096, lb[2] + (w & 1) * 4096};
  asm volatile("" : "+v"(lb[0]), "+v"(lb[1]), "+v"(lb[2]), "+v"(lh[0]), "+v"(lh[1]), "+v"(lh[2]));
  f32x4 acc[2][8];
  auto swiglu = [&](const f32x4& a, const f32x4& u, float c) -> u32x2 {
    float r[4];
#pragma unroll
    for (int j = 0; j < 4; ++j) { float e = __builtin_amdgcn_exp2f(a[j] * c); float pr = a[j] * u[j]; pin(pr); r[j] = pr * __builtin_amdgcn_rcpf(1.f + e); pin(r[j]); }
    return (u32x2){pk2(r[0], r[1]), pk2(r[2], r[3])};
  };
  char* actp = ws_out + WS_ACT + ((size_t)rt * 4 * 288 + fgp * 18) * 1024 + ((g & 1) * 32 + (g >> 1) * 16 + l15) * 16;
  auto epi2 = [&](int T, int tile32, const f32x4& g0, const f32x4& u0, float ca, const f32x4& g1, const f32x4& u1, float cb) {
    const u32x2 x0 = swiglu(g0, u0, ca), x1 = swiglu(g1, u1, cb);
    auto r0 = __builtin_amdgcn_permlane32_swap(x0[0], x1[0], false, false);
    auto r1 = __builtin_amdgcn_permlane32_swap(x0[1], x1[1], false, false);
    store16<2>(actp + ((size_t)tile32 * 288 + T) * 1024, (u32x4){r0[0], r1[0], r0[1], r1[1]});
  };
  auto epi_full = [&](int T) {
#pragma unroll
    for (int i = 0; i < 4; ++i) epi2(T, i, acc[0][2 * i], acc[1][2 * i], c1[2 * i], acc[0][2 * i + 1], acc[1][2 * i + 1], c1[2 * i + 1]);
  };
  k3_unit<8, true, true, 0, 0>(acc, wq, sA, HALF == 0 ? sC : sB, lb, rot, voff, hload);
  tlmark(25); ckmark(1);
  epi_full(t0);
  ckmark(2);
  if constexpr (HALF == 0) {
    k3_unit<4, false, true, 9 % K3_Q, 4>(acc, wq, sC, sB, lh, rot, voff, nohl);
    const bool hi = w & 1;
#pragma unroll
    for (int i = 0; i < 2; ++i) {
      float a0 = c1[2 * i], a1 = c1[4 + 2 * i], b0 = c1[2 * i + 1], b1 = c1[5 + 2 * i]; pin(a0); pin(a1); pin(b0); pin(b1);
      const float ca = hi ? a1 : a0, cb = hi ? b1 : b0;
      epi2(t2, (w & 1) * 2 + i, acc[0][2 * i], acc[1][2 * i], ca, acc[0][2 * i + 1], acc[1][2 * i + 1], cb);
    }
  }
  k3_unit<8, false, false, (HALF == 0 ? 18 : 9) % K3_Q, HALF == 0 ? 2 : 4>(acc, wq, sB, sB, lb, rot, voff, nohl);
  tlmark(29); ckmark(3);
  epi_full(t1);
  ckmark(4);
}
__global__ __launch_bounds__(512, 2) void k3_gateup(const char* __restrict__ ws_in, char* __restrict__ ws_out) {
  extern __shared__ __attribute__((aligned(16))) char lds[];
  const int w = uni(threadIdx.x >> 6);
  if (w < 4) k3_body<0>(ws_in, ws_out, lds, w); else k3_body<1>(ws_in, ws_out, lds, w);
}

#define K4_LA 4
#define K4_LW 4
constexpr int K4_R = 8, K4_D = 4;
constexpr int K4_LDS = K4_R * 16384;
constexpr int K4_NST = 21;
struct K4Sched {
  int aslot[18], sslot[K4_NST], posA[18], posS[K4_NST], cntBar[K4_NST], cntSlot[162];
  constexpr K4Sched() : aslot{}, sslot{}, posA{}, posS{}, cntBar{}, cntSlot{} {
    for (int a = 0; a < 18; ++a) aslot[a] = a < K4_LA ? -1 : 9 * (a - K4_LA) + 4;
    for (int s = 0; s < K4_NST; ++s) sslot[s] = s < K4_LW ? -1 : s - K4_LW;
    int c = 0;
    for (int s = 0; s < (K4_LW > K4_LA ? K4_LW : K4_LA); ++s) {
      if (s < K4_LW) { c += 2; posS[s] = c; }
      if (s < K4_LA) { c += 2; posA[s] = c; }
    }
    for (int n = -K4_D; n < 162; ++n) {
      if ((n + K4_D) % 8 == 0 && (n + K4_D) < 162) {
        int i = (n + K4_D) / 8; cntBar[i] = c;
        for (int s = 0; s < K4_NST; ++s) if (sslot[s] == i) { c += 2; posS[s] = c; }
      }
      if (n >= 0) {
        for (int q = 0; q < 18; ++q) if (aslot[q] == n) { c += 2; posA[q] = c; }
        cntSlot[n] = c;
      }
    }
  }
};
constexpr K4Sched k4s{};
DI u32x4 load16_sc1(const void* p) { u32x4 v; asm volatile("global_load_dwordx4 %0, %1, off sc1" : "=v"(v) : "v"(p) : "memory"); return v; }
DI u32x2 load8_sc1(const void* p) { u32x2 v; asm volatile("global_load_dwordx2 %0, %1, off sc1" : "=v"(v) : "v"(p) : "memory"); return v; }
DI void store8_sc1(void* p, u32x2 v) { asm volatile("global_store_dwordx2 %0, %1, off sc1\n\ts_nop 1" :: "v"(p), "v"(v) : "memory"); }

__global__ __launch_bounds__(512, 2) void k4_down(const char* __restrict__ ws_in, char* __restrict__ ws_out, u16* __restrict__ out) {
  TLSCOPE(4);
  extern __shared__ __attribute__((aligned(16))) char lds[];
  const int tid = threadIdx.x, lane = tid & 63, w = uni(tid >> 6);
  const int b = blockIdx.x, xcd = b & 7, idx = b >> 3;
  const int ksl = xcd >> 1, mt = (xcd & 1) * 8 + (idx & 7), ng = idx >> 3;
  const int tg = w & 3, kh = w >> 2;
  const int tt = mt * 4 + tg;
  const unsigned ldsb = (unsigned)(size_t)(LAS char*)lds;
  const char* wsrc = ws_in + WS_WD + (size_t)((ng * 4 + ksl) * 2 + kh) * WD_STREAM * 1024 + tg * 2048;
  const char* asrc = ws_in + WS_ACT + ((size_t)tt * 288 + ksl * 72 + kh * 36) * 1024;
  const unsigned voff = lane * 16;
  const int g = lane >> 4;
  const unsigned avoff = (g >> 1) * 1024 + (g & 1) * 512 + (lane & 15) * 16;
  u32x4 af[K4_LA + 1][2];
  auto issue_stage = [&](auto SI) {
    constexpr int s = decltype(SI)::value;
    glds16x2(wsrc + (size_t)s * 8192, voff, ldsb + (s % K4_R) * 16384 + kh * 8192 + tg * 2048);
  };
  auto issue_a = [&](auto AI) {
    constexpr int a = decltype(AI)::value;
    gload16<(a % 2) * 2048>(af[a % (K4_LA + 1)][0], asrc + (a / 2) * 4096, avoff);
    gload16<(a % 2) * 2048 + 256>(af[a % (K4_LA + 1)][1], asrc + (a / 2) * 4096, avoff);
  };
  sfor<(K4_LW > K4_LA ? K4_LW : K4_LA)>([&](auto SI) {
    constexpr int s = decltype(SI)::value;
    if constexpr (s < K4_LW) issue_stage(SI);
    if constexpr (s < K4_LA) issue_a(SI);
    if constexpr (s == 0) bar();
  });
  f32x4 acc[9][2];
  u32x4 wf[K4_D + 1];
  unsigned lrd0 = ldsb + kh * 8192 + lane * 16, lrd1 = lrd0 + 65536;
  asm volatile("" : "+v"(lrd0), "+v"(lrd1));
  auto rd = [&](auto NI) {
    constexpr int n = decltype(NI)::value;
    if constexpr (n < 162) {
      constexpr int st = n / 8;
      if constexpr (n % 8 == 0) {
        wait_vm<k4s.cntBar[st] - k4s.posS[st]>();
        bar();
        if constexpr (st == 0) { tlmark(26); ckmark(5); }
        sfor<K4_NST>([&](auto SI) { if constexpr (k4s.sslot[decltype(SI)::value] == st) issue_stage(SI); });
      }
      constexpr int off = (st % K4_R) * 16384 + (n % 8) * 1024;
      wf[n % (K4_D + 1)] = *(const LAS u32x4*)(size_t)((off < 65536 ? lrd0 : lrd1) + (off & 65535));
    }
  };
  sfor<K4_D>([&](auto I) { rd(I); });
  sfor<162>([&](auto NI) {
    constexpr int n = decltype(NI)::value;
    constexpr int ks = n / 9, t = n % 9, ab = ks % (K4_LA + 1);
    rd(std::integral_constant<int, n + K4_D>{});
    sfor<18>([&](auto AI) { if constexpr (k4s.aslot[decltype(AI)::value] == n) issue_a(AI); });
    if constexpr (t == 0) {
      wait_vm<k4s.cntSlot[n] - k4s.posA[ks]>();
      pin(af[ab][0]); pin(af[ab][1]);
    }
    acc[t][0] = mfma16(wf[n % (K4_D + 1)], af[ab][0], ks == 0 ? (f32x4){} : acc[t][0]);
    acc[t][1] = mfma16(wf[n % (K4_D + 1)], af[ab][1], ks == 0 ? (f32x4){} : acc[t][1]);
    sb0();
  });
  tlmark(27); ckmark(6);
  const int th = w & 1, a = w >> 1, ott = mt * 4 + ksl, tok = th * 16 + (lane & 15);
  const char* hfp = ws_in + WS_HF + (size_t)ott * 36 * 1024;
  auto hres = [&](int col) { const int k16 = col >> 4, r = (col >> 3) & 1, h = (col >> 2) & 1; return *(const u32x2*)(hfp + k16 * 1024 + (h * 32 + tok) * 16 + r * 8); };
  const int col = 32 * a + 8 * g, col8 = 128 + 4 * g;
  const u32x2 h0 = hres(ng * 144 + col), h1 = hres(ng * 144 + col + 4), h2 = hres(ng * 144 + col8);
  const float r2 = ((const float*)(ws_in + WS_R2))[ott * 32 + tok], mu = ((const float*)(ws_in + WS_MEAN))[ott * 32 + tok];
  wait_lgkm0(); bar();
  f32x4* xb = (f32x4*)lds;
#pragma unroll
  for (int t = 0; t < 9; ++t) xb[(t * 8 + w) * 64 + lane] = kh ? acc[t][0] : acc[t][1];
  wait_lgkm0(); bar();
  f32x4 v[9];
#pragma unroll
  for (int t = 0; t < 9; ++t) v[t] = xb[(t * 8 + (w ^ 4)) * 64 + lane] + (kh ? acc[t][1] : acc[t][0]);
  if (tg == ksl) {
#pragma unroll
    for (int t = 0; t < 9; ++t) xb[(t * 8 + (w ^ 4)) * 64 + lane] = v[t];
  } else {
    char* pp = ws_out + WS_PART + ((((size_t)ksl * 64 + tt) * 4 + ng) * 2 + kh) * 5120;
#pragma unroll
    for (int i = 0; i < 4; ++i)
      store16<2>(pp + i * 1024 + lane * 16, (u32x4){pk2(v[2 * i][0], v[2 * i][1]), pk2(v[2 * i][2], v[2 * i][3]), pk2(v[2 * i + 1][0], v[2 * i + 1][1]), pk2(v[2 * i + 1][2], v[2 * i + 1][3])});
    store8_sc1(pp + 4096 + lane * 8, (u32x2){pk2(v[8][0], v[8][1]), pk2(v[8][2], v[8][3])});
  }
  asm volatile("s_waitcnt vmcnt(0) lgkmcnt(0)" ::: "memory");
  bar();
  unsigned* cnt = (unsigned*)(ws_out + WS_CNT) + (mt * 4 + ng) * 32;
  if (tid == 0) {
    __hip_atomic_fetch_add(cnt, 1u, __ATOMIC_RELAXED, __HIP_MEMORY_SCOPE_AGENT);
    unsigned spins = 0;
    while (__hip_atomic_load(cnt, __ATOMIC_RELAXED, __HIP_MEMORY_SCOPE_AGENT) < 4u) {
      __builtin_amdgcn_s_sleep(1);
      if (++spins > (1u << 22)) break;
    }
  }
  bar();
  tlmark(28);
  const f32x4* ox = xb + ((th ^ 1) * 4 + ksl) * 64 + lane;
  f32x4 s0 = ox[(2 * a) * 512], s1 = ox[(2 * a + 1) * 512], s2 = ox[8 * 512];
  const char* pb = ws_in + WS_PART + (((size_t)ott * 4 + ng) * 2 + th) * 5120;
  constexpr size_t KS = (size_t)64 * 4 * 2 * 5120;
  u32x4 pv[3]; u32x2 pw[3];
#pragma unroll
  for (int k = 0; k < 3; ++k) {
    const char* q = pb + (size_t)((ksl + 1 + k) & 3) * KS;
    pv[k] = load16_sc1(q + a * 1024 + lane * 16); pw[k] = load8_sc1(q + 4096 + lane * 8);
  }
  wait_vm<0>();
#pragma unroll
  for (int k = 0; k < 3; ++k) {
    pin(pv[k]); pin(pw[k]);
    const unsigned a0 = pv[k][0], a1 = pv[k][1], a2 = pv[k][2], a3 = pv[k][3], b0 = pw[k][0], b1 = pw[k][1];
    s0 += (f32x4){bflo(a0), bfhi(a0), bflo(a1), bfhi(a1)}; s1 += (f32x4){bflo(a2), bfhi(a2), bflo(a3), bfhi(a3)}; s2 += (f32x4){bflo(b0), bfhi(b0), bflo(b1), bfhi(b1)};
  }
  u16* orow = out + (size_t)(ott * 32 + tok) * D_ + ng * 144;
  s0 = s0 * r2 + mu; s1 = s1 * r2 + mu; s2 = s2 * r2 + mu;
  u32x4 o;
  o[0] = pk2(s0[0] + bflo(h0[0]), s0[1] + bfhi(h0[0])); o[1] = pk2(s0[2] + bflo(h0[1]), s0[3] + bfhi(h0[1]));
  o[2] = pk2(s1[0] + bflo(h1[0]), s1[1] + bfhi(h1[0])); o[3] = pk2(s1[2] + bflo(h1[1]), s1[3] + bfhi(h1[1]));
  *(u32x4*)(orow + col) = o;
  if (a == 0) *(u32x2*)(orow + col8) = (u32x2){pk2(s2[0] + bflo(h2[0]), s2[1] + bfhi(h2[0])), pk2(s2[2] + bflo(h2[1]), s2[3] + bfhi(h2[1]))};
}
}
#define PACK_ST 2
namespace fk {
constexpr size_t WS_PQ   = (WS_END_FFN + 4095) / 4096 * 4096;
constexpr size_t WS_KVP  = WS_PQ + SZ_HF;
constexpr size_t SZ_KVP  = (size_t)16 * 8 * 16 * 1024;
constexpr size_t WS_KVF  = WS_KVP + SZ_KVP;
constexpr size_t SZ_KVF  = 64 * 1024;
constexpr size_t WS_WOP  = WS_KVF + SZ_KVF;
constexpr size_t SZ_WOP  = (size_t)4 * 4 * 9 * 5 * 1024;
constexpr size_t WS_MX   = WS_WOP + SZ_WOP;
constexpr size_t WS_END  = WS_MX + S_ * 4;

DI float phi_f(float t) { return (t > 0.f ? t + 1.f : __builtin_amdgcn_exp2f(t * 1.4426950408889634f)) + 1e-6f; }

DI void pack_wo_task(const u16* wo, char* ws, int id) {
  const int lane = id & 63, fi = id >> 6, i = fi % 5, u = (fi / 5) % 9, w = (fi / 45) & 3, ng = fi / 180;
  const int row = lane & 31, h = lane >> 5;
  if (i == 4 && row >= 16) {
    if (row >= 18) *(u32x4*)(ws + WS_WOP + (size_t)id * 16) = (u32x4){0u, 0u, 0u, 0u};
    return;
  }
  const int n = ng * 144 + i * 32 + row;
  unsigned v[8];
#pragma unroll
  for (int j = 0; j < 8; ++j) {
    const int e = u < 8 ? 18 * (8 * w + u) + 8 * (j >> 2) + 4 * h + (j & 3) : 18 * (8 * w + 4 * h + (j >> 1)) + 16 + (j & 1);
    v[j] = wo[e * D_ + n];
  }
  *(u32x4*)(ws + WS_WOP + (size_t)id * 16) = (u32x4){v[0] | (v[1] << 16), v[2] | (v[3] << 16), v[4] | (v[5] << 16), v[6] | (v[7] << 16)};
}
DI void pack_worow_task(const u16* wo, char* ws, int e, int lane) {
  float s = 0.f;
#pragma unroll
  for (int i = 0; i < 9; ++i) s += bf2f(wo[e * D_ + i * 64 + lane]);
#pragma unroll
  for (int o = 32; o; o >>= 1) s += __shfl_xor(s, o);
  const unsigned hi = pk2(s, 0.f) & 0xffffu; const unsigned lo = pk2(s - bflo(hi), 0.f) & 0xffffu;
  const int hh = e / 18, el = e % 18, w = hh >> 3, ul = hh & 7;
  int u, h, j;
  if (el < 16) { u = ul; h = (el >> 2) & 1; j = (el >> 3) * 4 + (el & 3); } else { u = 8; h = ul >> 2; j = (ul & 3) * 2 + (el - 16); }
  if (lane < 8) {
    const int ng = lane >> 1, part = lane & 1;
    u16* p = (u16*)(ws + WS_WOP) + ((((size_t)(ng * 4 + w) * 9 + u) * 5 + 4) * 64 + h * 32 + 16 + part) * 8 + j;
    *p = (u16)(part ? lo : hi);
  }
}

constexpr int K1_WST = 18432, K1_STG = K1_WST + 16384, K1_R = 4;
constexpr int K1_TAB = K1_STG * K1_R;
constexpr int K1_LDS = 8 * 18432;
static_assert(K1_TAB + 4 * 144 * 4 + 1024 <= K1_LDS, "lds");
#define K1_NGU 64
#define K1_NOPACK 0
#define K1_DD 3
constexpr int K1_D = K1_DD;
DI float phi3(float u, float rs, float rl) { return fmaxf(fmaf(u, rs, 1e-6f), 1e-6f) + __builtin_amdgcn_exp2f(fminf(u * rl, 0.f)); }
template <bool ISQ, int NH> constexpr int k1_tile(int j) {
  if (ISQ) return NH ? 5 + j : j;
  if (NH == 0) return j < 2 ? j : (j == 2 ? 4 : j + 2);
  return j < 2 ? 2 + j : 5 + j;
}
template <bool ISQ> constexpr int k1_toff(int t) { return ISQ ? 32 * t : (t < 4 ? 32 * t : 144 + 32 * (t - 5)); }

template <bool ISQ, int NH, class ISSUE>
DI void k1_body(char* lds, unsigned ldsb, int lane, int tg, int rt, int grp, char* ws, ISSUE&& issue) {
  constexpr int NT = NH ? 4 : 5, NWF = NT + K1_D;
  const int g = lane >> 4, l15 = lane & 15, q4 = (lane >> 2) & 3, p4 = lane & 3;
  const unsigned rrow = ldsb + (16 * (g >> 1) + 4 * (g & 1) + q4) * 288;
  unsigned rbA[2], rbB[2], rbX[2][2];
  rbA[0] = rrow + p4 * 8;
  rbB[0] = rrow + (p4 < 2 ? 128 + 8 * p4 : 272 + 8 * (p4 - 2));
  rbA[1] = rbA[0] + 2 * K1_STG; rbB[1] = rbB[0] + 2 * K1_STG;
  const unsigned xb = ldsb + K1_WST + tg * 4096 + l15 * 128;
#pragma unroll
  for (int kh = 0; kh < 2; ++kh) { rbX[kh][0] = xb + (((4 * kh + g) ^ ((l15 >> 1) & 7)) * 16); rbX[kh][1] = rbX[kh][0] + 2 * K1_STG; }
  asm volatile("" : "+v"(rbA[0]), "+v"(rbA[1]), "+v"(rbB[0]), "+v"(rbB[1]), "+v"(rbX[0][0]), "+v"(rbX[0][1]), "+v"(rbX[1][0]), "+v"(rbX[1][1]));
  u32x4 ones = {0x3f803f80u, 0x3f803f80u, 0x3f803f80u, 0x3f803f80u};
  pin(ones);
  f32x4 acc[NT][2], accs[NT], sst[2][2];
#pragma unroll
  for (int t = 0; t < NT; ++t) accs[t] = (f32x4){};
  u32x4 xf[2][2], wf[NWF];
  auto rd = [&](auto NI) {
    constexpr int n = decltype(NI)::value;
    if constexpr (n < 18 * NT) {
      constexpr int ks = n / NT, j = n % NT, st = ks >> 1, kh = ks & 1, slot = st % K1_R, hb = slot >> 1, so = (slot & 1) * K1_STG;
      constexpr int t = k1_tile<ISQ, NH>(j);
      constexpr int pos = n % (2 * NT);
      constexpr int nxt = st == 0 ? 3 : (st >= 2 && st + 2 < 9 ? st + 2 : -1);
      if constexpr (pos == 0) {
        constexpr int upto = st == 0 ? 2 : (st + 1 > 3 ? (st + 1 < 8 ? st + 1 : 8) : 3);
        wait_vm<4 * (upto - st)>();
        bar();
        if constexpr (st == 0) tlmark(20);
        if constexpr (st == 4) tlmark(21);
      }
      if constexpr (j == 0) {
        xf[kh][0] = *(const LAS u32x4*)(size_t)(rbX[kh][hb] + so);
        xf[kh][1] = *(const LAS u32x4*)(size_t)(rbX[kh][hb] + so + 2048);
      }
      const unsigned a = ((!ISQ && t == 4) ? rbB[hb] : rbA[hb] + k1_toff<ISQ>(t)) + so + kh * 9216;
      u32x2 lo = __builtin_bit_cast(u32x2, tr_read(a)), hi = __builtin_bit_cast(u32x2, tr_read(a + 8 * 288));
      wf[n % NWF] = (u32x4){lo[0], lo[1], hi[0], hi[1]};
      if constexpr (nxt >= 0 && (pos & 1) == 1 && pos / 2 < 4) issue(std::integral_constant<int, nxt>{}, std::integral_constant<int, pos / 2>{});
    }
  };
  sfor<K1_D>([&](auto I) { rd(I); });
  sfor<18 * NT>([&](auto NI) {
    constexpr int n = decltype(NI)::value, ks = n / NT, j = n % NT, kh = ks & 1;
    rd(std::integral_constant<int, n + K1_D>{});
    const u32x4 wv = wf[n % NWF];
    sfor<2>([&](auto HI) {
      constexpr int th = decltype(HI)::value;
      const f32x4 c = ks == 0 ? (f32x4){} : acc[j][th];
      acc[j][th] = ISQ ? mfma16(wv, xf[kh][th], c) : mfma16(xf[kh][th], wv, c);
    });
    if constexpr (NH == 1 && j < 2) {
      sst[j][0] = mfma16(ones, xf[kh][j], ks == 0 ? (f32x4){} : sst[j][0]);
      sst[j][1] = mfma16(xf[kh][j], xf[kh][j], ks == 0 ? (f32x4){} : sst[j][1]);
    }
    if constexpr (j == NT - 1) {
      if (tg == (ks & 3)) {
        sfor<NT>([&](auto JI) { constexpr int jj = decltype(JI)::value; accs[jj] = mfma16(ones, wf[(ks * NT + jj) % NWF], accs[jj]); });
      }
    }
    sb0();
  });
  tlmark(16);
  float* tab = (float*)(lds + K1_TAB);
  float* stt = tab + 4 * 144;
  if (g == 0) {
    sfor<NT>([&](auto JI) { constexpr int jj = decltype(JI)::value; tab[tg * 144 + k1_tile<ISQ, NH>(jj) * 16 + l15] = accs[jj][0]; });
  }
  if constexpr (NH == 1) {
    if (g == (l15 >> 2)) {
#pragma unroll
      for (int th = 0; th < 2; ++th) {
        const int r = l15 & 3;
        const float q = r == 0 ? sst[th][1][0] : (r == 1 ? sst[th][1][1] : (r == 2 ? sst[th][1][2] : sst[th][1][3]));
        const float mean = sst[th][0][0] * (1.f / D_);
        stt[tg * 32 + th * 16 + l15] = mean;
        stt[128 + tg * 32 + th * 16 + l15] = ln_rstd(q * (1.f / D_) - mean * mean);
      }
    }
  }
  wait_lgkm0(); bar();
  tlmark(22);
  constexpr float L2E = 1.4426950408889634f;
  if constexpr (ISQ) {
    const int tt = rt * 4 + tg;
    float nm[2], rs[2], rl[2];
#pragma unroll
    for (int th = 0; th < 2; ++th) { nm[th] = -stt[tg * 32 + th * 16 + l15]; rs[th] = stt[128 + tg * 32 + th * 16 + l15]; rl[th] = rs[th] * L2E; }
    if (NH == 0 && grp == 0 && g < 2) ((float*)(ws + WS_MX))[tt * 32 + g * 16 + l15] = g ? -nm[1] : -nm[0];
    char* pq = ws + WS_PQ + ((size_t)tt * 36 + grp * 9) * 1024 + ((g & 1) * 32 + (g >> 1) * 16 + l15) * 16;
    sfor<NT>([&](auto JI) {
      constexpr int j = decltype(JI)::value, t = k1_tile<ISQ, NH>(j);
      const float* cp = tab + t * 16 + g * 4;
      const f32x4 cs = (*(const f32x4*)cp + *(const f32x4*)(cp + 144)) + (*(const f32x4*)(cp + 288) + *(const f32x4*)(cp + 432));
      unsigned pk[2][2];
#pragma unroll
      for (int th = 0; th < 2; ++th) {
        float v[4];
#pragma unroll
        for (int e = 0; e < 4; ++e) v[e] = phi3(fmaf(nm[th], cs[e], acc[j][th][e]), rs[th], rl[th]);
        pk[th][0] = pk2(v[0], v[1]); pk[th][1] = pk2(v[2], v[3]);
      }
      auto r0 = __builtin_amdgcn_permlane32_swap(pk[0][0], pk[1][0], false, false);
      auto r1 = __builtin_amdgcn_permlane32_swap(pk[0][1], pk[1][1], false, false);
      *(u32x4*)(pq + t * 1024) = (u32x4){r0[0], r1[0], r0[1], r1[1]};
    });
  } else {
    f32x4 mn[2], rs[2], rl[2];
#pragma unroll
    for (int th = 0; th < 2; ++th) { mn[th] = *(const f32x4*)(stt + tg * 32 + th * 16 + g * 4); rs[th] = *(const f32x4*)(stt + 128 + tg * 32 + th * 16 + g * 4); rl[th] = rs[th] * L2E; }
    char* ob = lds + (tg * 64 + lane) * 16;
    sfor<NT>([&](auto JI) {
      constexpr int j = decltype(JI)::value, t = k1_tile<ISQ, NH>(j);
      const float* cp = tab + t * 16 + l15;
      const float ncs = -((cp[0] + cp[144]) + (cp[288] + cp[432]));
      u32x4 pv;
#pragma unroll
      for (int th = 0; th < 2; ++th) {
        float v[4];
#pragma unroll
        for (int e = 0; e < 4; ++e) {
          const float u = fmaf(mn[th][e], ncs, acc[j][th][e]);
          if (t < 4) v[e] = phi3(u, rs[th][e], rl[th][e]);
          else if (t > 4) v[e] = u * rs[th][e];
          else v[e] = l15 < 8 ? phi3(u, rs[th][e], rl[th][e]) : u * rs[th][e];
        }
        pv[2 * th] = pk2(v[0], v[1]); pv[2 * th + 1] = pk2(v[2], v[3]);
      }
      *(u32x4*)(ob + t * 4096) = pv;
      if constexpr (t == 4) *(u32x4*)(ob + 9 * 4096) = l15 < 8 ? ones : pv;
    });
    wait_lgkm0(); bar();
    tlmark(23);
    const int w = NH * 4 + tg;
#pragma unroll
    for (int pi = 0; pi < 2; ++pi) {
      const int p = 2 * w + pi;
      int a, bt;
      if (p < 10) { a = (p + 1) / 3; bt = 5 + a + ((p + 1) % 3) - 1; } else if (p == 10) { a = 4; bt = 8; } else { a = p - 11; bt = 9; }
      f32x4 c = {};
#pragma unroll
      for (int i = 0; i < 4; ++i) {
        const u32x4 av = *(const u32x4*)(lds + a * 4096 + (i * 64 + lane) * 16);
        const u32x4 bv = *(const u32x4*)(lds + bt * 4096 + (i * 64 + lane) * 16);
        c = mfma16(av, bv, c);
      }
      *(f32x4*)(ws + WS_KVP + (((size_t)rt * 8 + grp) * 16 + p) * 1024 + lane * 16) = c;
    }
  }
}

__global__ __launch_bounds__(512, 2) void k1_qkv(const u16* __restrict__ x, const u16* __restrict__ wqkv, const u16* __restrict__ wo,
                                                  const u16* __restrict__ wg, const u16* __restrict__ wu, const u16* __restrict__ wd, char* __restrict__ ws) {
  TLSCOPE(0);
  extern __shared__ __attribute__((aligned(16))) char lds[];
  const int tid = threadIdx.x, lane = tid & 63, w = uni(tid >> 6);
  const int b = blockIdx.x, xcd = b & 7, idx = b >> 3;
  const unsigned ldsb = (unsigned)(size_t)(LAS char*)lds;
  if (idx >= 24) {
    if (K1_NOPACK) return;
    const int pw = ((idx - 24) * 8 + xcd) * 8 + w;
    if (pw == 0) ((unsigned*)(ws + WS_CNT))[lane * 32] = 0u;
    const int u = (idx - 24) * 8 + w;
    for (int id = pw * 64 + lane; id < 4 * 4 * 9 * 5 * 64; id += 512 * 64) pack_wo_task(wo, ws, id);
    for (int e = pw; e < D_; e += 512) pack_worow_task(wo, ws, e, lane);
    tlmark(18);
    if (u < K1_NGU) pack_gu_unit<PACK_ST>(wg, wu, ws, xcd * 9 + u % 9, u / 9, ldsb + w * 16384, lane);
    tlmark(19);
    return;
  }
  const int rt = (xcd & 3) * 4 + (idx & 3), cgl = idx >> 2;
  const int ch = xcd >> 2;
  const bool isq = cgl < 2;
  const int grp = isq ? ch * 2 + cgl : ch * 4 + (cgl - 2);
  auto wsrc_off = [&](int n) -> unsigned {
    const int c = n * 64 + lane, rho = c / 18, cc = c % 18;
    const int k = (rho & ~12) | ((rho & 4) << 1) | ((rho & 8) >> 1);
    const int col = isq ? 144 * grp + 8 * cc : (cc < 9 ? 576 + 72 * grp + 8 * cc : 1152 + 72 * grp + 8 * (cc - 9));
    return (unsigned)((k * 1728 + col) * 2);
  };
  const char* xsrc = (const char*)x + (size_t)(rt * 128 + w * 16) * 1152;
  const unsigned xoff0 = (lane >> 3) * 1152 + (((lane & 7) ^ (lane >> 4)) * 16);
  const unsigned xoff1 = ((lane >> 3) + 8) * 1152 + (((lane & 7) ^ (4 + (lane >> 4))) * 16);
  glds16(xsrc, xoff0, ldsb + K1_WST + w * 2048);
  glds16(xsrc, xoff1, ldsb + K1_WST + w * 2048 + 1024);
  const unsigned woffA = wsrc_off(w), woffB = wsrc_off(w + 8), woffC = wsrc_off(16 + (w & 1));
  auto issue = [&](auto SI, auto PI) {
    constexpr int st = decltype(SI)::value, slot = st % K1_R, pc = decltype(PI)::value;
    const char* wb = (const char*)wqkv + (size_t)st * 64 * 3456;
    const unsigned sb = ldsb + slot * K1_STG;
    if constexpr (pc == 0) glds16(xsrc + st * 128, xoff0, sb + K1_WST + w * 2048);
    if constexpr (pc == 1) glds16(xsrc + st * 128, xoff1, sb + K1_WST + w * 2048 + 1024);
    if constexpr (pc == 2) { if (w < 2) glds16(wb, woffC, sb + (16 + w) * 1024); glds16(wb, woffA, sb + w * 1024); }
    if constexpr (pc == 3) glds16(wb, woffB, sb + (w + 8) * 1024);
  };
  issue(std::integral_constant<int, 0>{}, std::integral_constant<int, 2>{});
  issue(std::integral_constant<int, 0>{}, std::integral_constant<int, 3>{});
  bar();
  sfor<4>([&](auto PI) { issue(std::integral_constant<int, 1>{}, PI); });
  sfor<4>([&](auto PI) { issue(std::integral_constant<int, 2>{}, PI); });
  const int tg = w & 3;
  if (isq) { if (w < 4) k1_body<true, 0>(lds, ldsb, lane, tg, rt, grp, ws, issue); else k1_body<true, 1>(lds, ldsb, lane, tg, rt, grp, ws, issue); }
  else     { if (w < 4) k1_body<false, 0>(lds, ldsb, lane, tg, rt, grp, ws, issue); else k1_body<false, 1>(lds, ldsb, lane, tg, rt, grp, ws, issue); }
}

constexpr int K1B_LDS = 4 * 18432;
__global__ __launch_bounds__(256) void k1b_reduce(const u16* __restrict__ wg, const u16* __restrict__ wu, const u16* __restrict__ wd, char* __restrict__ ws) {
  TLSCOPE(1);
  extern __shared__ __attribute__((aligned(16))) char lds[];
  const int tid = threadIdx.x;
  const int b = blockIdx.x;
  if (b >= 32) {
    const int lane = tid & 63, w = uni(tid >> 6), xcd = b & 7, u = w * 28 + ((b - 32) >> 3);
    const unsigned ldsb = (unsigned)(size_t)(LAS char*)lds + w * 18432;
    if (u < 81 - K1_NGU) pack_gu_unit<PACK_ST>(wg, wu, ws, xcd * 9 + (K1_NGU + u) % 9, (K1_NGU + u) / 9, ldsb, lane);
    else if (u - (81 - K1_NGU) < 36) pack_wd_unit<PACK_ST>(wd, ws, xcd * 18 + ((u - (81 - K1_NGU)) >> 1), (u - (81 - K1_NGU)) & 1, ldsb, lane);
    return;
  }
  const int hh = b, s = tid >> 7, l = (tid >> 1) & 63, r = tid & 1;
  const int row = l & 31, h = l >> 5;
  const int T = (18 * hh) / 16 + s;
  const int d0 = 16 * T + 8 * r + 4 * h;
  const int G = hh >> 2, hl = hh & 3;
  int el = -1; bool isz = false;
  if (row < 16) el = row;
  else if (row == 18 || row == 22) isz = true;
  else if ((hh & 7) < 4 ? (row == 16 || row == 17) : (row == 20 || row == 21)) el = 16 + (row & 1);
  const int dlo = 18 * hh, dhi = dlo + 18;
  f32x4 sum = {};
  if ((el >= 0 || isz) && d0 + 3 >= dlo && d0 < dhi) {
    const int dk = d0 - 72 * G, a = dk >> 4, dr = dk & 15;
    int p, col;
    if (isz) { p = 11 + a; col = 0; }
    else {
      const int ev = 18 * hl + el;
      if (ev >= 64) { p = 11 + a; col = 8 + ev - 64; }
      else { const int vt = ev >> 4; col = ev & 15; p = a == 4 ? 10 : 3 * a + (vt - a); }
    }
    const char* src = ws + WS_KVP + ((size_t)G * 16 + p) * 1024 + ((dr >> 2) * 16 + col) * 16;
    f32x4 v[16];
#pragma unroll
    for (int i = 0; i < 16; ++i) v[i] = *(const f32x4*)(src + (size_t)i * 8 * 16 * 1024);
#pragma unroll
    for (int i = 0; i < 16; ++i) sum += v[i];
#pragma unroll
    for (int j = 0; j < 4; ++j) if (d0 + j < dlo || d0 + j >= dhi) sum[j] = 0.f;
  }
  *(u32x2*)(ws + WS_KVF + ((size_t)(hh * 2 + s) * 64 + l) * 16 + r * 8) = (u32x2){pk2(sum[0], sum[1]), pk2(sum[2], sum[3])};
}

#define K2_WIN 16
constexpr int K2_LDS = 4 * 19 * 1024;
__global__ __launch_bounds__(256, 1) void k2_attn_out(const u16* __restrict__ x, const char* __restrict__ ws_in, char* __restrict__ ws) {
  TLSCOPE(2);
  extern __shared__ __attribute__((aligned(16))) char lds[];
  const int tid = threadIdx.x, lane = tid & 63, w = uni(tid >> 6);
  const int b = blockIdx.x, xcd = b & 7, idx = b >> 3;
  const int tt = xcd * 8 + (idx & 7), ng = idx >> 3;
  const int tok = lane & 31, h = lane >> 5;
  const char* pqp = ws_in + WS_PQ + ((size_t)tt * 36 + w * 9) * 1024;
  const char* kvp = ws_in + WS_KVF + (size_t)w * 16 * 1024;
  const char* wop = ws_in + WS_WOP + (size_t)(ng * 4 + w) * 45 * 1024;
  const unsigned voff = lane * 16;
  u32x4 pq[9], kvf[16], wf[45];
  auto issue = [&](auto II) {
    constexpr int i = decltype(II)::value;
    if constexpr (i == 0) gload16<0>(pq[0], pqp, voff);
    else if constexpr (i < 70) {
      constexpr int u = (i - 1) / 8, k = (i - 1) % 8;
      if constexpr (u < 8 && k < 2) gload16<((2 * u + k) % 4) * 1024>(kvf[2 * u + k], kvp + ((2 * u + k) / 4) * 4096, voff);
      else if constexpr (u < 8 && k == 2) gload16<((u + 1) % 4) * 1024>(pq[u + 1], pqp + ((u + 1) / 4) * 4096, voff);
      else { constexpr int n = u < 8 ? 5 * u + k - 3 : 40 + k; gload16<(n % 4) * 1024>(wf[n], wop + (n / 4) * 4096, voff); }
    }
  };
  const int u0 = w == 0 ? 0 : 2 * w + 1;
  u32x4 xr[3]; unsigned mxb;
  sfor<4>([&](auto I) { issue(I); });
  {
    const char* xb0 = (const char*)x + (size_t)tt * 32 * 1152 + ng * 288 + u0 * 32;
    const unsigned xo = tok * 1152 + h * 16;
    gload16<0>(xr[0], xb0, xo); gload16<32>(xr[1], xb0, xo);
    gload16<0>(xr[2], xb0 + (w == 0 ? 64 : 0), xo);
    gload4<0>(mxb, ws_in + WS_MX + (size_t)tt * 128, tok * 4);
  }
  sfor<K2_WIN - 4>([&](auto I) { issue(std::integral_constant<int, 4 + decltype(I)::value>{}); });
  u32x4 ofr; unsigned lo[8];
  f32x16 acc[5], c;
  auto ostep = [&](auto UI) {
    constexpr int u = decltype(UI)::value;
    if constexpr (u == 0) pin(pq[0]);
    pin(kvf[2 * u]); pin(kvf[2 * u + 1]); pin(pq[u + 1]);
    c = mfma32(kvf[2 * u], pq[u], (f32x16){});
    c = mfma32(kvf[2 * u + 1], pq[u + 1], c);
  };
  wait_vm<K2_WIN - 4 + 4>();
  ostep(std::integral_constant<int, 0>{});
  sfor<9>([&](auto UI) {
    constexpr int u = decltype(UI)::value;
    if constexpr (u < 8) {
      const float r = __builtin_amdgcn_rcpf(c[10] + 1e-6f);
      ofr = (u32x4){pk2(c[0] * r, c[1] * r), pk2(c[2] * r, c[3] * r), pk2(c[4] * r, c[5] * r), pk2(c[6] * r, c[7] * r)};
      lo[u] = pk2(c[8] * r, c[9] * r);
    } else {
      sfor<4>([&](auto J) { constexpr int j = decltype(J)::value; unsigned a = lo[j], d = lo[4 + j]; pin(a); pin(d); ofr[j] = h ? d : a; });
    }
    sfor<8>([&](auto KI) { issue(std::integral_constant<int, K2_WIN + 8 * u + decltype(KI)::value>{}); });
    constexpr int issued = (K2_WIN + 8 * (u + 1)) < 70 ? (K2_WIN + 8 * (u + 1)) : 70;
    constexpr int need = u < 7 ? 8 * (u + 1) + 4 : (u == 7 ? 65 : 70);
    wait_vm<issued - need>();
    if constexpr (u < 7) ostep(std::integral_constant<int, u + 1>{});
    sfor<5>([&](auto NI) {
      constexpr int i = decltype(NI)::value, n = 5 * u + i;
      pin(wf[n]);
      acc[i] = mfma32(wf[n], ofr, u == 0 ? (f32x16){} : acc[i]);
    });
    sb0();
  });
#pragma unroll
  for (int i = 0; i < 3; ++i) pin(xr[i]);
  pin(mxb);
  const float mx = __uint_as_float(mxb);
  f32x4* xb = (f32x4*)lds;
#pragma unroll
  for (int q = 0; q < 19; ++q) {
    const int i = q >> 2, rb = (q & 3) * 4;
    xb[(w * 19 + q) * 64 + lane] = (f32x4){acc[i][rb], acc[i][rb + 1], acc[i][rb + 2], acc[i][rb + 3]};
  }
  tlmark(31);
#pragma unroll
  for (int i = 0; i < 3; ++i) {
    auto r0 = __builtin_amdgcn_permlane32_swap(xr[i][0], xr[i][2], false, false);
    auto r1 = __builtin_amdgcn_permlane32_swap(xr[i][1], xr[i][3], false, false);
    xr[i] = (u32x4){r0[0], r1[0], r0[1], r1[1]};
  }
  wait_lgkm0(); bar();
  float ms = 0.f;
#pragma unroll
  for (int k = 0; k < 4; ++k) { const f32x4 v = xb[(k * 19 + 18) * 64 + tok]; ms += v[0] + v[1]; }
  const float mean = mx + ms * (1.f / D_);
  if (ng == 0 && w == 0 && h == 0) ((float*)(ws + WS_MEAN))[tt * 32 + tok] = mean;
  float s1 = 0.f, s2 = 0.f;
#pragma unroll
  for (int i = 0; i < 3; ++i) {
    if (i < 2 || w == 0) {
      const int u = u0 + i;
      f32x4 a = {}, d = {};
#pragma unroll
      for (int k = 0; k < 4; ++k) { a += xb[(k * 19 + 2 * u) * 64 + lane]; d += xb[(k * 19 + 2 * u + 1) * 64 + lane]; }
      u32x4 o;
      o[0] = pk2(a[0] + bflo(xr[i][0]) - mean, a[1] + bfhi(xr[i][0]) - mean);
      o[1] = pk2(a[2] + bflo(xr[i][1]) - mean, a[3] + bfhi(xr[i][1]) - mean);
      o[2] = pk2(d[0] + bflo(xr[i][2]) - mean, d[1] + bfhi(xr[i][2]) - mean);
      o[3] = pk2(d[2] + bflo(xr[i][3]) - mean, d[3] + bfhi(xr[i][3]) - mean);
#pragma unroll
      for (int j = 0; j < 4; ++j) { const unsigned ov = o[j]; s1 = dot2_ones(ov, s1); s2 = __builtin_amdgcn_fdot2_f32_bf16(as_bf2(ov), as_bf2(ov), s2, false); }
      *(u32x4*)(ws + WS_HF + (((size_t)tt * 36 + ng * 9 + u) * 64 + lane) * 16) = o;
    }
  }
  s1 += __shfl_xor(s1, 32); s2 += __shfl_xor(s2, 32);
  if (h == 0) *(f32x2*)(ws + WS_HST + ((size_t)(tt * 32 + tok) * 16 + ng * 4 + w) * 8) = (f32x2){s1, s2};
}
}
extern "C" void kernel_launch(void* const* d_in, const int* in_sizes, int n_in, void* d_out, int out_size, void* d_ws, size_t ws_size, hipStream_t st) {
  using namespace fk;
  const u16* x = (const u16*)d_in[0]; const u16* wqkv = (const u16*)d_in[1]; const u16* wo = (const u16*)d_in[2];
  const u16* wg = (const u16*)d_in[4]; const u16* wu = (const u16*)d_in[5]; const u16* wd = (const u16*)d_in[6];
  char* ws = (char*)d_ws;
  (void)hipFuncSetAttribute((const void*)k1_qkv, hipFuncAttributeMaxDynamicSharedMemorySize, K1_LDS);
  (void)hipFuncSetAttribute((const void*)k1b_reduce, hipFuncAttributeMaxDynamicSharedMemorySize, K1B_LDS);
  (void)hipFuncSetAttribute((const void*)k2_attn_out, hipFuncAttributeMaxDynamicSharedMemorySize, K2_LDS);
  (void)hipFuncSetAttribute((const void*)k3_gateup, hipFuncAttributeMaxDynamicSharedMemorySize, K3_LDS);
  (void)hipFuncSetAttribute((const void*)k4_down, hipFuncAttributeMaxDynamicSharedMemorySize, K4_LDS);
  k1_qkv<<<256, 512, K1_LDS, st>>>(x, wqkv, wo, wg, wu, wd, ws);
  k1b_reduce<<<256, 256, K1B_LDS, st>>>(wg, wu, wd, ws);
  k2_attn_out<<<256, 256, K2_LDS, st>>>(x, ws, ws);
  k3_gateup<<<256, 512, K3_LDS, st>>>(ws, ws);
  k4_down<<<256, 512, K4_LDS, st>>>(ws, ws, (u16*)d_out);
}
```

```cpp
#include <hip/hip_runtime.h>
#include <hip/hip_bf16.h>
#include <stdint.h>
#include <type_traits>
#include <utility>

namespace fk {
typedef unsigned short u16;
typedef short bf16x8 __attribute__((ext_vector_type(8)));
typedef short s16x4 __attribute__((ext_vector_type(4)));
typedef float f32x4 __attribute__((ext_vector_type(4)));
typedef float f32x2 __attribute__((ext_vector_type(2)));
typedef float f32x16 __attribute__((ext_vector_type(16)));
typedef unsigned u32x4 __attribute__((ext_vector_type(4)));
typedef unsigned u32x2 __attribute__((ext_vector_type(2)));
typedef __bf16 bf16x2v __attribute__((ext_vector_type(2)));
#define LAS __attribute__((address_space(3)))
#define DI __device__ __forceinline__

constexpr int S_ = 2048, D_ = 576, H_ = 32, DH_ = 18, F_ = 4608;

template <int N, class Fn, int... I> DI void sfor_impl(Fn&& f, std::integer_sequence<int, I...>) { (f(std::integral_constant<int, I>{}), ...); }
template <int N, class Fn> DI void sfor(Fn&& f) { sfor_impl<N>(f, std::make_integer_sequence<int, N>{}); }

template <int N> DI void wait_vm() { asm volatile("s_waitcnt vmcnt(%0)" ::"i"(N) : "memory"); }
DI void wait_lgkm0() { asm volatile("s_waitcnt lgkmcnt(0)" ::: "memory"); }
DI void bar() { asm volatile("" ::: "memory"); __builtin_amdgcn_s_barrier(); asm volatile("" ::: "memory"); }
template <class T> DI void pin(T& v) { asm volatile("" : "+v"(v)); }

DI void glds16(const void* sbase, unsigned voff, unsigned ldsdst) {
  asm volatile("s_mov_b32 m0, %2\n\ts_nop 0\n\tglobal_load_lds_dwordx4 %0, %1" :: "v"(voff), "s"(sbase), "s"(ldsdst) : "memory");
}
DI void glds16x2(const void* sbase, unsigned voff, unsigned ldsdst) {
  asm volatile("s_mov_b32 m0, %2\n\ts_nop 0\n\tglobal_load_lds_dwordx4 %0, %1\n\tglobal_load_lds_dwordx4 %0, %1 offset:1024" :: "v"(voff), "s"(sbase), "s"(ldsdst) : "memory");
}
DI void glds16v(const void* gptr, unsigned ldsdst) {
  asm volatile("s_mov_b32 m0, %1\n\ts_nop 0\n\tglobal_load_lds_dwordx4 %0, off" :: "v"(gptr), "s"(ldsdst) : "memory");
}
DI void sb0() { __builtin_amdgcn_sched_barrier(0); }
template <int OFF> DI void gload16(u32x4& dst, const void* sbase, unsigned voff) {
  asm volatile("global_load_dwordx4 %0, %1, %2 offset:%3" : "=v"(dst) : "v"(voff), "s"(sbase), "i"(OFF) : "memory");
}
template <int OFF> DI void gload8(u32x2& dst, const void* sbase, unsigned voff) {
  asm volatile("global_load_dwordx2 %0, %1, %2 offset:%3" : "=v"(dst) : "v"(voff), "s"(sbase), "i"(OFF) : "memory");
}
template <int OFF> DI void gload4(unsigned& dst, const void* sbase, unsigned voff) {
  asm volatile("global_load_dword %0, %1, %2 offset:%3" : "=v"(dst) : "v"(voff), "s"(sbase), "i"(OFF) : "memory");
}
template <int OFF> DI void gload16a(u32x4& dst, const void* sbase, unsigned voff) {
  asm volatile("global_load_dwordx4 %0, %1, %2 offset:%3" : "=a"(dst) : "v"(voff), "s"(sbase), "i"(OFF) : "memory");
}
DI void l2touch(const void* sbase, unsigned voff, unsigned ldsdummy) {
  asm volatile("s_mov_b32 m0, %2\n\ts_nop 0\n\tglobal_load_lds_dword %0, %1" :: "v"(voff), "s"(sbase), "s"(ldsdummy) : "memory");
}
template <class T> DI void pina(T& v) { asm volatile("" : "+a"(v)); }
template <int MODE> DI void store16(void* p, u32x4 v) {
  if constexpr (MODE == 0) *(u32x4*)p = v;
  else if constexpr (MODE == 1) __builtin_nontemporal_store(v, (u32x4*)p);
  else if constexpr (MODE == 2) asm volatile("global_store_dwordx4 %0, %1, off sc1\n\ts_nop 1" :: "v"(p), "v"(v) : "memory");
  else if constexpr (MODE == 3) asm volatile("global_store_dwordx4 %0, %1, off sc0 sc1\n\ts_nop 1" :: "v"(p), "v"(v) : "memory");
  else if constexpr (MODE == 4) asm volatile("global_store_dwordx4 %0, %1, off sc1 nt\n\ts_nop 1" :: "v"(p), "v"(v) : "memory");
  else asm volatile("" :: "v"(v));
}
DI float bf2f(u16 v) { return __uint_as_float(((unsigned)v) << 16); }
DI float bflo(unsigned v) { return __uint_as_float(v << 16); }
DI float bfhi(unsigned v) { return __uint_as_float(v & 0xffff0000u); }
DI unsigned pk2(float lo, float hi) {
  bf16x2v r; r[0] = (__bf16)lo; r[1] = (__bf16)hi; return __builtin_bit_cast(unsigned, r);
}
DI float rbf(float v) { return bflo(pk2(v, 0.f)); }
DI float ln_rstd(float var) { return __builtin_amdgcn_rcpf(rbf(__builtin_amdgcn_sqrtf(rbf(rbf(fmaxf(var, 0.f)) + 1e-5f)))); }
DI s16x4 tr_read(unsigned lds_addr) {
  return __builtin_amdgcn_ds_read_tr16_b64_v4i16((LAS s16x4*)(size_t)lds_addr);
}
DI f32x16 mfma32(u32x4 a, u32x4 b, f32x16 c) { return __builtin_amdgcn_mfma_f32_32x32x16_bf16(__builtin_bit_cast(bf16x8, a), __builtin_bit_cast(bf16x8, b), c, 0, 0, 0); }
DI f32x4 mfma16(u32x4 a, u32x4 b, f32x4 c) { return __builtin_amdgcn_mfma_f32_16x16x32_bf16(__builtin_bit_cast(bf16x8, a), __builtin_bit_cast(bf16x8, b), c, 0, 0, 0); }
#define TLSCOPE(k)
DI void tlmark(int) {}
DI void ckmark(int) {}
DI int uni(int v) { return __builtin_amdgcn_readfirstlane(v); }
}
namespace fk {
DI bf16x2v as_bf2(unsigned v) { return __builtin_bit_cast(bf16x2v, v); }
DI float dot2_ones(unsigned v, float c) { return __builtin_amdgcn_fdot2_f32_bf16(as_bf2(v), as_bf2(0x3f803f80u), c, false); }

constexpr size_t WS_HF   = 0;
constexpr size_t SZ_HF   = (size_t)S_ * D_ * 2;
constexpr size_t WS_HST  = WS_HF + SZ_HF;
constexpr size_t SZ_HST  = (size_t)S_ * 16 * 8;
constexpr size_t WS_WGU  = WS_HST + SZ_HST;
constexpr size_t SZ_WGU  = (size_t)288 * 36 * 1024;
constexpr int    WD_STREAM = 168;
constexpr size_t WS_WD   = WS_WGU + SZ_WGU + 16384;
constexpr size_t SZ_WD   = (size_t)32 * WD_STREAM * 1024;
constexpr size_t WS_ACT  = WS_WD + SZ_WD;
constexpr size_t SZ_ACT  = (size_t)S_ * F_ * 2;
constexpr size_t WS_PART = WS_ACT + SZ_ACT;
constexpr size_t SZ_PART = (size_t)4 * S_ * D_ * 4;
constexpr size_t WS_R2   = WS_PART + SZ_PART;
constexpr size_t WS_MEAN = WS_R2 + S_ * 4;
constexpr size_t WS_CNT  = WS_MEAN + S_ * 4;
constexpr size_t WS_END_FFN = WS_CNT + 256 * 128;

DI void pack_gu_load(const u16* wg, const u16* wu, int lc, int kp, unsigned ldsb, int lane) {
  const unsigned voff = (unsigned)(((kp * 64 + (lane >> 3)) * F_ + lc * 64 + (lane & 7) * 8) * 2);
  sfor<16>([&](auto I) {
    constexpr int i = decltype(I)::value, s = i >> 3, mat = (i >> 2) & 1, r8 = s * 4 + (i & 3);
    glds16((const char*)(mat ? wu : wg) + (size_t)r8 * 8 * F_ * 2, voff, ldsb + mat * 8192 + r8 * 1024);
  });
}
template <int ST, bool SPLITWAIT> DI void pack_gu_store(char* ws, int lc, int kp, unsigned ldsb, int lane) {
  const int g = lane >> 4;
  const unsigned rbase = ldsb + (16 * (g >> 1) + 4 * (g & 1) + ((lane >> 2) & 3)) * 128 + (lane & 3) * 8;
  char* dst = ws + WS_WGU + ((size_t)(lc * 4) * 18 + kp * 2) * 2048 + lane * 16;
  sfor<2>([&](auto I) {
    constexpr int s = decltype(I)::value;
    if constexpr (SPLITWAIT) wait_vm<8>();
    u32x4 v[8];
    sfor<8>([&](auto J) {
      constexpr int t = decltype(J)::value >> 1, mat = decltype(J)::value & 1;
      u32x2 a = __builtin_bit_cast(u32x2, tr_read(rbase + s * 4096 + mat * 8192 + t * 32)), b = __builtin_bit_cast(u32x2, tr_read(rbase + s * 4096 + mat * 8192 + t * 32 + 1024));
      v[decltype(J)::value] = (u32x4){a[0], a[1], b[0], b[1]};
    });
    sfor<8>([&](auto J) {
      constexpr int t = decltype(J)::value >> 1, mat = decltype(J)::value & 1;
      store16<ST>(dst + (t * 18 + s) * 2048 + mat * 1024, v[decltype(J)::value]);
    });
  });
}
template <int ST> DI void pack_gu_unit(const u16* wg, const u16* wu, char* ws, int lc, int kp, unsigned ldsb, int lane) {
  pack_gu_load(wg, wu, lc, kp, ldsb, lane); pack_gu_store<ST, true>(ws, lc, kp, ldsb, lane);
}
DI void pack_wd_load(const u16* wd, int S, int c2, unsigned ldsb, int lane) {
  const char* base = (const char*)wd + ((size_t)S * 32 * D_ + c2 * 288) * 2;
  sfor<9>([&](auto I) {
    constexpr int n = decltype(I)::value;
    const int c = n * 64 + lane, row = c / 36, cc = c % 36;
    const unsigned vo = (unsigned)(row * D_ * 2 + cc * 16);
    glds16(base, vo, ldsb + n * 1024);
    glds16(base + 16 * D_ * 2, vo, ldsb + 9216 + n * 1024);
  });
}
template <int ST> DI void pack_wd_store(char* ws, int S, int c2, unsigned ldsb, int lane) {
  const int g = lane >> 4, q = (lane >> 2) & 3, p = lane & 3;
  const unsigned rb = ldsb + (16 * (g >> 1) + 4 * (g & 1) + q) * 576;
  const int ksl = S / 36, kh = (S % 36) / 18, ks = S % 18;
  char* dst = ws + WS_WD + ((size_t)((c2 * 2 * 4 + ksl) * 2 + kh) * WD_STREAM + ks * 9) * 1024 + lane * 16;
  u32x4 v[18];
  sfor<18>([&](auto I) {
    constexpr int ngl = decltype(I)::value / 9, t = decltype(I)::value % 9;
    const unsigned a0 = rb + ngl * 288 + (t < 8 ? 64 * (t >> 1) + 16 * p + 8 * (t & 1) : 256 + 8 * p);
    u32x2 a = __builtin_bit_cast(u32x2, tr_read(a0)), b = __builtin_bit_cast(u32x2, tr_read(a0 + 8 * 576));
    v[decltype(I)::value] = (u32x4){a[0], a[1], b[0], b[1]};
  });
  sfor<18>([&](auto I) {
    constexpr int ngl = decltype(I)::value / 9, t = decltype(I)::value % 9;
    store16<ST>(dst + (size_t)ngl * 8 * WD_STREAM * 1024 + t * 1024, v[decltype(I)::value]);
  });
}
template <int ST> DI void pack_wd_unit(const u16* wd, char* ws, int S, int c2, unsigned ldsb, int lane) {
  pack_wd_load(wd, S, c2, ldsb, lane); wait_vm<0>(); pack_wd_store<ST>(ws, S, c2, ldsb, lane);
}

constexpr int K3_LDS = 144 * 1024 + 512;
#define K3_P 4
constexpr int K3_Q = K3_P + 1;
DI void k3_wload(u32x4 (&dst)[4], const char* base, int s, int rot, unsigned voff) {
  const int sa = s + rot >= 9 ? s + rot - 9 : s + rot;
  sfor<4>([&](auto FI) { constexpr int f = decltype(FI)::value; gload16<f * 1024>(dst[f], base + sa * 4096, voff); });
}
template <int NTK, bool FIRST, bool NEXT, int QB, int NST, class HL>
DI void k3_unit(f32x4 (&acc)[2][8], u32x4 (&wq)[K3_Q][4], const char* cur, const char* nxt, const unsigned (&lb)[3], int rot, unsigned voff, HL&& hload) {
  constexpr int NG = NTK / 4;
  u32x4 af[2][4];
  auto aread = [&](auto II) {
    constexpr int it = decltype(II)::value, ks = it / NG, gp = it % NG;
#pragma unroll
    for (int tk = 0; tk < 4; ++tk) af[it & 1][tk] = *(const LAS u32x4*)(size_t)(lb[ks / 6] + ((ks % 6) * 8 + gp * 4 + tk) * 1024);
  };
  aread(std::integral_constant<int, 0>{});
  sfor<9>([&](auto SI) {
    constexpr int s = decltype(SI)::value;
    if constexpr (FIRST) {
      if constexpr (s == 1) { wait_vm<4>(); bar(); }
      if constexpr (s + 2 < 9) k3_wload(wq[(QB + s + 2) % K3_Q], cur, s + 2, rot, voff);
      if constexpr (s < 2) sfor<6>([&](auto HI) { hload(std::integral_constant<int, 6 + 6 * s + decltype(HI)::value>{}); });
      constexpr int n0 = s < 6 ? 0 : (K3_P * (s - 6)) / 3, n1 = s < 6 ? 0 : (K3_P * (s - 5)) / 3;
      constexpr int m0 = K3_P == 2 ? (s < 7 ? 0 : s - 7) : n0, m1 = K3_P == 2 ? (s < 7 ? 0 : s - 6) : n1;
      sfor<m1 - m0>([&](auto JI) { constexpr int j = m0 + decltype(JI)::value; static_assert(9 + j - K3_Q < s, "slot busy"); k3_wload(wq[(QB + 9 + j) % K3_Q], nxt, j, rot, voff); });
      if constexpr (s == 0) wait_vm<2 + 4 + 4 + 6>();
      else if constexpr (s >= 3) wait_vm<(s <= 6 ? 8 : (s == 7 ? 4 : 0)) + 4 * m1>();
      if constexpr (s == 3) bar();
    } else {
      if constexpr (s + K3_P < 9) k3_wload(wq[(QB + s + K3_P) % K3_Q], cur, s + K3_P, rot, voff);
      else if constexpr (NEXT) k3_wload(wq[(QB + s + K3_P) % K3_Q], nxt, s + K3_P - 9, rot, voff);
      constexpr int ny = NEXT ? K3_P : ((8 - s) < K3_P ? (8 - s) : K3_P);
      wait_vm<4 * ny + (s < K3_P ? NST : 0)>();
    }
    sfor<4>([&](auto FI) { pin(wq[(QB + s) % K3_Q][decltype(FI)::value]); });
    sfor<2 * NG>([&](auto QI) {
      constexpr int q = decltype(QI)::value, it = s * 2 * NG + q, ks = it / NG, gp = it % NG, kq = ks & 1;
      if constexpr (it + 1 < 18 * NG) aread(std::integral_constant<int, it + 1>{});
      sb0();
#pragma unroll
      for (int f = 0; f < 2; ++f)
#pragma unroll
        for (int tk = 0; tk < 4; ++tk)
          acc[f][gp * 4 + tk] = mfma16(wq[(QB + s) % K3_Q][kq * 2 + f], af[it & 1][tk], ks == 0 ? (f32x4){} : acc[f][gp * 4 + tk]);
      sb0();
    });
  });
}

template <int HALF>
DI void k3_body(const char* __restrict__ ws_in, char* __restrict__ ws_out, char* lds, int w) {
  TLSCOPE(3);
  const int tid = threadIdx.x, lane = tid & 63;
  const int b = blockIdx.x, xcd = b & 7, idx = b >> 3;
  const int rt = (xcd & 1) * 8 + (idx & 7), fgp = (xcd >> 1) * 4 + (idx >> 3);
  const unsigned ldsb = (unsigned)(size_t)(LAS char*)lds;
  const unsigned voff = lane * 16;
  const int g = lane >> 4, l15 = lane & 15;
  const unsigned avoff = (g >> 1) * 1024 + (g & 1) * 512 + l15 * 16;
  const char* hsrc = ws_in + WS_HF + (size_t)(rt * 4 + (w >> 1)) * 36 * 1024 + (w & 1) * 256;
#define K3_ROT 0
  const int rot = K3_ROT;
  auto hload = [&](auto I) { constexpr int i = decltype(I)::value; const int ia = i + 2 * rot >= 18 ? i + 2 * rot - 18 : i + 2 * rot;
    glds16(hsrc + ia * 2048, avoff, ldsb + (i * 8 + w) * 1024); };
  auto nohl = [&](auto) {};
  const int t0 = w, t1 = w + 8, t2 = 16 + ((w >> 1) & 1);
  const char* wgu = ws_in + WS_WGU + (size_t)fgp * 18 * 36 * 1024;
  const char* sA = wgu + (size_t)t0 * 36 * 1024;
  const char* sB = wgu + (size_t)t1 * 36 * 1024;
  const char* sC = wgu + (size_t)t2 * 36 * 1024;
  u32x4 wq[K3_Q][4];
  u32x4 stv[2];
  {
    const char* sp = ws_in + WS_HST + (size_t)(rt * 128 + w * 16) * 128;
    gload16<0>(stv[0], sp, l15 * 128 + g * 32); gload16<16>(stv[1], sp, l15 * 128 + g * 32);
  }
  sfor<4>([&](auto I) { hload(I); });
  k3_wload(wq[0], sA, 0, rot, voff);
  hload(std::integral_constant<int, 4>{}); hload(std::integral_constant<int, 5>{});
  k3_wload(wq[1], sA, 1, rot, voff);
  wait_vm<10>();
  float* ctab = (float*)(lds + 144 * 1024);
  {
    pin(stv[0]); pin(stv[1]);
    float sm = (__uint_as_float(stv[0][0]) + __uint_as_float(stv[0][2])) + (__uint_as_float(stv[1][0]) + __uint_as_float(stv[1][2]));
    float sq = (__uint_as_float(stv[0][1]) + __uint_as_float(stv[0][3])) + (__uint_as_float(stv[1][1]) + __uint_as_float(stv[1][3]));
    sm += __shfl_xor(sm, 16); sq += __shfl_xor(sq, 16);
    sm += __shfl_xor(sm, 32); sq += __shfl_xor(sq, 32);
    const float mean = sm * (1.f / D_);
    const float rstd = ln_rstd(sq * (1.f / D_) - mean * mean);
    if (lane < 16) {
      ctab[w * 16 + lane] = -1.4426950408889634f * rstd;
      if (fgp == 0) ((float*)(ws_out + WS_R2))[rt * 128 + w * 16 + lane] = rstd * rstd;
    }
  }
  wait_lgkm0();
  bar();
  float c1[8];
#pragma unroll
  for (int tk = 0; tk < 8; ++tk) c1[tk] = ctab[tk * 16 + l15];
  tlmark(24); ckmark(0);
  unsigned lb[3] = {ldsb + voff, ldsb + voff + 49152, ldsb + voff + 98304};
  unsigned lh[3] = {lb[0] + (w & 1) * 4096, lb[1] + (w & 1) * 4096, lb[2] + (w & 1) * 4096};
  asm volatile("" : "+v"(lb[0]), "+v"(lb[1]), "+v"(lb[2]), "+v"(lh[0]), "+v"(lh[1]), "+v"(lh[2]));
  f32x4 acc[2][8];
  auto swiglu = [&](const f32x4& a, const f32x4& u, float c) -> u32x2 {
    float r[4];
#pragma unroll
    for (int j = 0; j < 4; ++j) { float e = __builtin_amdgcn_exp2f(a[j] * c); float pr = a[j] * u[j]; pin(pr); r[j] = pr * __builtin_amdgcn_rcpf(1.f + e); pin(r[j]); }
    return (u32x2){pk2(r[0], r[1]), pk2(r[2], r[3])};
  };
  char* actp = ws_out + WS_ACT + ((size_t)rt * 4 * 288 + fgp * 18) * 1024 + ((g & 1) * 32 + (g >> 1) * 16 + l15) * 16;
  auto epi2 = [&](int T, int tile32, const f32x4& g0, const f32x4& u0, float ca, const f32x4& g1, const f32x4& u1, float cb) {
    const u32x2 x0 = swiglu(g0, u0, ca), x1 = swiglu(g1, u1, cb);
    auto r0 = __builtin_amdgcn_permlane32_swap(x0[0], x1[0], false, false);
    auto r1 = __builtin_amdgcn_permlane32_swap(x0[1], x1[1], false, false);
    store16<2>(actp + ((size_t)tile32 * 288 + T) * 1024, (u32x4){r0[0], r1[0], r0[1], r1[1]});
  };
  auto epi_full = [&](int T) {
#pragma unroll
    for (int i = 0; i < 4; ++i) epi2(T, i, acc[0][2 * i], acc[1][2 * i], c1[2 * i], acc[0][2 * i + 1], acc[1][2 * i + 1], c1[2 * i + 1]);
  };
  k3_unit<8, true, true, 0, 0>(acc, wq, sA, HALF == 0 ? sC : sB, lb, rot, voff, hload);
  tlmark(25); ckmark(1);
  epi_full(t0);
  ckmark(2);
  if constexpr (HALF == 0) {
    k3_unit<4, false, true, 9 % K3_Q, 4>(acc, wq, sC, sB, lh, rot, voff, nohl);
    const bool hi = w & 1;
#pragma unroll
    for (int i = 0; i < 2; ++i) {
      float a0 = c1[2 * i], a1 = c1[4 + 2 * i], b0 = c1[2 * i + 1], b1 = c1[5 + 2 * i]; pin(a0); pin(a1); pin(b0); pin(b1);
      const float ca = hi ? a1 : a0, cb = hi ? b1 : b0;
      epi2(t2, (w & 1) * 2 + i, acc[0][2 * i], acc[1][2 * i], ca, acc[0][2 * i + 1], acc[1][2 * i + 1], cb);
    }
  }
  k3_unit<8, false, false, (HALF == 0 ? 18 : 9) % K3_Q, HALF == 0 ? 2 : 4>(acc, wq, sB, sB, lb, rot, voff, nohl);
  tlmark(29); ckmark(3);
  epi_full(t1);
  ckmark(4);
}
__global__ __launch_bounds__(512, 2) void k3_gateup(const char* __restrict__ ws_in, char* __restrict__ ws_out) {
  extern __shared__ __attribute__((aligned(16))) char lds[];
  const int w = uni(threadIdx.x >> 6);
  if (w < 4) k3_body<0>(ws_in, ws_out, lds, w); else k3_body<1>(ws_in, ws_out, lds, w);
}

#define K4_LA 4
#define K4_LW 4
constexpr int K4_R = 8, K4_D = 4;
constexpr int K4_LDS = K4_R * 16384;
constexpr int K4_NST = 21;
struct K4Sched {
  int aslot[18], sslot[K4_NST], posA[18], posS[K4_NST], cntBar[K4_NST], cntSlot[162];
  constexpr K4Sched() : aslot{}, sslot{}, posA{}, posS{}, cntBar{}, cntSlot{} {
    for (int a = 0; a < 18; ++a) aslot[a] = a < K4_LA ? -1 : 9 * (a - K4_LA) + 4;
    for (int s = 0; s < K4_NST; ++s) sslot[s] = s < K4_LW ? -1 : s - K4_LW;
    int c = 0;
    for (int s = 0; s < (K4_LW > K4_LA ? K4_LW : K4_LA); ++s) {
      if (s < K4_LW) { c += 2; posS[s] = c; }
      if (s < K4_LA) { c += 2; posA[s] = c; }
    }
    for (int n = -K4_D; n < 162; ++n) {
      if ((n + K4_D) % 8 == 0 && (n + K4_D) < 162) {
        int i = (n + K4_D) / 8; cntBar[i] = c;
        for (int s = 0; s < K4_NST; ++s) if (sslot[s] == i) { c += 2; posS[s] = c; }
      }
      if (n >= 0) {
        for (int q = 0; q < 18; ++q) if (aslot[q] == n) { c += 2; posA[q] = c; }
        cntSlot[n] = c;
      }
    }
  }
};
constexpr K4Sched k4s{};
DI u32x4 load16_sc1(const void* p) { u32x4 v; asm volatile("global_load_dwordx4 %0, %1, off sc1" : "=v"(v) : "v"(p) : "memory"); return v; }
DI u32x2 load8_sc1(const void* p) { u32x2 v; asm volatile("global_load_dwordx2 %0, %1, off sc1" : "=v"(v) : "v"(p) : "memory"); return v; }
DI unsigned load4_sc1(const void* p) { unsigned v; asm volatile("global_load_dword %0, %1, off sc1" : "=v"(v) : "v"(p) : "memory"); return v; }
DI void store8_sc1(void* p, u32x2 v) { asm volatile("global_store_dwordx2 %0, %1, off sc1\n\ts_nop 1" :: "v"(p), "v"(v) : "memory"); }

__global__ __launch_bounds__(512, 2) void k4_down(const char* __restrict__ ws_in, char* __restrict__ ws_out, u16* __restrict__ out) {
  TLSCOPE(4);
  extern __shared__ __attribute__((aligned(16))) char lds[];
  const int tid = threadIdx.x, lane = tid & 63, w = uni(tid >> 6);
  const int b = blockIdx.x, xcd = b & 7, idx = b >> 3;
  const int ksl = xcd >> 1, mt = (xcd & 1) * 8 + (idx & 7), ng = idx >> 3;
  const int tg = w & 3, kh = w >> 2;
  const int tt = mt * 4 + tg;
  const unsigned ldsb = (unsigned)(size_t)(LAS char*)lds;
  const char* wsrc = ws_in + WS_WD + (size_t)((ng * 4 + ksl) * 2 + kh) * WD_STREAM * 1024 + tg * 2048;
  const char* asrc = ws_in + WS_ACT + ((size_t)tt * 288 + ksl * 72 + kh * 36) * 1024;
  const unsigned voff = lane * 16;
  const int g = lane >> 4;
  const unsigned avoff0 = (g >> 1) * 1024 + (g & 1) * 512 + (lane & 15) * 16 + kh * 256, avoff1 = avoff0 ^ 256;
  const int th = w & 1, a = w >> 1, ott = mt * 4 + ksl, tok = th * 16 + (lane & 15);
  const int col = 32 * a + 8 * g, col8 = 128 + 4 * g;
  u32x2 h0, h1, h2; unsigned r2b, mub;
  {
    const char* hfp = ws_in + WS_HF + ((size_t)ott * 36 + ng * 9) * 1024;
    auto ho = [&](int c) -> unsigned { return (c >> 4) * 1024 + (((c >> 2) & 1) * 32 + tok) * 16 + ((c >> 3) & 1) * 8; };
    gload8<0>(h0, hfp, ho(col)); gload8<512>(h1, hfp, ho(col)); gload8<0>(h2, hfp, ho(col8));
    gload4<0>(r2b, ws_in + WS_R2 + (size_t)ott * 128, tok * 4); gload4<0>(mub, ws_in + WS_MEAN + (size_t)ott * 128, tok * 4);
  }
  u32x4 af[K4_LA + 1][2];
  auto issue_stage = [&](auto SI) {
    constexpr int s = decltype(SI)::value;
    glds16x2(wsrc + (size_t)s * 8192, voff, ldsb + (s % K4_R) * 16384 + kh * 8192 + tg * 2048);
  };
  auto issue_a = [&](auto AI) {
    constexpr int a = decltype(AI)::value;
    gload16<(a % 2) * 2048>(af[a % (K4_LA + 1)][0], asrc + (a / 2) * 4096, avoff0);
    gload16<(a % 2) * 2048>(af[a % (K4_LA + 1)][1], asrc + (a / 2) * 4096, avoff1);
  };
  sfor<(K4_LW > K4_LA ? K4_LW : K4_LA)>([&](auto SI) {
    constexpr int s = decltype(SI)::value;
    if constexpr (s < K4_LW) issue_stage(SI);
    if constexpr (s < K4_LA) issue_a(SI);
    if constexpr (s == 0) bar();
  });
  f32x4 acc[9][2];
  u32x4 wf[K4_D + 1];
  unsigned lrd0 = ldsb + kh * 8192 + lane * 16, lrd1 = lrd0 + 65536;
  unsigned xw = ldsb + w * 1024 + lane * 16, xw5 = xw + 5 * 16384;
  asm volatile("" : "+v"(lrd0), "+v"(lrd1), "+v"(xw), "+v"(xw5));
  auto xad = [&](unsigned lo, unsigned hi, int t) -> unsigned { return t < 6 ? hi + t * 8192 : lo + (t - 6) * 8192; };
  auto rd = [&](auto NI) {
    constexpr int n = decltype(NI)::value;
    if constexpr (n < 162) {
      constexpr int st = n / 8;
      if constexpr (n % 8 == 0) {
        wait_vm<k4s.cntBar[st] - k4s.posS[st]>();
        bar();
        if constexpr (st == 0) { tlmark(26); ckmark(5); }
        sfor<K4_NST>([&](auto SI) { if constexpr (k4s.sslot[decltype(SI)::value] == st) issue_stage(SI); });
      }
      constexpr int off = (st % K4_R) * 16384 + (n % 8) * 1024;
      wf[n % (K4_D + 1)] = *(const LAS u32x4*)(size_t)((off < 65536 ? lrd0 : lrd1) + (off & 65535));
    }
  };
  sfor<K4_D>([&](auto I) { rd(I); });
  sfor<162>([&](auto NI) {
    constexpr int n = decltype(NI)::value;
    constexpr int ks = n / 9, t = n % 9, ab = ks % (K4_LA + 1);
    rd(std::integral_constant<int, n + K4_D>{});
    sfor<18>([&](auto AI) { if constexpr (k4s.aslot[decltype(AI)::value] == n) issue_a(AI); });
    if constexpr (t == 0) {
      wait_vm<k4s.cntSlot[n] - k4s.posA[ks]>();
      pin(af[ab][0]); pin(af[ab][1]);
    }
    acc[t][0] = mfma16(wf[n % (K4_D + 1)], af[ab][0], ks == 0 ? (f32x4){} : acc[t][0]);
    acc[t][1] = mfma16(wf[n % (K4_D + 1)], af[ab][1], ks == 0 ? (f32x4){} : acc[t][1]);
    if constexpr (n >= 155) *(LAS f32x4*)(size_t)xad(xw, xw5, n - 155) = acc[n - 155][1];
    sb0();
  });
  tlmark(27); ckmark(6);
  *(LAS f32x4*)(size_t)xad(xw, xw5, 7) = acc[7][1]; *(LAS f32x4*)(size_t)xad(xw, xw5, 8) = acc[8][1];
  wait_lgkm0(); bar();
  tlmark(20);
  const unsigned xr = ldsb + (w ^ 4) * 1024 + lane * 16, xr5 = xr + 5 * 16384;
  f32x4 v[9];
#pragma unroll
  for (int t = 0; t < 9; ++t) v[t] = *(const LAS f32x4*)(size_t)xad(xr, xr5, t) + acc[t][0];
  if (tg == ksl) {
#pragma unroll
    for (int t = 0; t < 9; ++t) *(LAS f32x4*)(size_t)xad(xr, xr5, t) = v[t];
  } else {
    char* pp = ws_out + WS_PART + ((((size_t)ksl * 64 + tt) * 4 + ng) * 2 + kh) * 5120;
#pragma unroll
    for (int i = 0; i < 4; ++i)
      store16<2>(pp + i * 1024 + lane * 16, (u32x4){pk2(v[2 * i][0], v[2 * i][1]), pk2(v[2 * i][2], v[2 * i][3]), pk2(v[2 * i + 1][0], v[2 * i + 1][1]), pk2(v[2 * i + 1][2], v[2 * i + 1][3])});
    store8_sc1(pp + 4096 + lane * 8, (u32x2){pk2(v[8][0], v[8][1]), pk2(v[8][2], v[8][3])});
    wait_vm<0>();
    if (lane == 0) __hip_atomic_fetch_add((unsigned*)(ws_out + WS_CNT) + ((mt * 4 + ng) * 4 + tg) * 32, 1u, __ATOMIC_RELAXED, __HIP_MEMORY_SCOPE_AGENT);
  }
  tlmark(21);
  if (w == ksl) {
    const unsigned* cnt = (const unsigned*)(ws_out + WS_CNT) + ((mt * 4 + ng) * 4 + ksl) * 32;
    unsigned spins = 0;
    while (__hip_atomic_load(cnt, __ATOMIC_RELAXED, __HIP_MEMORY_SCOPE_AGENT) < 6u) {
      __builtin_amdgcn_s_sleep(1);
      if (++spins > (1u << 22)) break;
    }
  }
  wait_lgkm0();
  bar();
  tlmark(28);
  const unsigned ox = ldsb + ((th ^ 1) * 4 + ksl) * 1024 + lane * 16, oa = ox + (a < 3 ? 5 * 16384 + a * 16384 : 0);
  f32x4 s0 = *(const LAS f32x4*)(size_t)oa, s1 = *(const LAS f32x4*)(size_t)(oa + 8192), s2 = *(const LAS f32x4*)(size_t)(ox + 16384);
  const char* pb = ws_in + WS_PART + (((size_t)ott * 4 + ng) * 2 + th) * 5120;
  constexpr size_t KS = (size_t)64 * 4 * 2 * 5120;
  u32x4 pv[3]; unsigned pw[3][2];
#pragma unroll
  for (int k = 0; k < 3; ++k) {
    const char* q = pb + (size_t)((ksl + 1 + k) & 3) * KS;
    pv[k] = load16_sc1(q + a * 1024 + lane * 16);
    const char* q8 = q + (a == 0 ? 4096 + lane * 8 : a * 1024);
    pw[k][0] = load4_sc1(q8); pw[k][1] = load4_sc1(q8 + 4);
  }
  wait_vm<0>();
#pragma unroll
  for (int k = 0; k < 3; ++k) { pin(pv[k]); pin(pw[k][0]); pin(pw[k][1]); }
#pragma unroll
  for (int k = 0; k < 3; ++k) {
    const unsigned a0 = pv[k][0], a1 = pv[k][1], a2 = pv[k][2], a3 = pv[k][3], b0 = pw[k][0], b1 = pw[k][1];
    s0 += (f32x4){bflo(a0), bfhi(a0), bflo(a1), bfhi(a1)}; s1 += (f32x4){bflo(a2), bfhi(a2), bflo(a3), bfhi(a3)}; s2 += (f32x4){bflo(b0), bfhi(b0), bflo(b1), bfhi(b1)};
  }
  pin(h0); pin(h1); pin(h2); pin(r2b); pin(mub);
  const float r2 = __uint_as_float(r2b), mu = __uint_as_float(mub);
  u16* orow = out + (size_t)(ott * 32 + tok) * D_ + ng * 144;
  s0 = s0 * r2 + mu; s1 = s1 * r2 + mu; s2 = s2 * r2 + mu;
  u32x4 o;
  o[0] = pk2(s0[0] + bflo(h0[0]), s0[1] + bfhi(h0[0])); o[1] = pk2(s0[2] + bflo(h0[1]), s0[3] + bfhi(h0[1]));
  o[2] = pk2(s1[0] + bflo(h1[0]), s1[1] + bfhi(h1[0])); o[3] = pk2(s1[2] + bflo(h1[1]), s1[3] + bfhi(h1[1]));
  *(u32x4*)(orow + col) = o;
  if (a == 0) *(u32x2*)(orow + col8) = (u32x2){pk2(s2[0] + bflo(h2[0]), s2[1] + bfhi(h2[0])), pk2(s2[2] + bflo(h2[1]), s2[3] + bfhi(h2[1]))};
}
}
#define PACK_ST 2
namespace fk {
constexpr size_t WS_PQ   = (WS_END_FFN + 4095) / 4096 * 4096;
constexpr size_t WS_KVP  = WS_PQ + SZ_HF;
constexpr size_t SZ_KVP  = (size_t)16 * 8 * 16 * 1024;
constexpr size_t WS_KVF  = WS_KVP + SZ_KVP;
constexpr size_t SZ_KVF  = 64 * 1024;
constexpr size_t WS_WOP  = WS_KVF + SZ_KVF;
constexpr size_t SZ_WOP  = (size_t)4 * 4 * 9 * 5 * 1024;
constexpr size_t WS_MX   = WS_WOP + SZ_WOP;
constexpr size_t WS_END  = WS_MX + S_ * 4;

DI float phi_f(float t) { return (t > 0.f ? t + 1.f : __builtin_amdgcn_exp2f(t * 1.4426950408889634f)) + 1e-6f; }

DI void pack_wo_task(const u16* wo, char* ws, int id) {
  const int lane = id & 63, fi = id >> 6, i = fi % 5, u = (fi / 5) % 9, w = (fi / 45) & 3, ng = fi / 180;
  const int row = lane & 31, h = lane >> 5;
  if (i == 4 && row >= 16) {
    if (row >= 18) *(u32x4*)(ws + WS_WOP + (size_t)id * 16) = (u32x4){0u, 0u, 0u, 0u};
    return;
  }
  const int n = ng * 144 + i * 32 + row;
  unsigned v[8];
#pragma unroll
  for (int j = 0; j < 8; ++j) {
    const int e = u < 8 ? 18 * (8 * w + u) + 8 * (j >> 2) + 4 * h + (j & 3) : 18 * (8 * w + 4 * h + (j >> 1)) + 16 + (j & 1);
    v[j] = wo[e * D_ + n];
  }
  *(u32x4*)(ws + WS_WOP + (size_t)id * 16) = (u32x4){v[0] | (v[1] << 16), v[2] | (v[3] << 16), v[4] | (v[5] << 16), v[6] | (v[7] << 16)};
}
DI void pack_worow_task(const u16* wo, char* ws, int e, int lane) {
  float s = 0.f;
#pragma unroll
  for (int i = 0; i < 9; ++i) s += bf2f(wo[e * D_ + i * 64 + lane]);
#pragma unroll
  for (int o = 32; o; o >>= 1) s += __shfl_xor(s, o);
  const unsigned hi = pk2(s, 0.f) & 0xffffu; const unsigned lo = pk2(s - bflo(hi), 0.f) & 0xffffu;
  const int hh = e / 18, el = e % 18, w = hh >> 3, ul = hh & 7;
  int u, h, j;
  if (el < 16) { u = ul; h = (el >> 2) & 1; j = (el >> 3) * 4 + (el & 3); } else { u = 8; h = ul >> 2; j = (ul & 3) * 2 + (el - 16); }
  if (lane < 8) {
    const int ng = lane >> 1, part = lane & 1;
    u16* p = (u16*)(ws + WS_WOP) + ((((size_t)(ng * 4 + w) * 9 + u) * 5 + 4) * 64 + h * 32 + 16 + part) * 8 + j;
    *p = (u16)(part ? lo : hi);
  }
}

constexpr int K1_WST = 18432, K1_STG = K1_WST + 16384, K1_R = 4;
constexpr int K1_TAB = K1_STG * K1_R;
constexpr int K1_LDS = 8 * 18432;
static_assert(K1_TAB + 4 * 144 * 4 + 1024 <= K1_LDS, "lds");
#define K1_NGU 64
#define K1_NOPACK 0
#define K1_DD 3
constexpr int K1_D = K1_DD;
DI float phi3(float u, float rs, float rl) { return fmaxf(fmaf(u, rs, 1e-6f), 1e-6f) + __builtin_amdgcn_exp2f(fminf(u * rl, 0.f)); }
template <bool ISQ, int NH> constexpr int k1_tile(int j) {
  if (ISQ) return NH ? 5 + j : j;
  if (NH == 0) return j < 2 ? j : (j == 2 ? 4 : j + 2);
  return j < 2 ? 2 + j : 5 + j;
}
template <bool ISQ> constexpr int k1_toff(int t) { return ISQ ? 32 * t : (t < 4 ? 32 * t : 144 + 32 * (t - 5)); }

__host__ __device__ constexpr int k1_img(int t) { return t < 8 ? t * 4096 : 3 * K1_STG + (t - 8) * 4096; }
template <bool ISQ, int NH, class ISSUE, class TAIL>
DI void k1_body(char* lds, unsigned ldsb, int lane, int tg, int rt, int grp, char* ws, ISSUE&& issue, TAIL&& tail) {
  constexpr int NT = NH ? 4 : 5, NWF = NT + K1_D;
  const int g = lane >> 4, l15 = lane & 15, q4 = (lane >> 2) & 3, p4 = lane & 3;
  const unsigned rrow = ldsb + (16 * (g >> 1) + 4 * (g & 1) + q4) * 288;
  unsigned rbA[2], rbB[2], rbX[2][2];
  rbA[0] = rrow + p4 * 8;
  rbB[0] = rrow + (p4 < 2 ? 128 + 8 * p4 : 272 + 8 * (p4 - 2));
  rbA[1] = rbA[0] + 2 * K1_STG; rbB[1] = rbB[0] + 2 * K1_STG;
  const unsigned xb = ldsb + K1_WST + tg * 4096 + l15 * 128;
#pragma unroll
  for (int kh = 0; kh < 2; ++kh) { rbX[kh][0] = xb + (((4 * kh + g) ^ ((l15 >> 1) & 7)) * 16); rbX[kh][1] = rbX[kh][0] + 2 * K1_STG; }
  asm volatile("" : "+v"(rbA[0]), "+v"(rbA[1]), "+v"(rbB[0]), "+v"(rbB[1]), "+v"(rbX[0][0]), "+v"(rbX[0][1]), "+v"(rbX[1][0]), "+v"(rbX[1][1]));
  u32x4 ones = {0x3f803f80u, 0x3f803f80u, 0x3f803f80u, 0x3f803f80u};
  pin(ones);
  f32x4 acc[NT][2], accs[NT], sst[2][2];
#pragma unroll
  for (int t = 0; t < NT; ++t) accs[t] = (f32x4){};
  u32x4 xf[2][2], wf[NWF];
  auto rd = [&](auto NI) {
    constexpr int n = decltype(NI)::value;
    if constexpr (n < 18 * NT) {
      constexpr int ks = n / NT, j = n % NT, st = ks >> 1, kh = ks & 1, slot = st % K1_R, hb = slot >> 1, so = (slot & 1) * K1_STG;
      constexpr int t = k1_tile<ISQ, NH>(j);
      constexpr int pos = n % (2 * NT);
      constexpr int nxt = st == 0 ? 3 : (st >= 2 && st + 2 < 9 ? st + 2 : -1);
      if constexpr (pos == 0) {
        constexpr int upto = st == 0 ? 2 : (st + 1 > 3 ? (st + 1 < 8 ? st + 1 : 8) : 3);
        if constexpr (NH == 1 && st == 8) tail(std::integral_constant<int, 0>{});
        else wait_vm<4 * (upto - st)>();
        bar();
        if constexpr (st == 0) tlmark(17);
        if constexpr (NH == 1 && st >= 7) tail(std::integral_constant<int, st>{});
      }
      if constexpr (j == 0) {
        xf[kh][0] = *(const LAS u32x4*)(size_t)(rbX[kh][hb] + so);
        xf[kh][1] = *(const LAS u32x4*)(size_t)(rbX[kh][hb] + so + 2048);
      }
      const unsigned a = ((!ISQ && t == 4) ? rbB[hb] : rbA[hb] + k1_toff<ISQ>(t)) + so + kh * 9216;
      u32x2 lo = __builtin_bit_cast(u32x2, tr_read(a)), hi = __builtin_bit_cast(u32x2, tr_read(a + 8 * 288));
      wf[n % NWF] = (u32x4){lo[0], lo[1], hi[0], hi[1]};
      if constexpr (nxt >= 0 && (pos & 1) == 1 && pos / 2 < 4) issue(std::integral_constant<int, nxt>{}, std::integral_constant<int, pos / 2>{});
    }
  };
  sfor<K1_D>([&](auto I) { rd(I); });
  sfor<18 * NT>([&](auto NI) {
    constexpr int n = decltype(NI)::value, ks = n / NT, j = n % NT, kh = ks & 1;
    rd(std::integral_constant<int, n + K1_D>{});
    const u32x4 wv = wf[n % NWF];
    sfor<2>([&](auto HI) {
      constexpr int th = decltype(HI)::value;
      const f32x4 c = ks == 0 ? (f32x4){} : acc[j][th];
      acc[j][th] = ISQ ? mfma16(wv, xf[kh][th], c) : mfma16(xf[kh][th], wv, c);
    });
    if constexpr (NH == 1 && j < 2) {
      sst[j][0] = mfma16(ones, xf[kh][j], ks == 0 ? (f32x4){} : sst[j][0]);
      sst[j][1] = mfma16(xf[kh][j], xf[kh][j], ks == 0 ? (f32x4){} : sst[j][1]);
    }
    if constexpr (j == NT - 1) {
      if (tg == (ks & 3)) {
        sfor<NT>([&](auto JI) { constexpr int jj = decltype(JI)::value; accs[jj] = mfma16(ones, wf[(ks * NT + jj) % NWF], accs[jj]); });
      }
    }
    sb0();
  });
  tlmark(16);
  float* tab = (float*)(lds + K1_TAB);
  float* stt = tab + 4 * 144;
  if (g == 0) {
    sfor<NT>([&](auto JI) { constexpr int jj = decltype(JI)::value; tab[tg * 144 + k1_tile<ISQ, NH>(jj) * 16 + l15] = accs[jj][0]; });
  }
  if constexpr (NH == 1) {
    if (g == (l15 >> 2)) {
#pragma unroll
      for (int th = 0; th < 2; ++th) {
        const int r = l15 & 3;
        const float q = r == 0 ? sst[th][1][0] : (r == 1 ? sst[th][1][1] : (r == 2 ? sst[th][1][2] : sst[th][1][3]));
        const float mean = sst[th][0][0] * (1.f / D_);
        stt[tg * 32 + th * 16 + l15] = mean;
        stt[128 + tg * 32 + th * 16 + l15] = ln_rstd(q * (1.f / D_) - mean * mean);
      }
    }
  }
  wait_lgkm0(); bar();
  constexpr float L2E = 1.4426950408889634f;
  if constexpr (ISQ) {
    const int tt = rt * 4 + tg;
    float nm[2], rs[2], rl[2];
#pragma unroll
    for (int th = 0; th < 2; ++th) { nm[th] = -stt[tg * 32 + th * 16 + l15]; rs[th] = stt[128 + tg * 32 + th * 16 + l15]; rl[th] = rs[th] * L2E; }
    if (NH == 0 && grp == 0 && g < 2) ((float*)(ws + WS_MX))[tt * 32 + g * 16 + l15] = g ? -nm[1] : -nm[0];
    char* pq = ws + WS_PQ + ((size_t)tt * 36 + grp * 9) * 1024 + ((g & 1) * 32 + (g >> 1) * 16 + l15) * 16;
    sfor<NT>([&](auto JI) {
      constexpr int j = decltype(JI)::value, t = k1_tile<ISQ, NH>(j);
      const float* cp = tab + t * 16 + g * 4;
      const f32x4 cs = (*(const f32x4*)cp + *(const f32x4*)(cp + 144)) + (*(const f32x4*)(cp + 288) + *(const f32x4*)(cp + 432));
      unsigned pk[2][2];
#pragma unroll
      for (int th = 0; th < 2; ++th) {
        float v[4];
#pragma unroll
        for (int e = 0; e < 4; ++e) v[e] = phi3(fmaf(nm[th], cs[e], acc[j][th][e]), rs[th], rl[th]);
        pk[th][0] = pk2(v[0], v[1]); pk[th][1] = pk2(v[2], v[3]);
      }
      auto r0 = __builtin_amdgcn_permlane32_swap(pk[0][0], pk[1][0], false, false);
      auto r1 = __builtin_amdgcn_permlane32_swap(pk[0][1], pk[1][1], false, false);
      *(u32x4*)(pq + t * 1024) = (u32x4){r0[0], r1[0], r0[1], r1[1]};
    });
  } else {
    f32x4 mn[2], rs[2], rl[2];
#pragma unroll
    for (int th = 0; th < 2; ++th) { mn[th] = *(const f32x4*)(stt + tg * 32 + th * 16 + g * 4); rs[th] = *(const f32x4*)(stt + 128 + tg * 32 + th * 16 + g * 4); rl[th] = rs[th] * L2E; }
    char* ob = lds + (tg * 64 + lane) * 16;
    sfor<NT>([&](auto JI) {
      constexpr int j = decltype(JI)::value, t = k1_tile<ISQ, NH>(j);
      const float* cp = tab + t * 16 + l15;
      const float ncs = -((cp[0] + cp[144]) + (cp[288] + cp[432]));
      u32x4 pv;
#pragma unroll
      for (int th = 0; th < 2; ++th) {
        float v[4];
#pragma unroll
        for (int e = 0; e < 4; ++e) {
          const float u = fmaf(mn[th][e], ncs, acc[j][th][e]);
          if (t < 4) v[e] = phi3(u, rs[th][e], rl[th][e]);
          else if (t > 4) v[e] = u * rs[th][e];
          else v[e] = l15 < 8 ? phi3(u, rs[th][e], rl[th][e]) : u * rs[th][e];
        }
        pv[2 * th] = pk2(v[0], v[1]); pv[2 * th + 1] = pk2(v[2], v[3]);
      }
      *(u32x4*)(ob + k1_img(t)) = pv;
      if constexpr (t == 4) *(u32x4*)(ob + k1_img(9)) = l15 < 8 ? ones : pv;
    });
    wait_lgkm0(); bar();

    const int w = NH * 4 + tg;
#pragma unroll
    for (int pi = 0; pi < 2; ++pi) {
      const int p = 2 * w + pi;
      int a, bt;
      if (p < 10) { a = (p + 1) / 3; bt = 5 + a + ((p + 1) % 3) - 1; } else if (p == 10) { a = 4; bt = 8; } else { a = p - 11; bt = 9; }
      f32x4 c = {};
#pragma unroll
      for (int i = 0; i < 4; ++i) {
        const u32x4 av = *(const u32x4*)(lds + a * 4096 + (i * 64 + lane) * 16);
        const u32x4 bv = *(const u32x4*)(lds + k1_img(bt) + (i * 64 + lane) * 16);
        c = mfma16(av, bv, c);
      }
      *(f32x4*)(ws + WS_KVP + (((size_t)rt * 8 + grp) * 16 + p) * 1024 + lane * 16) = c;
    }
  }
}

__global__ __launch_bounds__(512, 2) void k1_qkv(const u16* __restrict__ x, const u16* __restrict__ wqkv, const u16* __restrict__ wo,
                                                  const u16* __restrict__ wg, const u16* __restrict__ wu, const u16* __restrict__ wd, char* __restrict__ ws) {
  TLSCOPE(0);
  extern __shared__ __attribute__((aligned(16))) char lds[];
  const int tid = threadIdx.x, lane = tid & 63, w = uni(tid >> 6);
  const int b = blockIdx.x, xcd = b & 7, idx = b >> 3;
  const unsigned ldsb = (unsigned)(size_t)(LAS char*)lds;
  if (idx >= 24) {
    if (K1_NOPACK) return;
    const int pw = ((idx - 24) * 8 + xcd) * 8 + w;
    if (pw < 4) ((unsigned*)(ws + WS_CNT))[(pw * 64 + lane) * 32] = 0u;
    const int u = (idx - 24) * 8 + w;
    for (int id = pw * 64 + lane; id < 4 * 4 * 9 * 5 * 64; id += 512 * 64) pack_wo_task(wo, ws, id);
    for (int e = pw; e < D_; e += 512) pack_worow_task(wo, ws, e, lane);
    tlmark(18);
    if (u < K1_NGU) pack_gu_unit<PACK_ST>(wg, wu, ws, xcd * 9 + u % 9, u / 9, ldsb + w * 16384, lane);
    tlmark(19);
    return;
  }
  const int rt = (xcd & 3) * 4 + (idx & 3), cgl = idx >> 2;
  const int ch = xcd >> 2;
  const bool isq = cgl < 2;
  const int grp = isq ? ch * 2 + cgl : ch * 4 + (cgl - 2);
  auto wsrc_off = [&](int n) -> unsigned {
    const int c = n * 64 + lane, rho = c / 18, cc = c % 18;
    const int k = (rho & ~12) | ((rho & 4) << 1) | ((rho & 8) >> 1);
    const int col = isq ? 144 * grp + 8 * cc : (cc < 9 ? 576 + 72 * grp + 8 * cc : 1152 + 72 * grp + 8 * (cc - 9));
    return (unsigned)((k * 1728 + col) * 2);
  };
  const char* xsrc = (const char*)x + (size_t)(rt * 128 + w * 16) * 1152;
  const unsigned xoff0 = (lane >> 3) * 1152 + (((lane & 7) ^ (lane >> 4)) * 16);
  const unsigned xoff1 = ((lane >> 3) + 8) * 1152 + (((lane & 7) ^ (4 + (lane >> 4))) * 16);
#define K1_ROT (((idx & 3) * 2 + cgl) % 9)
  const int rot = K1_ROT;
  glds16(xsrc + rot * 128, xoff0, ldsb + K1_WST + w * 2048);
  glds16(xsrc + rot * 128, xoff1, ldsb + K1_WST + w * 2048 + 1024);
  const unsigned woffA = wsrc_off(w), woffB = wsrc_off(w + 8), woffC = wsrc_off(16 + (w & 1));
  auto issue = [&](auto SI, auto PI) {
    constexpr int st = decltype(SI)::value, slot = st % K1_R, pc = decltype(PI)::value;
    const int sa = st + rot >= 9 ? st + rot - 9 : st + rot;
    const char* wb = (const char*)wqkv + (size_t)sa * 64 * 3456;
    const unsigned sb = ldsb + slot * K1_STG;
    if constexpr (pc == 0) glds16(xsrc + sa * 128, xoff0, sb + K1_WST + w * 2048);
    if constexpr (pc == 1) glds16(xsrc + sa * 128, xoff1, sb + K1_WST + w * 2048 + 1024);
    if constexpr (pc == 2) { if (w < 2) glds16(wb, woffC, sb + (16 + w) * 1024); glds16(wb, woffA, sb + w * 1024); }
    if constexpr (pc == 3) glds16(wb, woffB, sb + (w + 8) * 1024);
  };
  issue(std::integral_constant<int, 0>{}, std::integral_constant<int, 2>{});
  issue(std::integral_constant<int, 0>{}, std::integral_constant<int, 3>{});
  bar();
  sfor<4>([&](auto PI) { issue(std::integral_constant<int, 1>{}, PI); });
  sfor<4>([&](auto PI) { issue(std::integral_constant<int, 2>{}, PI); });
  const int tg = w & 3;
  const int pslot = w == 5 ? idx : (w == 6 ? (idx < 21 ? 24 + idx : 99) : ((w == 7 && isq) ? 45 + idx : 99));
  const unsigned pl = ldsb + (w == 6 ? 2 * K1_STG : K1_STG + (w == 7 ? 18432 : 0));
  auto tail = [&](auto SI) {
    constexpr int st = decltype(SI)::value;
    if constexpr (st == 0) { if (w == 5) wait_vm<18>(); else if (pslot < 99 && w == 7) wait_vm<16>(); else wait_vm<0>(); }
    else if constexpr (st == 9) {
      wait_vm<0>();
      if (pslot < 36) pack_wd_store<PACK_ST>(ws, xcd * 18 + (pslot >> 1), pslot & 1, pl, lane);
      else if (pslot < 53) pack_gu_store<PACK_ST, false>(ws, xcd * 9 + (K1_NGU + pslot - 36) % 9, (K1_NGU + pslot - 36) / 9, pl, lane);
    } else if ((w == 6) == (st == 8)) {
      if (pslot < 36) pack_wd_load(wd, xcd * 18 + (pslot >> 1), pslot & 1, pl, lane);
      else if (pslot < 53) pack_gu_load(wg, wu, xcd * 9 + (K1_NGU + pslot - 36) % 9, (K1_NGU + pslot - 36) / 9, pl, lane);
    }
  };
  static_assert(K1_NGU == 64, "tail slot map");
  if (isq) { if (w < 4) k1_body<true, 0>(lds, ldsb, lane, tg, rt, grp, ws, issue, tail); else k1_body<true, 1>(lds, ldsb, lane, tg, rt, grp, ws, issue, tail); }
  else     { if (w < 4) k1_body<false, 0>(lds, ldsb, lane, tg, rt, grp, ws, issue, tail); else k1_body<false, 1>(lds, ldsb, lane, tg, rt, grp, ws, issue, tail); }
  tlmark(30);
  tail(std::integral_constant<int, 9>{});
}

constexpr int K1B_LDS = 4 * 18432;
__global__ __launch_bounds__(256) void k1b_reduce(const u16* __restrict__ wg, const u16* __restrict__ wu, const u16* __restrict__ wd, char* __restrict__ ws) {
  TLSCOPE(1);
  extern __shared__ __attribute__((aligned(16))) char lds[];
  const int tid = threadIdx.x;
  const int b = blockIdx.x;
  const int hh = b, s = tid >> 7, l = (tid >> 1) & 63, r = tid & 1;
  const int row = l & 31, h = l >> 5;
  const int T = (18 * hh) / 16 + s;
  const int d0 = 16 * T + 8 * r + 4 * h;
  const int G = hh >> 2, hl = hh & 3;
  int el = -1; bool isz = false;
  if (row < 16) el = row;
  else if (row == 18 || row == 22) isz = true;
  else if ((hh & 7) < 4 ? (row == 16 || row == 17) : (row == 20 || row == 21)) el = 16 + (row & 1);
  const int dlo = 18 * hh, dhi = dlo + 18;
  f32x4 sum = {};
  if ((el >= 0 || isz) && d0 + 3 >= dlo && d0 < dhi) {
    const int dk = d0 - 72 * G, a = dk >> 4, dr = dk & 15;
    int p, col;
    if (isz) { p = 11 + a; col = 0; }
    else {
      const int ev = 18 * hl + el;
      if (ev >= 64) { p = 11 + a; col = 8 + ev - 64; }
      else { const int vt = ev >> 4; col = ev & 15; p = a == 4 ? 10 : 3 * a + (vt - a); }
    }
    const char* src = ws + WS_KVP + ((size_t)G * 16 + p) * 1024 + ((dr >> 2) * 16 + col) * 16;
    f32x4 v[16];
#pragma unroll
    for (int i = 0; i < 16; ++i) v[i] = *(const f32x4*)(src + (size_t)i * 8 * 16 * 1024);
#pragma unroll
    for (int i = 0; i < 16; ++i) sum += v[i];
#pragma unroll
    for (int j = 0; j < 4; ++j) if (d0 + j < dlo || d0 + j >= dhi) sum[j] = 0.f;
  }
  *(u32x2*)(ws + WS_KVF + ((size_t)(hh * 2 + s) * 64 + l) * 16 + r * 8) = (u32x2){pk2(sum[0], sum[1]), pk2(sum[2], sum[3])};
}

#define K2_WIN 16
constexpr int K2_LDS = 4 * 19 * 1024;
__global__ __launch_bounds__(256, 1) void k2_attn_out(const u16* __restrict__ x, const char* __restrict__ ws_in, char* __restrict__ ws) {
  TLSCOPE(2);
  extern __shared__ __attribute__((aligned(16))) char lds[];
  const int tid = threadIdx.x, lane = tid & 63, w = uni(tid >> 6);
  const int b = blockIdx.x, xcd = b & 7, idx = b >> 3;
  const int tt = xcd * 8 + (idx & 7), ng = idx >> 3;
  const int tok = lane & 31, h = lane >> 5;
  const char* pqp = ws_in + WS_PQ + ((size_t)tt * 36 + w * 9) * 1024;
  const char* kvp = ws_in + WS_KVF + (size_t)w * 16 * 1024;
  const char* wop = ws_in + WS_WOP + (size_t)(ng * 4 + w) * 45 * 1024;
  const unsigned voff = lane * 16, voffc = (tok < 24 ? lane : (lane & 32)) * 16;
  u32x4 pq[9], kvf[16], wf[45];
  auto issue = [&](auto II) {
    constexpr int i = decltype(II)::value;
    if constexpr (i == 0) gload16<0>(pq[0], pqp, voff);
    else if constexpr (i < 70) {
      constexpr int u = (i - 1) / 8, k = (i - 1) % 8;
      if constexpr (u < 8 && k < 2) gload16<((2 * u + k) % 4) * 1024>(kvf[2 * u + k], kvp + ((2 * u + k) / 4) * 4096, voffc);
      else if constexpr (u < 8 && k == 2) gload16<((u + 1) % 4) * 1024>(pq[u + 1], pqp + ((u + 1) / 4) * 4096, voff);
      else { constexpr int n = u < 8 ? 5 * u + k - 3 : 40 + k; gload16<(n % 4) * 1024>(wf[n], wop + (n / 4) * 4096, n % 5 == 4 ? voffc : voff); }
    }
  };
  const int u0 = w == 0 ? 0 : 2 * w + 1;
  u32x4 xr[3]; unsigned mxb;
  sfor<4>([&](auto I) { issue(I); });
  {
    const char* xb0 = (const char*)x + (size_t)tt * 32 * 1152 + ng * 288 + u0 * 32;
    const unsigned xo = tok * 1152 + h * 16;
    gload16<0>(xr[0], xb0, xo); gload16<32>(xr[1], xb0, xo);
    gload16<0>(xr[2], xb0 + (w == 0 ? 64 : 0), xo);
    gload4<0>(mxb, ws_in + WS_MX + (size_t)tt * 128, tok * 4);
  }
  sfor<K2_WIN - 4>([&](auto I) { issue(std::integral_constant<int, 4 + decltype(I)::value>{}); });
  u32x4 ofr; unsigned lo[8];
  f32x16 acc[5], c;
  auto ostep = [&](auto UI) {
    constexpr int u = decltype(UI)::value;
    if constexpr (u == 0) pin(pq[0]);
    pin(kvf[2 * u]); pin(kvf[2 * u + 1]); pin(pq[u + 1]);
    c = mfma32(kvf[2 * u], pq[u], (f32x16){});
    c = mfma32(kvf[2 * u + 1], pq[u + 1], c);
  };
  wait_vm<K2_WIN - 4 + 4>();
  ostep(std::integral_constant<int, 0>{});
  sfor<9>([&](auto UI) {
    constexpr int u = decltype(UI)::value;
    if constexpr (u < 8) {
      const float r = __builtin_amdgcn_rcpf(c[10] + 1e-6f);
      ofr = (u32x4){pk2(c[0] * r, c[1] * r), pk2(c[2] * r, c[3] * r), pk2(c[4] * r, c[5] * r), pk2(c[6] * r, c[7] * r)};
      lo[u] = pk2(c[8] * r, c[9] * r);
    } else {
      sfor<4>([&](auto J) { constexpr int j = decltype(J)::value; unsigned a = lo[j], d = lo[4 + j]; pin(a); pin(d); ofr[j] = h ? d : a; });
    }
    sfor<8>([&](auto KI) { issue(std::integral_constant<int, K2_WIN + 8 * u + decltype(KI)::value>{}); });
    constexpr int issued = (K2_WIN + 8 * (u + 1)) < 70 ? (K2_WIN + 8 * (u + 1)) : 70;
    constexpr int need = u < 7 ? 8 * (u + 1) + 4 : (u == 7 ? 65 : 70);
    wait_vm<issued - need>();
    if constexpr (u < 7) ostep(std::integral_constant<int, u + 1>{});
    sfor<5>([&](auto NI) {
      constexpr int i = decltype(NI)::value, n = 5 * u + i;
      pin(wf[n]);
      acc[i] = mfma32(wf[n], ofr, u == 0 ? (f32x16){} : acc[i]);
    });
    sb0();
  });
#pragma unroll
  for (int i = 0; i < 3; ++i) pin(xr[i]);
  pin(mxb);
  const float mx = __uint_as_float(mxb);
  f32x4* xb = (f32x4*)lds;
#pragma unroll
  for (int q = 0; q < 19; ++q) {
    const int i = q >> 2, rb = (q & 3) * 4;
    xb[(w * 19 + q) * 64 + lane] = (f32x4){acc[i][rb], acc[i][rb + 1], acc[i][rb + 2], acc[i][rb + 3]};
  }
  tlmark(31);
#pragma unroll
  for (int i = 0; i < 3; ++i) {
    auto r0 = __builtin_amdgcn_permlane32_swap(xr[i][0], xr[i][2], false, false);
    auto r1 = __builtin_amdgcn_permlane32_swap(xr[i][1], xr[i][3], false, false);
    xr[i] = (u32x4){r0[0], r1[0], r0[1], r1[1]};
  }
  wait_lgkm0(); bar();
  float ms = 0.f;
#pragma unroll
  for (int k = 0; k < 4; ++k) { const f32x4 v = xb[(k * 19 + 18) * 64 + tok]; ms += v[0] + v[1]; }
  const float mean = mx + ms * (1.f / D_);
  if (ng == 0 && w == 0 && h == 0) ((float*)(ws + WS_MEAN))[tt * 32 + tok] = mean;
  float s1 = 0.f, s2 = 0.f;
#pragma unroll
  for (int i = 0; i < 3; ++i) {
    if (i < 2 || w == 0) {
      const int u = u0 + i;
      f32x4 a = {}, d = {};
#pragma unroll
      for (int k = 0; k < 4; ++k) { a += xb[(k * 19 + 2 * u) * 64 + lane]; d += xb[(k * 19 + 2 * u + 1) * 64 + lane]; }
      u32x4 o;
      o[0] = pk2(a[0] + bflo(xr[i][0]) - mean, a[1] + bfhi(xr[i][0]) - mean);
      o[1] = pk2(a[2] + bflo(xr[i][1]) - mean, a[3] + bfhi(xr[i][1]) - mean);
      o[2] = pk2(d[0] + bflo(xr[i][2]) - mean, d[1] + bfhi(xr[i][2]) - mean);
      o[3] = pk2(d[2] + bflo(xr[i][3]) - mean, d[3] + bfhi(xr[i][3]) - mean);
#pragma unroll
      for (int j = 0; j < 4; ++j) { const unsigned ov = o[j]; s1 = dot2_ones(ov, s1); s2 = __builtin_amdgcn_fdot2_f32_bf16(as_bf2(ov), as_bf2(ov), s2, false); }
      *(u32x4*)(ws + WS_HF + (((size_t)tt * 36 + ng * 9 + u) * 64 + lane) * 16) = o;
    }
  }
  s1 += __shfl_xor(s1, 32); s2 += __shfl_xor(s2, 32);
  if (h == 0) *(f32x2*)(ws + WS_HST + ((size_t)(tt * 32 + tok) * 16 + ng * 4 + w) * 8) = (f32x2){s1, s2};
}
}
extern "C" void kernel_launch(void* const* d_in, const int* in_sizes, int n_in, void* d_out, int out_size, void* d_ws, size_t ws_size, hipStream_t st) {
  using namespace fk;
  const u16* x = (const u16*)d_in[0]; const u16* wqkv = (const u16*)d_in[1]; const u16* wo = (const u16*)d_in[2];
  const u16* wg = (const u16*)d_in[4]; const u16* wu = (const u16*)d_in[5]; const u16* wd = (const u16*)d_in[6];
  char* ws = (char*)d_ws;
  (void)hipFuncSetAttribute((const void*)k1_qkv, hipFuncAttributeMaxDynamicSharedMemorySize, K1_LDS);
  (void)hipFuncSetAttribute((const void*)k1b_reduce, hipFuncAttributeMaxDynamicSharedMemorySize, K1B_LDS);
  (void)hipFuncSetAttribute((const void*)k2_attn_out, hipFuncAttributeMaxDynamicSharedMemorySize, K2_LDS);
  (void)hipFuncSetAttribute((const void*)k3_gateup, hipFuncAttributeMaxDynamicSharedMemorySize, K3_LDS);
  (void)hipFuncSetAttribute((const void*)k4_down, hipFuncAttributeMaxDynamicSharedMemorySize, K4_LDS);
  k1_qkv<<<256, 512, K1_LDS, st>>>(x, wqkv, wo, wg, wu, wd, ws);

  k1b_reduce<<<32, 256, 0, st>>>(wg, wu, wd, ws);
  k2_attn_out<<<256, 256, K2_LDS, st>>>(x, ws, ws);
  k3_gateup<<<256, 512, K3_LDS, st>>>(ws, ws);
  k4_down<<<256, 512, K4_LDS, st>>>(ws, ws, (u16*)d_out);
}
```

```cpp
#include <hip/hip_runtime.h>
#include <hip/hip_bf16.h>
#include <stdint.h>
#include <type_traits>
#include <utility>

namespace fk {
typedef unsigned short u16;
typedef short bf16x8 __attribute__((ext_vector_type(8)));
typedef short s16x4 __attribute__((ext_vector_type(4)));
typedef float f32x4 __attribute__((ext_vector_type(4)));
typedef float f32x2 __attribute__((ext_vector_type(2)));
typedef float f32x16 __attribute__((ext_vector_type(16)));
typedef unsigned u32x4 __attribute__((ext_vector_type(4)));
typedef unsigned u32x2 __attribute__((ext_vector_type(2)));
typedef __bf16 bf16x2v __attribute__((ext_vector_type(2)));
#define LAS __attribute__((address_space(3)))
#define DI __device__ __forceinline__

constexpr int S_ = 2048, D_ = 576, H_ = 32, DH_ = 18, F_ = 4608;

template <int N, class Fn, int... I> DI void sfor_impl(Fn&& f, std::integer_sequence<int, I...>) { (f(std::integral_constant<int, I>{}), ...); }
template <int N, class Fn> DI void sfor(Fn&& f) { sfor_impl<N>(f, std::make_integer_sequence<int, N>{}); }

template <int N> DI void wait_vm() { asm volatile("s_waitcnt vmcnt(%0)" ::"i"(N) : "memory"); }
DI void wait_lgkm0() { asm volatile("s_waitcnt lgkmcnt(0)" ::: "memory"); }
DI void bar() { asm volatile("" ::: "memory"); __builtin_amdgcn_s_barrier(); asm volatile("" ::: "memory"); }
template <class T> DI void pin(T& v) { asm volatile("" : "+v"(v)); }

DI void glds16(const void* sbase, unsigned voff, unsigned ldsdst) {
  asm volatile("s_mov_b32 m0, %2\n\ts_nop 0\n\tglobal_load_lds_dwordx4 %0, %1" :: "v"(voff), "s"(sbase), "s"(ldsdst) : "memory");
}
DI void glds16x2(const void* sbase, unsigned voff, unsigned ldsdst) {
  asm volatile("s_mov_b32 m0, %2\n\ts_nop 0\n\tglobal_load_lds_dwordx4 %0, %1\n\tglobal_load_lds_dwordx4 %0, %1 offset:1024" :: "v"(voff), "s"(sbase), "s"(ldsdst) : "memory");
}
DI void glds16v(const void* gptr, unsigned ldsdst) {
  asm volatile("s_mov_b32 m0, %1\n\ts_nop 0\n\tglobal_load_lds_dwordx4 %0, off" :: "v"(gptr), "s"(ldsdst) : "memory");
}
DI void sb0() { __builtin_amdgcn_sched_barrier(0); }
template <int OFF> DI void gload16(u32x4& dst, const void* sbase, unsigned voff) {
  asm volatile("global_load_dwordx4 %0, %1, %2 offset:%3" : "=v"(dst) : "v"(voff), "s"(sbase), "i"(OFF) : "memory");
}
template <int OFF> DI void gload8(u32x2& dst, const void* sbase, unsigned voff) {
  asm volatile("global_load_dwordx2 %0, %1, %2 offset:%3" : "=v"(dst) : "v"(voff), "s"(sbase), "i"(OFF) : "memory");
}
template <int OFF> DI void gload4(unsigned& dst, const void* sbase, unsigned voff) {
  asm volatile("global_load_dword %0, %1, %2 offset:%3" : "=v"(dst) : "v"(voff), "s"(sbase), "i"(OFF) : "memory");
}
template <int OFF> DI void gload16a(u32x4& dst, const void* sbase, unsigned voff) {
  asm volatile("global_load_dwordx4 %0, %1, %2 offset:%3" : "=a"(dst) : "v"(voff), "s"(sbase), "i"(OFF) : "memory");
}
DI void l2touch(const void* sbase, unsigned voff, unsigned ldsdummy) {
  asm volatile("s_mov_b32 m0, %2\n\ts_nop 0\n\tglobal_load_lds_dword %0, %1" :: "v"(voff), "s"(sbase), "s"(ldsdummy) : "memory");
}
template <class T> DI void pina(T& v) { asm volatile("" : "+a"(v)); }
template <int MODE> DI void store16(void* p, u32x4 v) {
  if constexpr (MODE == 0) *(u32x4*)p = v;
  else if constexpr (MODE == 1) __builtin_nontemporal_store(v, (u32x4*)p);
  else if constexpr (MODE == 2) asm volatile("global_store_dwordx4 %0, %1, off sc1\n\ts_nop 1" :: "v"(p), "v"(v) : "memory");
  else if constexpr (MODE == 3) asm volatile("global_store_dwordx4 %0, %1, off sc0 sc1\n\ts_nop 1" :: "v"(p), "v"(v) : "memory");
  else if constexpr (MODE == 4) asm volatile("global_store_dwordx4 %0, %1, off sc1 nt\n\ts_nop 1" :: "v"(p), "v"(v) : "memory");
  else asm volatile("" :: "v"(v));
}
DI float bf2f(u16 v) { return __uint_as_float(((unsigned)v) << 16); }
DI float bflo(unsigned v) { return __uint_as_float(v << 16); }
DI float bfhi(unsigned v) { return __uint_as_float(v & 0xffff0000u); }
DI unsigned pk2(float lo, float hi) {
  bf16x2v r; r[0] = (__bf16)lo; r[1] = (__bf16)hi; return __builtin_bit_cast(unsigned, r);
}
DI float rbf(float v) { return bflo(pk2(v, 0.f)); }
DI float ln_rstd(float var) { return __builtin_amdgcn_rcpf(rbf(__builtin_amdgcn_sqrtf(rbf(rbf(fmaxf(var, 0.f)) + 1e-5f)))); }
DI s16x4 tr_read(unsigned lds_addr) {
  return __builtin_amdgcn_ds_read_tr16_b64_v4i16((LAS s16x4*)(size_t)lds_addr);
}
DI f32x16 mfma32(u32x4 a, u32x4 b, f32x16 c) { return __builtin_amdgcn_mfma_f32_32x32x16_bf16(__builtin_bit_cast(bf16x8, a), __builtin_bit_cast(bf16x8, b), c, 0, 0, 0); }
DI f32x4 mfma16(u32x4 a, u32x4 b, f32x4 c) { return __builtin_amdgcn_mfma_f32_16x16x32_bf16(__builtin_bit_cast(bf16x8, a), __builtin_bit_cast(bf16x8, b), c, 0, 0, 0); }
#define TLSCOPE(k)
DI void tlmark(int) {}
DI void ckmark(int) {}
DI int uni(int v) { return __builtin_amdgcn_readfirstlane(v); }
}
namespace fk {
DI bf16x2v as_bf2(unsigned v) { return __builtin_bit_cast(bf16x2v, v); }
DI float dot2_ones(unsigned v, float c) { return __builtin_amdgcn_fdot2_f32_bf16(as_bf2(v), as_bf2(0x3f803f80u), c, false); }

constexpr size_t WS_HF   = 0;
constexpr size_t SZ_HF   = (size_t)S_ * D_ * 2;
constexpr size_t WS_HST  = WS_HF + SZ_HF;
constexpr size_t SZ_HST  = (size_t)S_ * 16 * 8;
constexpr size_t WS_WGU  = WS_HST + SZ_HST;
constexpr size_t SZ_WGU  = (size_t)288 * 36 * 1024;
constexpr int    WD_STREAM = 168;
constexpr size_t WS_WD   = WS_WGU + SZ_WGU + 16384;
constexpr size_t SZ_WD   = (size_t)32 * WD_STREAM * 1024;
constexpr size_t WS_ACT  = WS_WD + SZ_WD;
constexpr size_t SZ_ACT  = (size_t)S_ * F_ * 2;
constexpr size_t WS_PART = WS_ACT + SZ_ACT;
constexpr size_t SZ_PART = (size_t)4 * S_ * D_ * 4;
constexpr size_t WS_R2   = WS_PART + SZ_PART;
constexpr size_t WS_MEAN = WS_R2 + S_ * 4;
constexpr size_t WS_CNT  = WS_MEAN + S_ * 4;
constexpr size_t WS_END_FFN = WS_CNT + 512 * 128;
__host__ __device__ constexpr int k4_act(int v) { return v < 10 ? (v / 5) * 9 + (v % 5 < 4 ? v % 5 : 8) : ((v - 10) / 4) * 9 + 4 + (v - 10) % 4; }
DI int k4_virt(int ks) { const int j = ks / 9, p = ks % 9; return p < 4 ? j * 5 + p : (p == 8 ? j * 5 + 4 : 10 + j * 4 + p - 4); }

DI void pack_gu_load(const u16* wg, const u16* wu, int lc, int kp, unsigned ldsb, int lane) {
  const unsigned voff = (unsigned)(((kp * 64 + (lane >> 3)) * F_ + lc * 64 + (lane & 7) * 8) * 2);
  sfor<16>([&](auto I) {
    constexpr int i = decltype(I)::value, s = i >> 3, mat = (i >> 2) & 1, r8 = s * 4 + (i & 3);
    glds16((const char*)(mat ? wu : wg) + (size_t)r8 * 8 * F_ * 2, voff, ldsb + mat * 8192 + r8 * 1024);
  });
}
template <int ST, bool SPLITWAIT> DI void pack_gu_store(char* ws, int lc, int kp, unsigned ldsb, int lane) {
  const int g = lane >> 4;
  const unsigned rbase = ldsb + (16 * (g >> 1) + 4 * (g & 1) + ((lane >> 2) & 3)) * 128 + (lane & 3) * 8;
  char* dst = ws + WS_WGU + ((size_t)(lc * 4) * 18 + kp * 2) * 2048 + lane * 16;
  sfor<2>([&](auto I) {
    constexpr int s = decltype(I)::value;
    if constexpr (SPLITWAIT) wait_vm<8>();
    u32x4 v[8];
    sfor<8>([&](auto J) {
      constexpr int t = decltype(J)::value >> 1, mat = decltype(J)::value & 1;
      u32x2 a = __builtin_bit_cast(u32x2, tr_read(rbase + s * 4096 + mat * 8192 + t * 32)), b = __builtin_bit_cast(u32x2, tr_read(rbase + s * 4096 + mat * 8192 + t * 32 + 1024));
      v[decltype(J)::value] = (u32x4){a[0], a[1], b[0], b[1]};
    });
    sfor<8>([&](auto J) {
      constexpr int t = decltype(J)::value >> 1, mat = decltype(J)::value & 1;
      store16<ST>(dst + (t * 18 + s) * 2048 + mat * 1024, v[decltype(J)::value]);
    });
  });
}
template <int ST> DI void pack_gu_unit(const u16* wg, const u16* wu, char* ws, int lc, int kp, unsigned ldsb, int lane) {
  pack_gu_load(wg, wu, lc, kp, ldsb, lane); pack_gu_store<ST, true>(ws, lc, kp, ldsb, lane);
}
DI void pack_wd_load(const u16* wd, int S, int c2, unsigned ldsb, int lane) {
  const char* base = (const char*)wd + ((size_t)S * 32 * D_ + c2 * 288) * 2;
  sfor<9>([&](auto I) {
    constexpr int n = decltype(I)::value;
    const int c = n * 64 + lane, row = c / 36, cc = c % 36;
    const unsigned vo = (unsigned)(row * D_ * 2 + cc * 16);
    glds16(base, vo, ldsb + n * 1024);
    glds16(base + 16 * D_ * 2, vo, ldsb + 9216 + n * 1024);
  });
}
template <int ST> DI void pack_wd_store(char* ws, int S, int c2, unsigned ldsb, int lane) {
  const int g = lane >> 4, q = (lane >> 2) & 3, p = lane & 3;
  const unsigned rb = ldsb + (16 * (g >> 1) + 4 * (g & 1) + q) * 576;
  const int ksl = S / 36, kh = (S % 36) / 18, ks = S % 18;
  char* dst = ws + WS_WD + ((size_t)((c2 * 2 * 4 + ksl) * 2 + kh) * WD_STREAM + k4_virt(ks) * 9) * 1024 + lane * 16;
  u32x4 v[18];
  sfor<18>([&](auto I) {
    constexpr int ngl = decltype(I)::value / 9, t = decltype(I)::value % 9;
    const unsigned a0 = rb + ngl * 288 + (t < 8 ? 64 * (t >> 1) + 16 * p + 8 * (t & 1) : 256 + 8 * p);
    u32x2 a = __builtin_bit_cast(u32x2, tr_read(a0)), b = __builtin_bit_cast(u32x2, tr_read(a0 + 8 * 576));
    v[decltype(I)::value] = (u32x4){a[0], a[1], b[0], b[1]};
  });
  sfor<18>([&](auto I) {
    constexpr int ngl = decltype(I)::value / 9, t = decltype(I)::value % 9;
    store16<ST>(dst + (size_t)ngl * 8 * WD_STREAM * 1024 + t * 1024, v[decltype(I)::value]);
  });
}
template <int ST> DI void pack_wd_unit(const u16* wd, char* ws, int S, int c2, unsigned ldsb, int lane) {
  pack_wd_load(wd, S, c2, ldsb, lane); wait_vm<0>(); pack_wd_store<ST>(ws, S, c2, ldsb, lane);
}

DI u32x4 load16_sc1(const void* p) { u32x4 v; asm volatile("global_load_dwordx4 %0, %1, off sc1" : "=v"(v) : "v"(p) : "memory"); return v; }
DI u32x2 load8_sc1(const void* p) { u32x2 v; asm volatile("global_load_dwordx2 %0, %1, off sc1" : "=v"(v) : "v"(p) : "memory"); return v; }
DI unsigned load4_sc1(const void* p) { unsigned v; asm volatile("global_load_dword %0, %1, off sc1" : "=v"(v) : "v"(p) : "memory"); return v; }
DI void store8_sc1(void* p, u32x2 v) { asm volatile("global_store_dwordx2 %0, %1, off sc1\n\ts_nop 1" :: "v"(p), "v"(v) : "memory"); }

DI void sig_add(unsigned* p) {
  unsigned long long keep; unsigned one = 1u;
  asm volatile("s_mov_b64 %0, exec\n\ts_mov_b64 exec, 1\n\tglobal_atomic_add %1, %2, off\n\ts_mov_b64 exec, %0" : "=&s"(keep) : "v"(p), "v"(one) : "memory");
}
DI void spin_ge(unsigned& v, const unsigned* p, unsigned need) {
  unsigned t, n;
  asm volatile("s_mov_b32 %2, 0x400000\n\ts_nop 0\n"
               "1:\n\tv_readfirstlane_b32 %1, %0\n\ts_nop 3\n\ts_cmp_ge_u32 %1, %4\n\ts_cbranch_scc1 2f\n\t"
               "s_sub_u32 %2, %2, 1\n\ts_cbranch_scc1 2f\n\ts_sleep 1\n\t"
               "global_load_dword %0, %3, off sc1\n\ts_waitcnt vmcnt(0)\n\ts_branch 1b\n"
               "2:" : "+v"(v), "=&s"(t), "=&s"(n) : "v"(p), "s"(need) : "memory", "scc");
}
template <int OFF> DI void gload16_sc1(u32x4& dst, const void* sbase, unsigned voff) {
  asm volatile("global_load_dwordx4 %0, %1, %2 offset:%3 sc1" : "=v"(dst) : "v"(voff), "s"(sbase), "i"(OFF) : "memory");
}
constexpr int K3_LDS = 144 * 1024 + 512;
#define K3_P 4
constexpr int K3_Q = K3_P + 1;
DI void k3_wload(u32x4 (&dst)[4], const char* base, int s, int rot, unsigned voff) {
  const int sa = s + rot >= 9 ? s + rot - 9 : s + rot;
  sfor<4>([&](auto FI) { constexpr int f = decltype(FI)::value; gload16<f * 1024>(dst[f], base + sa * 4096, voff); });
}
template <int NTK, bool FIRST, bool NEXT, int QB, int NST, class HL>
DI void k3_unit(f32x4 (&acc)[2][8], u32x4 (&wq)[K3_Q][4], const char* cur, const char* nxt, const unsigned (&lb)[3], int rot, unsigned voff, HL&& hload) {
  constexpr int NG = NTK / 4;
  u32x4 af[2][4];
  auto aread = [&](auto II) {
    constexpr int it = decltype(II)::value, ks = it / NG, gp = it % NG;
#pragma unroll
    for (int tk = 0; tk < 4; ++tk) af[it & 1][tk] = *(const LAS u32x4*)(size_t)(lb[ks / 6] + ((ks % 6) * 8 + gp * 4 + tk) * 1024);
  };
  aread(std::integral_constant<int, 0>{});
  sfor<9>([&](auto SI) {
    constexpr int s = decltype(SI)::value;
    if constexpr (FIRST) {
      if constexpr (s == 1) { wait_vm<4>(); bar(); }
      if constexpr (s + 2 < 9) k3_wload(wq[(QB + s + 2) % K3_Q], cur, s + 2, rot, voff);
      if constexpr (s < 2) sfor<6>([&](auto HI) { hload(std::integral_constant<int, 6 + 6 * s + decltype(HI)::value>{}); });
      constexpr int n0 = s < 6 ? 0 : (K3_P * (s - 6)) / 3, n1 = s < 6 ? 0 : (K3_P * (s - 5)) / 3;
      constexpr int m0 = K3_P == 2 ? (s < 7 ? 0 : s - 7) : n0, m1 = K3_P == 2 ? (s < 7 ? 0 : s - 6) : n1;
      sfor<m1 - m0>([&](auto JI) { constexpr int j = m0 + decltype(JI)::value; static_assert(9 + j - K3_Q < s, "slot busy"); k3_wload(wq[(QB + 9 + j) % K3_Q], nxt, j, rot, voff); });
      if constexpr (s == 0) wait_vm<2 + 4 + 4 + 6>();
      else if constexpr (s >= 3) wait_vm<(s <= 6 ? 8 : (s == 7 ? 4 : 0)) + 4 * m1>();
      if constexpr (s == 3) bar();
    } else {
      if constexpr (s + K3_P < 9) k3_wload(wq[(QB + s + K3_P) % K3_Q], cur, s + K3_P, rot, voff);
      else if constexpr (NEXT) k3_wload(wq[(QB + s + K3_P) % K3_Q], nxt, s + K3_P - 9, rot, voff);
      constexpr int ny = NEXT ? K3_P : ((8 - s) < K3_P ? (8 - s) : K3_P);
      wait_vm<4 * ny + (s < K3_P ? NST : 0)>();
      if constexpr (!NEXT && s == K3_P) hload(std::integral_constant<int, 0>{});
    }
    sfor<4>([&](auto FI) { pin(wq[(QB + s) % K3_Q][decltype(FI)::value]); });
    sfor<2 * NG>([&](auto QI) {
      constexpr int q = decltype(QI)::value, it = s * 2 * NG + q, ks = it / NG, gp = it % NG, kq = ks & 1;
      if constexpr (it + 1 < 18 * NG) aread(std::integral_constant<int, it + 1>{});
      sb0();
#pragma unroll
      for (int f = 0; f < 2; ++f)
#pragma unroll
        for (int tk = 0; tk < 4; ++tk)
          acc[f][gp * 4 + tk] = mfma16(wq[(QB + s) % K3_Q][kq * 2 + f], af[it & 1][tk], ks == 0 ? (f32x4){} : acc[f][gp * 4 + tk]);
      sb0();
    });
  });
}

template <int HALF>
DI void k3_body(const char* __restrict__ ws_in, char* __restrict__ ws_out, char* lds, int w) {
  const int tid = threadIdx.x, lane = tid & 63;
  const int b = blockIdx.x, xcd = b & 7, idx = b >> 3;
  const int rt = (xcd & 1) * 8 + (idx & 7), fgp = (xcd >> 1) * 4 + (idx >> 3);
  const unsigned ldsb = (unsigned)(size_t)(LAS char*)lds;
  const unsigned voff = lane * 16;
  const int g = lane >> 4, l15 = lane & 15;
  const unsigned avoff = (g >> 1) * 1024 + (g & 1) * 512 + l15 * 16;
  const char* hsrc = ws_in + WS_HF + (size_t)(rt * 4 + (w >> 1)) * 36 * 1024 + (w & 1) * 256;
#define K3_ROT 0
  const int rot = K3_ROT;
  auto hload = [&](auto I) { constexpr int i = decltype(I)::value; const int ia = i + 2 * rot >= 18 ? i + 2 * rot - 18 : i + 2 * rot;
    glds16(hsrc + ia * 2048, avoff, ldsb + (i * 8 + w) * 1024); };
  auto nohl = [&](auto) {};
  const int t0 = w, t1 = w + 8, t2 = 16 + ((w >> 1) & 1);
  const char* wgu = ws_in + WS_WGU + (size_t)fgp * 18 * 36 * 1024;
  const char* sA = wgu + (size_t)t0 * 36 * 1024;
  const char* sB = wgu + (size_t)t1 * 36 * 1024;
  const char* sC = wgu + (size_t)t2 * 36 * 1024;
  u32x4 wq[K3_Q][4];
  u32x4 stv[2];
  {
    const char* sp = ws_in + WS_HST + (size_t)(rt * 128 + w * 16) * 128;
    gload16<0>(stv[0], sp, l15 * 128 + g * 32); gload16<16>(stv[1], sp, l15 * 128 + g * 32);
  }
  sfor<4>([&](auto I) { hload(I); });
  k3_wload(wq[0], sA, 0, rot, voff);
  hload(std::integral_constant<int, 4>{}); hload(std::integral_constant<int, 5>{});
  k3_wload(wq[1], sA, 1, rot, voff);
  wait_vm<10>();
  float* ctab = (float*)(lds + 144 * 1024);
  {
    pin(stv[0]); pin(stv[1]);
    float sm = (__uint_as_float(stv[0][0]) + __uint_as_float(stv[0][2])) + (__uint_as_float(stv[1][0]) + __uint_as_float(stv[1][2]));
    float sq = (__uint_as_float(stv[0][1]) + __uint_as_float(stv[0][3])) + (__uint_as_float(stv[1][1]) + __uint_as_float(stv[1][3]));
    sm += __shfl_xor(sm, 16); sq += __shfl_xor(sq, 16);
    sm += __shfl_xor(sm, 32); sq += __shfl_xor(sq, 32);
    const float mean = sm * (1.f / D_);
    const float rstd = ln_rstd(sq * (1.f / D_) - mean * mean);
    if (lane < 16) {
      ctab[w * 16 + lane] = -1.4426950408889634f * rstd;
    }
  }
  wait_lgkm0();
  bar();
  float c1[8];
#pragma unroll
  for (int tk = 0; tk < 8; ++tk) c1[tk] = ctab[tk * 16 + l15];
  tlmark(24); ckmark(0);
  unsigned lb[3] = {ldsb + voff, ldsb + voff + 49152, ldsb + voff + 98304};
  unsigned lh[3] = {lb[0] + (w & 1) * 4096, lb[1] + (w & 1) * 4096, lb[2] + (w & 1) * 4096};
  asm volatile("" : "+v"(lb[0]), "+v"(lb[1]), "+v"(lb[2]), "+v"(lh[0]), "+v"(lh[1]), "+v"(lh[2]));
  f32x4 acc[2][8];
  auto swiglu = [&](const f32x4& a, const f32x4& u, float c) -> u32x2 {
    float r[4];
#pragma unroll
    for (int j = 0; j < 4; ++j) { float e = __builtin_amdgcn_exp2f(a[j] * c); float pr = a[j] * u[j]; pin(pr); r[j] = pr * __builtin_amdgcn_rcpf(1.f + e); pin(r[j]); }
    return (u32x2){pk2(r[0], r[1]), pk2(r[2], r[3])};
  };
  char* actp = ws_out + WS_ACT + ((size_t)rt * 4 * 288 + fgp * 18) * 1024 + ((g & 1) * 32 + (g >> 1) * 16 + l15) * 16;
  auto epi2 = [&](int T, int tile32, const f32x4& g0, const f32x4& u0, float ca, const f32x4& g1, const f32x4& u1, float cb) {
    const u32x2 x0 = swiglu(g0, u0, ca), x1 = swiglu(g1, u1, cb);
    auto r0 = __builtin_amdgcn_permlane32_swap(x0[0], x1[0], false, false);
    auto r1 = __builtin_amdgcn_permlane32_swap(x0[1], x1[1], false, false);
    store16<2>(actp + ((size_t)tile32 * 288 + T) * 1024, (u32x4){r0[0], r1[0], r0[1], r1[1]});
  };
  auto epi_full = [&](int T) {
#pragma unroll
    for (int i = 0; i < 4; ++i) epi2(T, i, acc[0][2 * i], acc[1][2 * i], c1[2 * i], acc[0][2 * i + 1], acc[1][2 * i + 1], c1[2 * i + 1]);
  };
  k3_unit<8, true, true, 0, 0>(acc, wq, sA, HALF == 0 ? sC : sB, lb, rot, voff, hload);
  tlmark(25); ckmark(1);
  epi_full(t0);
  ckmark(2);
  if constexpr (HALF == 0) {
    k3_unit<4, false, true, 9 % K3_Q, 4>(acc, wq, sC, sB, lh, rot, voff, nohl);
    const bool hi = w & 1;
#pragma unroll
    for (int i = 0; i < 2; ++i) {
      float a0 = c1[2 * i], a1 = c1[4 + 2 * i], b0 = c1[2 * i + 1], b1 = c1[5 + 2 * i]; pin(a0); pin(a1); pin(b0); pin(b1);
      const float ca = hi ? a1 : a0, cb = hi ? b1 : b0;
      epi2(t2, (w & 1) * 2 + i, acc[0][2 * i], acc[1][2 * i], ca, acc[0][2 * i + 1], acc[1][2 * i + 1], cb);
    }
  }
  unsigned* cnt = (unsigned*)(ws_out + WS_CNT);
  auto sig1 = [&](auto) { sig_add(cnt + (256 + rt * 8 + (fgp >> 1)) * 32); };
  k3_unit<8, false, false, (HALF == 0 ? 18 : 9) % K3_Q, HALF == 0 ? 2 : 4>(acc, wq, sB, sB, lb, rot, voff, sig1);
  tlmark(29); ckmark(3);
  const unsigned* c1p = cnt + (256 + rt * 8 + (fgp >> 2) * 2 + HALF) * 32;
  unsigned pv = load4_sc1(c1p);
  epi_full(t1);
  wait_vm<4>();
  spin_ge(pv, c1p, 16u);
  ckmark(4);
}

#define K4_LA 4
#define K4_LW 4
constexpr int K4_R = 8, K4_D = 4;
constexpr int K4_LDS = K4_R * 16384;
constexpr int K4_NST = 21;
struct K4Sched {
  int aslot[18], sslot[K4_NST], posA[18], posS[K4_NST], cntBar[K4_NST], cntSlot[162];
  constexpr K4Sched() : aslot{}, sslot{}, posA{}, posS{}, cntBar{}, cntSlot{} {
    for (int a = 0; a < 18; ++a) aslot[a] = a < K4_LA ? -1 : 9 * (a - K4_LA) + 4;
    for (int s = 0; s < K4_NST; ++s) sslot[s] = s < K4_LW ? -1 : s - K4_LW;
    int c = 0;
    for (int s = 0; s < (K4_LW > K4_LA ? K4_LW : K4_LA); ++s) {
      if (s < K4_LW) { c += 2; posS[s] = c; }
      if (s < K4_LA) { c += 2; posA[s] = c; }
    }
    for (int n = -K4_D; n < 162; ++n) {
      if ((n + K4_D) % 8 == 0 && (n + K4_D) < 162) {
        int i = (n + K4_D) / 8; cntBar[i] = c;
        for (int s = 0; s < K4_NST; ++s) if (sslot[s] == i) { c += 2; posS[s] = c; }
      }
      if (n >= 0) {
        for (int q = 0; q < 18; ++q) if (aslot[q] == n) { c += 2; posA[q] = c; }
        cntSlot[n] = c;
      }
    }
  }
};
constexpr K4Sched k4s{};
#define K4_PISS 5
constexpr int K4_PCHK = 7;
static_assert(8 * K4_PCHK - K4_D <= k4s.aslot[10], "late activations must be requested behind the check");
DI void k4_body(const char* __restrict__ ws_in, char* __restrict__ ws_out, u16* __restrict__ out, char* lds, int w) {
  const int tid = threadIdx.x, lane = tid & 63;
  bar();
  tlmark(22);
  const int b = blockIdx.x, xcd = b & 7, idx = b >> 3;
  const int ksl = xcd >> 1, mt = (xcd & 1) * 8 + (idx & 7), ng = idx >> 3;
  const int tg = w & 3, kh = w >> 2;
  const int tt = mt * 4 + tg;
  const unsigned ldsb = (unsigned)(size_t)(LAS char*)lds;
  const char* wsrc = ws_in + WS_WD + (size_t)((ng * 4 + ksl) * 2 + kh) * WD_STREAM * 1024 + tg * 2048;
  const char* asrc = ws_in + WS_ACT + ((size_t)tt * 288 + ksl * 72 + kh * 36) * 1024;
  const unsigned voff = lane * 16;
  const int g = lane >> 4;
  const unsigned avoff0 = (g >> 1) * 1024 + (g & 1) * 512 + (lane & 15) * 16 + kh * 256, avoff1 = avoff0 ^ 256;
  const int th = w & 1, a = w >> 1, ott = mt * 4 + ksl, tok = th * 16 + (lane & 15);
  const int col = 32 * a + 8 * g, col8 = 128 + 4 * g;
  u32x2 h0, h1, h2; unsigned mub;
  {
    const char* hfp = ws_in + WS_HF + ((size_t)ott * 36 + ng * 9) * 1024;
    auto ho = [&](int c) -> unsigned { return (c >> 4) * 1024 + (((c >> 2) & 1) * 32 + tok) * 16 + ((c >> 3) & 1) * 8; };
    gload8<0>(h0, hfp, ho(col)); gload8<512>(h1, hfp, ho(col)); gload8<0>(h2, hfp, ho(col8));
    gload4<0>(mub, ws_in + WS_MEAN + (size_t)ott * 128, tok * 4);
  }
  u32x4 af[K4_LA + 1][2];
  auto issue_stage = [&](auto SI) {
    constexpr int s = decltype(SI)::value;
    glds16x2(wsrc + (size_t)s * 8192, voff, ldsb + (s % K4_R) * 16384 + kh * 8192 + tg * 2048);
  };
  auto issue_a = [&](auto AI) {
    constexpr int a = decltype(AI)::value;
    constexpr int ka = k4_act(a);
    gload16_sc1<(ka % 2) * 2048>(af[a % (K4_LA + 1)][0], asrc + (ka / 2) * 4096, avoff0);
    gload16_sc1<(ka % 2) * 2048>(af[a % (K4_LA + 1)][1], asrc + (ka / 2) * 4096, avoff1);
  };
  sfor<(K4_LW > K4_LA ? K4_LW : K4_LA)>([&](auto SI) {
    constexpr int s = decltype(SI)::value;
    if constexpr (s < K4_LW) issue_stage(SI);
    if constexpr (s < K4_LA) issue_a(SI);
    if constexpr (s == 0) bar();
  });
  f32x4 acc[9][2];
  u32x4 wf[K4_D + 1];
  unsigned* cntb = (unsigned*)(ws_out + WS_CNT);
  const unsigned* c2p = cntb + (384 + mt * 8 + ksl * 2 + kh) * 32;
  unsigned pv2 = 0;
  unsigned lrd0 = ldsb + kh * 8192 + lane * 16, lrd1 = lrd0 + 65536;
  unsigned xw = ldsb + w * 1024 + lane * 16, xw5 = xw + 5 * 16384;
  asm volatile("" : "+v"(lrd0), "+v"(lrd1), "+v"(xw), "+v"(xw5));
  auto xad = [&](unsigned lo, unsigned hi, int t) -> unsigned { return t < 6 ? hi + t * 8192 : lo + (t - 6) * 8192; };
  auto rd = [&](auto NI) {
    constexpr int n = decltype(NI)::value;
    if constexpr (n < 162) {
      constexpr int st = n / 8;
      if constexpr (n % 8 == 0) {
        wait_vm<k4s.cntBar[st] - k4s.posS[st]>();
        if constexpr (st == 0) {
          sig_add(cntb + (384 + mt * 8 + ksl * 2 + (ng >> 1)) * 32);
        }
        if constexpr (st == K4_PCHK) {
          wait_vm<k4s.cntBar[K4_PCHK] - k4s.cntBar[K4_PISS]>();
          spin_ge(pv2, c2p, 16u);
        }
        bar();
        if constexpr (st == 0) { tlmark(26); ckmark(5); }
        if constexpr (st == K4_PISS) pv2 = load4_sc1(c2p);
        sfor<K4_NST>([&](auto SI) { if constexpr (k4s.sslot[decltype(SI)::value] == st) issue_stage(SI); });
      }
      constexpr int off = (st % K4_R) * 16384 + (n % 8) * 1024;
      wf[n % (K4_D + 1)] = *(const LAS u32x4*)(size_t)((off < 65536 ? lrd0 : lrd1) + (off & 65535));
    }
  };
  sfor<K4_D>([&](auto I) { rd(I); });
  sfor<162>([&](auto NI) {
    constexpr int n = decltype(NI)::value;
    constexpr int ks = n / 9, t = n % 9, ab = ks % (K4_LA + 1);
    rd(std::integral_constant<int, n + K4_D>{});
    sfor<18>([&](auto AI) { if constexpr (k4s.aslot[decltype(AI)::value] == n) issue_a(AI); });
    if constexpr (t == 0) {
      wait_vm<k4s.cntSlot[n] - k4s.posA[ks]>();
      pin(af[ab][0]); pin(af[ab][1]);
    }
    acc[t][0] = mfma16(wf[n % (K4_D + 1)], af[ab][0], ks == 0 ? (f32x4){} : acc[t][0]);
    acc[t][1] = mfma16(wf[n % (K4_D + 1)], af[ab][1], ks == 0 ? (f32x4){} : acc[t][1]);
    if constexpr (n >= 155) *(LAS f32x4*)(size_t)xad(xw, xw5, n - 155) = acc[n - 155][1];
    sb0();
  });
  tlmark(27); ckmark(6);
  *(LAS f32x4*)(size_t)xad(xw, xw5, 7) = acc[7][1]; *(LAS f32x4*)(size_t)xad(xw, xw5, 8) = acc[8][1];
  wait_lgkm0(); bar();
  tlmark(20);
  const unsigned xr = ldsb + (w ^ 4) * 1024 + lane * 16, xr5 = xr + 5 * 16384;
  f32x4 v[9];
#pragma unroll
  for (int t = 0; t < 9; ++t) v[t] = *(const LAS f32x4*)(size_t)xad(xr, xr5, t) + acc[t][0];
  if (tg == ksl) {
#pragma unroll
    for (int t = 0; t < 9; ++t) *(LAS f32x4*)(size_t)xad(xr, xr5, t) = v[t];
  } else {
    char* pp = ws_out + WS_PART + ((((size_t)ksl * 64 + tt) * 4 + ng) * 2 + kh) * 5120;
#pragma unroll
    for (int i = 0; i < 4; ++i)
      store16<2>(pp + i * 1024 + lane * 16, (u32x4){pk2(v[2 * i][0], v[2 * i][1]), pk2(v[2 * i][2], v[2 * i][3]), pk2(v[2 * i + 1][0], v[2 * i + 1][1]), pk2(v[2 * i + 1][2], v[2 * i + 1][3])});
    store8_sc1(pp + 4096 + lane * 8, (u32x2){pk2(v[8][0], v[8][1]), pk2(v[8][2], v[8][3])});
    wait_vm<0>();
    if (lane == 0) __hip_atomic_fetch_add((unsigned*)(ws_out + WS_CNT) + ((mt * 4 + ng) * 4 + tg) * 32, 1u, __ATOMIC_RELAXED, __HIP_MEMORY_SCOPE_AGENT);
  }
  tlmark(21);
  if (w == ksl) {
    const unsigned* cnt = (const unsigned*)(ws_out + WS_CNT) + ((mt * 4 + ng) * 4 + ksl) * 32;
    unsigned spins = 0;
    while (__hip_atomic_load(cnt, __ATOMIC_RELAXED, __HIP_MEMORY_SCOPE_AGENT) < 6u) {
      __builtin_amdgcn_s_sleep(1);
      if (++spins > (1u << 22)) break;
    }
  }
  wait_lgkm0();
  bar();
  tlmark(28);
  const unsigned ox = ldsb + ((th ^ 1) * 4 + ksl) * 1024 + lane * 16, oa = ox + (a < 3 ? 5 * 16384 + a * 16384 : 0);
  f32x4 s0 = *(const LAS f32x4*)(size_t)oa, s1 = *(const LAS f32x4*)(size_t)(oa + 8192), s2 = *(const LAS f32x4*)(size_t)(ox + 16384);
  const char* pb = ws_in + WS_PART + (((size_t)ott * 4 + ng) * 2 + th) * 5120;
  constexpr size_t KS = (size_t)64 * 4 * 2 * 5120;
  u32x4 pv[3]; unsigned pw[3][2];
#pragma unroll
  for (int k = 0; k < 3; ++k) {
    const char* q = pb + (size_t)((ksl + 1 + k) & 3) * KS;
    pv[k] = load16_sc1(q + a * 1024 + lane * 16);
    const char* q8 = q + (a == 0 ? 4096 + lane * 8 : a * 1024);
    pw[k][0] = load4_sc1(q8); pw[k][1] = load4_sc1(q8 + 4);
  }
  wait_vm<0>();
#pragma unroll
  for (int k = 0; k < 3; ++k) { pin(pv[k]); pin(pw[k][0]); pin(pw[k][1]); }
#pragma unroll
  for (int k = 0; k < 3; ++k) {
    const unsigned a0 = pv[k][0], a1 = pv[k][1], a2 = pv[k][2], a3 = pv[k][3], b0 = pw[k][0], b1 = pw[k][1];
    s0 += (f32x4){bflo(a0), bfhi(a0), bflo(a1), bfhi(a1)}; s1 += (f32x4){bflo(a2), bfhi(a2), bflo(a3), bfhi(a3)}; s2 += (f32x4){bflo(b0), bfhi(b0), bflo(b1), bfhi(b1)};
  }
  pin(h0); pin(h1); pin(h2); pin(mub);
  const float rs = ((const float*)(lds + 144 * 1024))[ksl * 32 + tok] * 0.6931471805599453f, r2 = rs * rs, mu = __uint_as_float(mub);
  u16* orow = out + (size_t)(ott * 32 + tok) * D_ + ng * 144;
  s0 = s0 * r2 + mu; s1 = s1 * r2 + mu; s2 = s2 * r2 + mu;
  u32x4 o;
  o[0] = pk2(s0[0] + bflo(h0[0]), s0[1] + bfhi(h0[0])); o[1] = pk2(s0[2] + bflo(h0[1]), s0[3] + bfhi(h0[1]));
  o[2] = pk2(s1[0] + bflo(h1[0]), s1[1] + bfhi(h1[0])); o[3] = pk2(s1[2] + bflo(h1[1]), s1[3] + bfhi(h1[1]));
  *(u32x4*)(orow + col) = o;
  if (a == 0) *(u32x2*)(orow + col8) = (u32x2){pk2(s2[0] + bflo(h2[0]), s2[1] + bfhi(h2[0])), pk2(s2[2] + bflo(h2[1]), s2[3] + bfhi(h2[1]))};
}
__global__ __launch_bounds__(512, 2) void k34_ffn(const char* __restrict__ ws_in, char* __restrict__ ws_out, u16* __restrict__ out) {
  TLSCOPE(3);
  extern __shared__ __attribute__((aligned(16))) char lds[];
  const int w = uni(threadIdx.x >> 6);
  if (w < 4) k3_body<0>(ws_in, ws_out, lds, w); else k3_body<1>(ws_in, ws_out, lds, w);
  k4_body(ws_in, ws_out, out, lds, w);
}
}
#define OUT_ST 2
#define OUT2_ST 0
#define GU_ST 2
#define PACK_ST 2
namespace fk {
constexpr size_t WS_PQ   = (WS_END_FFN + 4095) / 4096 * 4096;
constexpr size_t WS_KVP  = WS_PQ + SZ_HF;
constexpr size_t SZ_KVP  = (size_t)16 * 8 * 16 * 1024;
constexpr size_t WS_KVF  = WS_KVP + SZ_KVP;
constexpr size_t SZ_KVF  = 64 * 1024;
constexpr size_t WS_WOP  = WS_KVF + SZ_KVF;
constexpr size_t SZ_WOP  = (size_t)4 * 4 * 9 * 5 * 1024;
constexpr size_t WS_MX   = WS_WOP + SZ_WOP;
constexpr size_t WS_END  = WS_MX + S_ * 4;

DI float phi_f(float t) { return (t > 0.f ? t + 1.f : __builtin_amdgcn_exp2f(t * 1.4426950408889634f)) + 1e-6f; }

DI void pack_wo_task(const u16* wo, char* ws, int id) {
  const int lane = id & 63, fi = id >> 6, i = fi % 5, u = (fi / 5) % 9, w = (fi / 45) & 3, ng = fi / 180;
  const int row = lane & 31, h = lane >> 5;
  if (i == 4 && row >= 16) {
    if (row >= 18) *(u32x4*)(ws + WS_WOP + (size_t)id * 16) = (u32x4){0u, 0u, 0u, 0u};
    return;
  }
  const int n = ng * 144 + i * 32 + row;
  unsigned v[8];
#pragma unroll
  for (int j = 0; j < 8; ++j) {
    const int e = u < 8 ? 18 * (8 * w + u) + 8 * (j >> 2) + 4 * h + (j & 3) : 18 * (8 * w + 4 * h + (j >> 1)) + 16 + (j & 1);
    v[j] = wo[e * D_ + n];
  }
  store16<OUT_ST>(ws + WS_WOP + (size_t)id * 16, (u32x4){v[0] | (v[1] << 16), v[2] | (v[3] << 16), v[4] | (v[5] << 16), v[6] | (v[7] << 16)});
}
DI void pack_worow_task(const u16* wo, char* ws, int e, int lane) {
  float s = 0.f;
#pragma unroll
  for (int i = 0; i < 9; ++i) s += bf2f(wo[e * D_ + i * 64 + lane]);
#pragma unroll
  for (int o = 32; o; o >>= 1) s += __shfl_xor(s, o);
  const unsigned hi = pk2(s, 0.f) & 0xffffu; const unsigned lo = pk2(s - bflo(hi), 0.f) & 0xffffu;
  const int hh = e / 18, el = e % 18, w = hh >> 3, ul = hh & 7;
  int u, h, j;
  if (el < 16) { u = ul; h = (el >> 2) & 1; j = (el >> 3) * 4 + (el & 3); } else { u = 8; h = ul >> 2; j = (ul & 3) * 2 + (el - 16); }
  if (lane < 8) {
    const int ng = lane >> 1, part = lane & 1;
    u16* p = (u16*)(ws + WS_WOP) + ((((size_t)(ng * 4 + w) * 9 + u) * 5 + 4) * 64 + h * 32 + 16 + part) * 8 + j;
    *p = (u16)(part ? lo : hi);
  }
}

constexpr int K1_WST = 18432, K1_STG = K1_WST + 16384, K1_R = 4;
constexpr int K1_TAB = K1_STG * K1_R;
constexpr int K1_LDS = 8 * 18432;
static_assert(K1_TAB + 4 * 144 * 4 + 1024 <= K1_LDS, "lds");
#define K1_NGU 64
#define K1_NOPACK 0
#define K1_DD 3
constexpr int K1_D = K1_DD;
DI float phi3(float u, float rs, float rl) { return fmaxf(fmaf(u, rs, 1e-6f), 1e-6f) + __builtin_amdgcn_exp2f(fminf(u * rl, 0.f)); }
template <bool ISQ, int NH> constexpr int k1_tile(int j) {
  if (ISQ) return NH ? 5 + j : j;
  if (NH == 0) return j < 2 ? j : (j == 2 ? 4 : j + 2);
  return j < 2 ? 2 + j : 5 + j;
}
template <bool ISQ> constexpr int k1_toff(int t) { return ISQ ? 32 * t : (t < 4 ? 32 * t : 144 + 32 * (t - 5)); }

__host__ __device__ constexpr int k1_img(int t) { return t < 8 ? t * 4096 : 3 * K1_STG + (t - 8) * 4096; }
template <bool ISQ, int NH, class ISSUE, class TAIL>
DI void k1_body(char* lds, unsigned ldsb, int lane, int tg, int rt, int grp, char* ws, ISSUE&& issue, TAIL&& tail) {
  constexpr int NT = NH ? 4 : 5, NWF = NT + K1_D;
  const int g = lane >> 4, l15 = lane & 15, q4 = (lane >> 2) & 3, p4 = lane & 3;
  const unsigned rrow = ldsb + (16 * (g >> 1) + 4 * (g & 1) + q4) * 288;
  unsigned rbA[2], rbB[2], rbX[2][2];
  rbA[0] = rrow + p4 * 8;
  rbB[0] = rrow + (p4 < 2 ? 128 + 8 * p4 : 272 + 8 * (p4 - 2));
  rbA[1] = rbA[0] + 2 * K1_STG; rbB[1] = rbB[0] + 2 * K1_STG;
  const unsigned xb = ldsb + K1_WST + tg * 4096 + l15 * 128;
#pragma unroll
  for (int kh = 0; kh < 2; ++kh) { rbX[kh][0] = xb + (((4 * kh + g) ^ ((l15 >> 1) & 7)) * 16); rbX[kh][1] = rbX[kh][0] + 2 * K1_STG; }
  asm volatile("" : "+v"(rbA[0]), "+v"(rbA[1]), "+v"(rbB[0]), "+v"(rbB[1]), "+v"(rbX[0][0]), "+v"(rbX[0][1]), "+v"(rbX[1][0]), "+v"(rbX[1][1]));
  u32x4 ones = {0x3f803f80u, 0x3f803f80u, 0x3f803f80u, 0x3f803f80u};
  pin(ones);
  f32x4 acc[NT][2], accs[NT], sst[2][2];
#pragma unroll
  for (int t = 0; t < NT; ++t) accs[t] = (f32x4){};
  u32x4 xf[2][2], wf[NWF];
  auto rd = [&](auto NI) {
    constexpr int n = decltype(NI)::value;
    if constexpr (n < 18 * NT) {
      constexpr int ks = n / NT, j = n % NT, st = ks >> 1, kh = ks & 1, slot = st % K1_R, hb = slot >> 1, so = (slot & 1) * K1_STG;
      constexpr int t = k1_tile<ISQ, NH>(j);
      constexpr int pos = n % (2 * NT);
      constexpr int nxt = st == 0 ? 3 : (st >= 2 && st + 2 < 9 ? st + 2 : -1);
      if constexpr (pos == 0) {
        constexpr int upto = st == 0 ? 2 : (st + 1 > 3 ? (st + 1 < 8 ? st + 1 : 8) : 3);
        if constexpr (NH == 1 && st == 8) tail(std::integral_constant<int, 0>{});
        else wait_vm<4 * (upto - st)>();
        bar();
        if constexpr (st == 0) tlmark(17);
        if constexpr (NH == 1 && st >= 7) tail(std::integral_constant<int, st>{});
      }
      if constexpr (j == 0) {
        xf[kh][0] = *(const LAS u32x4*)(size_t)(rbX[kh][hb] + so);
        xf[kh][1] = *(const LAS u32x4*)(size_t)(rbX[kh][hb] + so + 2048);
      }
      const unsigned a = ((!ISQ && t == 4) ? rbB[hb] : rbA[hb] + k1_toff<ISQ>(t)) + so + kh * 9216;
      u32x2 lo = __builtin_bit_cast(u32x2, tr_read(a)), hi = __builtin_bit_cast(u32x2, tr_read(a + 8 * 288));
      wf[n % NWF] = (u32x4){lo[0], lo[1], hi[0], hi[1]};
      if constexpr (nxt >= 0 && (pos & 1) == 1 && pos / 2 < 4) issue(std::integral_constant<int, nxt>{}, std::integral_constant<int, pos / 2>{});
    }
  };
  sfor<K1_D>([&](auto I) { rd(I); });
  sfor<18 * NT>([&](auto NI) {
    constexpr int n = decltype(NI)::value, ks = n / NT, j = n % NT, kh = ks & 1;
    rd(std::integral_constant<int, n + K1_D>{});
    const u32x4 wv = wf[n % NWF];
    sfor<2>([&](auto HI) {
      constexpr int th = decltype(HI)::value;
      const f32x4 c = ks == 0 ? (f32x4){} : acc[j][th];
      acc[j][th] = ISQ ? mfma16(wv, xf[kh][th], c) : mfma16(xf[kh][th], wv, c);
    });
    if constexpr (NH == 1 && j < 2) {
      sst[j][0] = mfma16(ones, xf[kh][j], ks == 0 ? (f32x4){} : sst[j][0]);
      sst[j][1] = mfma16(xf[kh][j], xf[kh][j], ks == 0 ? (f32x4){} : sst[j][1]);
    }
    if constexpr (j == NT - 1) {
      if (tg == (ks & 3)) {
        sfor<NT>([&](auto JI) { constexpr int jj = decltype(JI)::value; accs[jj] = mfma16(ones, wf[(ks * NT + jj) % NWF], accs[jj]); });
      }
    }
    sb0();
  });
  tlmark(16);
  float* tab = (float*)(lds + K1_TAB);
  float* stt = tab + 4 * 144;
  if (g == 0) {
    sfor<NT>([&](auto JI) { constexpr int jj = decltype(JI)::value; tab[tg * 144 + k1_tile<ISQ, NH>(jj) * 16 + l15] = accs[jj][0]; });
  }
  if constexpr (NH == 1) {
    if (g == (l15 >> 2)) {
#pragma unroll
      for (int th = 0; th < 2; ++th) {
        const int r = l15 & 3;
        const float q = r == 0 ? sst[th][1][0] : (r == 1 ? sst[th][1][1] : (r == 2 ? sst[th][1][2] : sst[th][1][3]));
        const float mean = sst[th][0][0] * (1.f / D_);
        stt[tg * 32 + th * 16 + l15] = mean;
        stt[128 + tg * 32 + th * 16 + l15] = ln_rstd(q * (1.f / D_) - mean * mean);
      }
    }
  }
  wait_lgkm0(); bar();
  constexpr float L2E = 1.4426950408889634f;
  if constexpr (ISQ) {
    const int tt = rt * 4 + tg;
    float nm[2], rs[2], rl[2];
#pragma unroll
    for (int th = 0; th < 2; ++th) { nm[th] = -stt[tg * 32 + th * 16 + l15]; rs[th] = stt[128 + tg * 32 + th * 16 + l15]; rl[th] = rs[th] * L2E; }
    if (NH == 0 && grp == 0 && g < 2) ((float*)(ws + WS_MX))[tt * 32 + g * 16 + l15] = g ? -nm[1] : -nm[0];
    char* pq = ws + WS_PQ + ((size_t)tt * 36 + grp * 9) * 1024 + ((g & 1) * 32 + (g >> 1) * 16 + l15) * 16;
    sfor<NT>([&](auto JI) {
      constexpr int j = decltype(JI)::value, t = k1_tile<ISQ, NH>(j);
      const float* cp = tab + t * 16 + g * 4;
      const f32x4 cs = (*(const f32x4*)cp + *(const f32x4*)(cp + 144)) + (*(const f32x4*)(cp + 288) + *(const f32x4*)(cp + 432));
      unsigned pk[2][2];
#pragma unroll
      for (int th = 0; th < 2; ++th) {
        float v[4];
#pragma unroll
        for (int e = 0; e < 4; ++e) v[e] = phi3(fmaf(nm[th], cs[e], acc[j][th][e]), rs[th], rl[th]);
        pk[th][0] = pk2(v[0], v[1]); pk[th][1] = pk2(v[2], v[3]);
      }
      auto r0 = __builtin_amdgcn_permlane32_swap(pk[0][0], pk[1][0], false, false);
      auto r1 = __builtin_amdgcn_permlane32_swap(pk[0][1], pk[1][1], false, false);
      store16<OUT_ST>(pq + t * 1024, (u32x4){r0[0], r1[0], r0[1], r1[1]});
    });
  } else {
    f32x4 mn[2], rs[2], rl[2];
#pragma unroll
    for (int th = 0; th < 2; ++th) { mn[th] = *(const f32x4*)(stt + tg * 32 + th * 16 + g * 4); rs[th] = *(const f32x4*)(stt + 128 + tg * 32 + th * 16 + g * 4); rl[th] = rs[th] * L2E; }
    char* ob = lds + (tg * 64 + lane) * 16;
    sfor<NT>([&](auto JI) {
      constexpr int j = decltype(JI)::value, t = k1_tile<ISQ, NH>(j);
      const float* cp = tab + t * 16 + l15;
      const float ncs = -((cp[0] + cp[144]) + (cp[288] + cp[432]));
      u32x4 pv;
#pragma unroll
      for (int th = 0; th < 2; ++th) {
        float v[4];
#pragma unroll
        for (int e = 0; e < 4; ++e) {
          const float u = fmaf(mn[th][e], ncs, acc[j][th][e]);
          if (t < 4) v[e] = phi3(u, rs[th][e], rl[th][e]);
          else if (t > 4) v[e] = u * rs[th][e];
          else v[e] = l15 < 8 ? phi3(u, rs[th][e], rl[th][e]) : u * rs[th][e];
        }
        pv[2 * th] = pk2(v[0], v[1]); pv[2 * th + 1] = pk2(v[2], v[3]);
      }
      *(u32x4*)(ob + k1_img(t)) = pv;
      if constexpr (t == 4) *(u32x4*)(ob + k1_img(9)) = l15 < 8 ? ones : pv;
    });
    wait_lgkm0(); bar();

    const int w = NH * 4 + tg;
#pragma unroll
    for (int pi = 0; pi < 2; ++pi) {
      const int p = 2 * w + pi;
      int a, bt;
      if (p < 10) { a = (p + 1) / 3; bt = 5 + a + ((p + 1) % 3) - 1; } else if (p == 10) { a = 4; bt = 8; } else { a = p - 11; bt = 9; }
      f32x4 c = {};
#pragma unroll
      for (int i = 0; i < 4; ++i) {
        const u32x4 av = *(const u32x4*)(lds + a * 4096 + (i * 64 + lane) * 16);
        const u32x4 bv = *(const u32x4*)(lds + k1_img(bt) + (i * 64 + lane) * 16);
        c = mfma16(av, bv, c);
      }
      if constexpr (OUT_ST != 0) asm volatile("s_nop 9" : "+v"(c));
      store16<OUT_ST>(ws + WS_KVP + (((size_t)rt * 8 + grp) * 16 + p) * 1024 + lane * 16, __builtin_bit_cast(u32x4, c));
    }
  }
}

__global__ __launch_bounds__(512, 2) void k1_qkv(const u16* __restrict__ x, const u16* __restrict__ wqkv, const u16* __restrict__ wo,
                                                  const u16* __restrict__ wg, const u16* __restrict__ wu, const u16* __restrict__ wd, char* __restrict__ ws) {
  TLSCOPE(0);
  extern __shared__ __attribute__((aligned(16))) char lds[];
  const int tid = threadIdx.x, lane = tid & 63, w = uni(tid >> 6);
  const int b = blockIdx.x, xcd = b & 7, idx = b >> 3;
  const unsigned ldsb = (unsigned)(size_t)(LAS char*)lds;
  if (idx >= 24) {
    if (K1_NOPACK) return;
    const int pw = ((idx - 24) * 8 + xcd) * 8 + w;
    if (pw < 8) ((unsigned*)(ws + WS_CNT))[(pw * 64 + lane) * 32] = 0u;
    const int u = (idx - 24) * 8 + w;
    for (int id = pw * 64 + lane; id < 4 * 4 * 9 * 5 * 64; id += 512 * 64) pack_wo_task(wo, ws, id);
    for (int e = pw; e < D_; e += 512) pack_worow_task(wo, ws, e, lane);
    tlmark(18);
    if (u < K1_NGU) pack_gu_unit<GU_ST>(wg, wu, ws, xcd * 9 + u % 9, u / 9, ldsb + w * 16384, lane);
    tlmark(19);
    return;
  }
  const int rt = (xcd & 3) * 4 + (idx & 3), cgl = idx >> 2;
  const int ch = xcd >> 2;
  const bool isq = cgl < 2;
  const int grp = isq ? ch * 2 + cgl : ch * 4 + (cgl - 2);
  auto wsrc_off = [&](int n) -> unsigned {
    const int c = n * 64 + lane, rho = c / 18, cc = c % 18;
    const int k = (rho & ~12) | ((rho & 4) << 1) | ((rho & 8) >> 1);
    const int col = isq ? 144 * grp + 8 * cc : (cc < 9 ? 576 + 72 * grp + 8 * cc : 1152 + 72 * grp + 8 * (cc - 9));
    return (unsigned)((k * 1728 + col) * 2);
  };
  const char* xsrc = (const char*)x + (size_t)(rt * 128 + w * 16) * 1152;
  const unsigned xoff0 = (lane >> 3) * 1152 + (((lane & 7) ^ (lane >> 4)) * 16);
  const unsigned xoff1 = ((lane >> 3) + 8) * 1152 + (((lane & 7) ^ (4 + (lane >> 4))) * 16);
#define K1_ROT (((idx & 3) * 2 + cgl) % 9)
  const int rot = K1_ROT;
  glds16(xsrc + rot * 128, xoff0, ldsb + K1_WST + w * 2048);
  glds16(xsrc + rot * 128, xoff1, ldsb + K1_WST + w * 2048 + 1024);
  const unsigned woffA = wsrc_off(w), woffB = wsrc_off(w + 8), woffC = wsrc_off(16 + (w & 1));
  auto issue = [&](auto SI, auto PI) {
    constexpr int st = decltype(SI)::value, slot = st % K1_R, pc = decltype(PI)::value;
    const int sa = st + rot >= 9 ? st + rot - 9 : st + rot;
    const char* wb = (const char*)wqkv + (size_t)sa * 64 * 3456;
    const unsigned sb = ldsb + slot * K1_STG;
    if constexpr (pc == 0) glds16(xsrc + sa * 128, xoff0, sb + K1_WST + w * 2048);
    if constexpr (pc == 1) glds16(xsrc + sa * 128, xoff1, sb + K1_WST + w * 2048 + 1024);
    if constexpr (pc == 2) { if (w < 2) glds16(wb, woffC, sb + (16 + w) * 1024); glds16(wb, woffA, sb + w * 1024); }
    if constexpr (pc == 3) glds16(wb, woffB, sb + (w + 8) * 1024);
  };
  issue(std::integral_constant<int, 0>{}, std::integral_constant<int, 2>{});
  issue(std::integral_constant<int, 0>{}, std::integral_constant<int, 3>{});
  bar();
  sfor<4>([&](auto PI) { issue(std::integral_constant<int, 1>{}, PI); });
  sfor<4>([&](auto PI) { issue(std::integral_constant<int, 2>{}, PI); });
  const int tg = w & 3;
  const int pslot = w == 5 ? idx : (w == 6 ? (idx < 21 ? 24 + idx : 99) : ((w == 7 && isq) ? 45 + idx : 99));
  const unsigned pl = ldsb + (w == 6 ? 2 * K1_STG : K1_STG + (w == 7 ? 18432 : 0));
  auto tail = [&](auto SI) {
    constexpr int st = decltype(SI)::value;
    if constexpr (st == 0) { if (w == 5) wait_vm<18>(); else if (pslot < 99 && w == 7) wait_vm<16>(); else wait_vm<0>(); }
    else if constexpr (st == 9) {
      wait_vm<0>();
      if (pslot < 36) pack_wd_store<PACK_ST>(ws, xcd * 18 + (pslot >> 1), pslot & 1, pl, lane);
      else if (pslot < 53) pack_gu_store<GU_ST, false>(ws, xcd * 9 + (K1_NGU + pslot - 36) % 9, (K1_NGU + pslot - 36) / 9, pl, lane);
    } else if ((w == 6) == (st == 8)) {
      if (pslot < 36) pack_wd_load(wd, xcd * 18 + (pslot >> 1), pslot & 1, pl, lane);
      else if (pslot < 53) pack_gu_load(wg, wu, xcd * 9 + (K1_NGU + pslot - 36) % 9, (K1_NGU + pslot - 36) / 9, pl, lane);
    }
  };
  static_assert(K1_NGU == 64, "tail slot map");
  if (isq) { if (w < 4) k1_body<true, 0>(lds, ldsb, lane, tg, rt, grp, ws, issue, tail); else k1_body<true, 1>(lds, ldsb, lane, tg, rt, grp, ws, issue, tail); }
  else     { if (w < 4) k1_body<false, 0>(lds, ldsb, lane, tg, rt, grp, ws, issue, tail); else k1_body<false, 1>(lds, ldsb, lane, tg, rt, grp, ws, issue, tail); }
  tlmark(30);
  tail(std::integral_constant<int, 9>{});
}

constexpr int K1B_LDS = 4 * 18432;
__global__ __launch_bounds__(256) void k1b_reduce(const u16* __restrict__ wg, const u16* __restrict__ wu, const u16* __restrict__ wd, char* __restrict__ ws) {
  TLSCOPE(1);
  extern __shared__ __attribute__((aligned(16))) char lds[];
  const int tid = threadIdx.x;
  const int b = blockIdx.x;
  const int hh = b, s = tid >> 7, l = (tid >> 1) & 63, r = tid & 1;
  const int row = l & 31, h = l >> 5;
  const int T = (18 * hh) / 16 + s;
  const int d0 = 16 * T + 8 * r + 4 * h;
  const int G = hh >> 2, hl = hh & 3;
  int el = -1; bool isz = false;
  if (row < 16) el = row;
  else if (row == 18 || row == 22) isz = true;
  else if ((hh & 7) < 4 ? (row == 16 || row == 17) : (row == 20 || row == 21)) el = 16 + (row & 1);
  const int dlo = 18 * hh, dhi = dlo + 18;
  f32x4 sum = {};
  if ((el >= 0 || isz) && d0 + 3 >= dlo && d0 < dhi) {
    const int dk = d0 - 72 * G, a = dk >> 4, dr = dk & 15;
    int p, col;
    if (isz) { p = 11 + a; col = 0; }
    else {
      const int ev = 18 * hl + el;
      if (ev >= 64) { p = 11 + a; col = 8 + ev - 64; }
      else { const int vt = ev >> 4; col = ev & 15; p = a == 4 ? 10 : 3 * a + (vt - a); }
    }
    const char* src = ws + WS_KVP + ((size_t)G * 16 + p) * 1024 + ((dr >> 2) * 16 + col) * 16;
    f32x4 v[16];
#pragma unroll
    for (int i = 0; i < 16; ++i) v[i] = *(const f32x4*)(src + (size_t)i * 8 * 16 * 1024);
#pragma unroll
    for (int i = 0; i < 16; ++i) sum += v[i];
#pragma unroll
    for (int j = 0; j < 4; ++j) if (d0 + j < dlo || d0 + j >= dhi) sum[j] = 0.f;
  }
  *(u32x2*)(ws + WS_KVF + ((size_t)(hh * 2 + s) * 64 + l) * 16 + r * 8) = (u32x2){pk2(sum[0], sum[1]), pk2(sum[2], sum[3])};
}

#define K2_WIN 16
constexpr int K2_LDS = 4 * 19 * 1024;
__global__ __launch_bounds__(256, 1) void k2_attn_out(const u16* __restrict__ x, const char* __restrict__ ws_in, char* __restrict__ ws) {
  TLSCOPE(2);
  extern __shared__ __attribute__((aligned(16))) char lds[];
  const int tid = threadIdx.x, lane = tid & 63, w = uni(tid >> 6);
  const int b = blockIdx.x, xcd = b & 7, idx = b >> 3;
  const int tt = xcd * 8 + (idx & 7), ng = idx >> 3;
  const int tok = lane & 31, h = lane >> 5;
  const char* pqp = ws_in + WS_PQ + ((size_t)tt * 36 + w * 9) * 1024;
  const char* kvp = ws_in + WS_KVF + (size_t)w * 16 * 1024;
  const char* wop = ws_in + WS_WOP + (size_t)(ng * 4 + w) * 45 * 1024;
  const unsigned voff = lane * 16, voffc = (tok < 24 ? lane : (lane & 32)) * 16;
  u32x4 pq[9], kvf[16], wf[45];
  auto issue = [&](auto II) {
    constexpr int i = decltype(II)::value;
    if constexpr (i == 0) gload16<0>(pq[0], pqp, voff);
    else if constexpr (i < 70) {
      constexpr int u = (i - 1) / 8, k = (i - 1) % 8;
      if constexpr (u < 8 && k < 2) gload16<((2 * u + k) % 4) * 1024>(kvf[2 * u + k], kvp + ((2 * u + k) / 4) * 4096, voffc);
      else if constexpr (u < 8 && k == 2) gload16<((u + 1) % 4) * 1024>(pq[u + 1], pqp + ((u + 1) / 4) * 4096, voff);
      else { constexpr int n = u < 8 ? 5 * u + k - 3 : 40 + k; gload16<(n % 4) * 1024>(wf[n], wop + (n / 4) * 4096, n % 5 == 4 ? voffc : voff); }
    }
  };
  const int u0 = w == 0 ? 0 : 2 * w + 1;
  u32x4 xr[3]; unsigned mxb;
  sfor<4>([&](auto I) { issue(I); });
  {
    const char* xb0 = (const char*)x + (size_t)tt * 32 * 1152 + ng * 288 + u0 * 32;
    const unsigned xo = tok * 1152 + h * 16;
    gload16<0>(xr[0], xb0, xo); gload16<32>(xr[1], xb0, xo);
    gload16<0>(xr[2], xb0 + (w == 0 ? 64 : 0), xo);
    gload4<0>(mxb, ws_in + WS_MX + (size_t)tt * 128, tok * 4);
  }
  sfor<K2_WIN - 4>([&](auto I) { issue(std::integral_constant<int, 4 + decltype(I)::value>{}); });
  u32x4 ofr; unsigned lo[8];
  f32x16 acc[5], c;
  auto ostep = [&](auto UI) {
    constexpr int u = decltype(UI)::value;
    if constexpr (u == 0) pin(pq[0]);
    pin(kvf[2 * u]); pin(kvf[2 * u + 1]); pin(pq[u + 1]);
    c = mfma32(kvf[2 * u], pq[u], (f32x16){});
    c = mfma32(kvf[2 * u + 1], pq[u + 1], c);
  };
  wait_vm<K2_WIN - 4 + 4>();
  ostep(std::integral_constant<int, 0>{});
  sfor<9>([&](auto UI) {
    constexpr int u = decltype(UI)::value;
    if constexpr (u < 8) {
      const float r = __builtin_amdgcn_rcpf(c[10] + 1e-6f);
      ofr = (u32x4){pk2(c[0] * r, c[1] * r), pk2(c[2] * r, c[3] * r), pk2(c[4] * r, c[5] * r), pk2(c[6] * r, c[7] * r)};
      lo[u] = pk2(c[8] * r, c[9] * r);
    } else {
      sfor<4>([&](auto J) { constexpr int j = decltype(J)::value; unsigned a = lo[j], d = lo[4 + j]; pin(a); pin(d); ofr[j] = h ? d : a; });
    }
    sfor<8>([&](auto KI) { issue(std::integral_constant<int, K2_WIN + 8 * u + decltype(KI)::value>{}); });
    constexpr int issued = (K2_WIN + 8 * (u + 1)) < 70 ? (K2_WIN + 8 * (u + 1)) : 70;
    constexpr int need = u < 7 ? 8 * (u + 1) + 4 : (u == 7 ? 65 : 70);
    wait_vm<issued - need>();
    if constexpr (u < 7) ostep(std::integral_constant<int, u + 1>{});
    sfor<5>([&](auto NI) {
      constexpr int i = decltype(NI)::value, n = 5 * u + i;
      pin(wf[n]);
      acc[i] = mfma32(wf[n], ofr, u == 0 ? (f32x16){} : acc[i]);
    });
    sb0();
  });
#pragma unroll
  for (int i = 0; i < 3; ++i) pin(xr[i]);
  pin(mxb);
  const float mx = __uint_as_float(mxb);
  f32x4* xb = (f32x4*)lds;
#pragma unroll
  for (int q = 0; q < 19; ++q) {
    const int i = q >> 2, rb = (q & 3) * 4;
    xb[(w * 19 + q) * 64 + lane] = (f32x4){acc[i][rb], acc[i][rb + 1], acc[i][rb + 2], acc[i][rb + 3]};
  }
  tlmark(31);
#pragma unroll
  for (int i = 0; i < 3; ++i) {
    auto r0 = __builtin_amdgcn_permlane32_swap(xr[i][0], xr[i][2], false, false);
    auto r1 = __builtin_amdgcn_permlane32_swap(xr[i][1], xr[i][3], false, false);
    xr[i] = (u32x4){r0[0], r1[0], r0[1], r1[1]};
  }
  wait_lgkm0(); bar();
  float ms = 0.f;
#pragma unroll
  for (int k = 0; k < 4; ++k) { const f32x4 v = xb[(k * 19 + 18) * 64 + tok]; ms += v[0] + v[1]; }
  const float mean = mx + ms * (1.f / D_);
  if (ng == 0 && w == 0 && h == 0) ((float*)(ws + WS_MEAN))[tt * 32 + tok] = mean;
  float s1 = 0.f, s2 = 0.f;
#pragma unroll
  for (int i = 0; i < 3; ++i) {
    if (i < 2 || w == 0) {
      const int u = u0 + i;
      f32x4 a = {}, d = {};
#pragma unroll
      for (int k = 0; k < 4; ++k) { a += xb[(k * 19 + 2 * u) * 64 + lane]; d += xb[(k * 19 + 2 * u + 1) * 64 + lane]; }
      u32x4 o;
      o[0] = pk2(a[0] + bflo(xr[i][0]) - mean, a[1] + bfhi(xr[i][0]) - mean);
      o[1] = pk2(a[2] + bflo(xr[i][1]) - mean, a[3] + bfhi(xr[i][1]) - mean);
      o[2] = pk2(d[0] + bflo(xr[i][2]) - mean, d[1] + bfhi(xr[i][2]) - mean);
      o[3] = pk2(d[2] + bflo(xr[i][3]) - mean, d[3] + bfhi(xr[i][3]) - mean);
#pragma unroll
      for (int j = 0; j < 4; ++j) { const unsigned ov = o[j]; s1 = dot2_ones(ov, s1); s2 = __builtin_amdgcn_fdot2_f32_bf16(as_bf2(ov), as_bf2(ov), s2, false); }
      store16<OUT2_ST>(ws + WS_HF + (((size_t)tt * 36 + ng * 9 + u) * 64 + lane) * 16, o);
    }
  }
  s1 += __shfl_xor(s1, 32); s2 += __shfl_xor(s2, 32);
  if (h == 0) *(f32x2*)(ws + WS_HST + ((size_t)(tt * 32 + tok) * 16 + ng * 4 + w) * 8) = (f32x2){s1, s2};
}
}
extern "C" void kernel_launch(void* const* d_in, const int* in_sizes, int n_in, void* d_out, int out_size, void* d_ws, size_t ws_size, hipStream_t st) {
  using namespace fk;
  const u16* x = (const u16*)d_in[0]; const u16* wqkv = (const u16*)d_in[1]; const u16* wo = (const u16*)d_in[2];
  const u16* wg = (const u16*)d_in[4]; const u16* wu = (const u16*)d_in[5]; const u16* wd = (const u16*)d_in[6];
  char* ws = (char*)d_ws;
  (void)hipFuncSetAttribute((const void*)k1_qkv, hipFuncAttributeMaxDynamicSharedMemorySize, K1_LDS);
  (void)hipFuncSetAttribute((const void*)k1b_reduce, hipFuncAttributeMaxDynamicSharedMemorySize, K1B_LDS);
  (void)hipFuncSetAttribute((const void*)k2_attn_out, hipFuncAttributeMaxDynamicSharedMemorySize, K2_LDS);
  (void)hipFuncSetAttribute((const void*)k34_ffn, hipFuncAttributeMaxDynamicSharedMemorySize, K3_LDS);
  k1_qkv<<<256, 512, K1_LDS, st>>>(x, wqkv, wo, wg, wu, wd, ws);

  k1b_reduce<<<32, 256, 0, st>>>(wg, wu, wd, ws);
  k2_attn_out<<<256, 256, K2_LDS, st>>>(x, ws, ws);
  k34_ffn<<<256, 512, K3_LDS, st>>>(ws, ws, (u16*)d_out);
}
```

```cpp
#include <hip/hip_runtime.h>
#include <hip/hip_bf16.h>
#include <stdint.h>
#include <type_traits>
#include <utility>

namespace fk {
typedef unsigned short u16;
typedef short bf16x8 __attribute__((ext_vector_type(8)));
typedef short s16x4 __attribute__((ext_vector_type(4)));
typedef float f32x4 __attribute__((ext_vector_type(4)));
typedef float f32x2 __attribute__((ext_vector_type(2)));
typedef float f32x16 __attribute__((ext_vector_type(16)));
typedef unsigned u32x4 __attribute__((ext_vector_type(4)));
typedef unsigned u32x2 __attribute__((ext_vector_type(2)));
typedef __bf16 bf16x2v __attribute__((ext_vector_type(2)));
#define LAS __attribute__((address_space(3)))
#define DI __device__ __forceinline__

constexpr int S_ = 2048, D_ = 576, H_ = 32, DH_ = 18, F_ = 4608;

template <int N, class Fn, int... I> DI void sfor_impl(Fn&& f, std::integer_sequence<int, I...>) { (f(std::integral_constant<int, I>{}), ...); }
template <int N, class Fn> DI void sfor(Fn&& f) { sfor_impl<N>(f, std::make_integer_sequence<int, N>{}); }

template <int N> DI void wait_vm() { asm volatile("s_waitcnt vmcnt(%0)" ::"i"(N) : "memory"); }
DI void wait_lgkm0() { asm volatile("s_waitcnt lgkmcnt(0)" ::: "memory"); }
DI void bar() { asm volatile("" ::: "memory"); __builtin_amdgcn_s_barrier(); asm volatile("" ::: "memory"); }
template <class T> DI void pin(T& v) { asm volatile("" : "+v"(v)); }

DI void glds16(const void* sbase, unsigned voff, unsigned ldsdst) {
  asm volatile("s_mov_b32 m0, %2\n\ts_nop 0\n\tglobal_load_lds_dwordx4 %0, %1" :: "v"(voff), "s"(sbase), "s"(ldsdst) : "memory");
}
DI void glds16x2(const void* sbase, unsigned voff, unsigned ldsdst) {
  asm volatile("s_mov_b32 m0, %2\n\ts_nop 0\n\tglobal_load_lds_dwordx4 %0, %1\n\tglobal_load_lds_dwordx4 %0, %1 offset:1024" :: "v"(voff), "s"(sbase), "s"(ldsdst) : "memory");
}
DI void glds16v(const void* gptr, unsigned ldsdst) {
  asm volatile("s_mov_b32 m0, %1\n\ts_nop 0\n\tglobal_load_lds_dwordx4 %0, off" :: "v"(gptr), "s"(ldsdst) : "memory");
}
DI void sb0() { __builtin_amdgcn_sched_barrier(0); }
template <int OFF> DI void gload16(u32x4& dst, const void* sbase, unsigned voff) {
  asm volatile("global_load_dwordx4 %0, %1, %2 offset:%3" : "=v"(dst) : "v"(voff), "s"(sbase), "i"(OFF) : "memory");
}
template <int OFF> DI void gload8(u32x2& dst, const void* sbase, unsigned voff) {
  asm volatile("global_load_dwordx2 %0, %1, %2 offset:%3" : "=v"(dst) : "v"(voff), "s"(sbase), "i"(OFF) : "memory");
}
template <int OFF> DI void gload4(unsigned& dst, const void* sbase, unsigned voff) {
  asm volatile("global_load_dword %0, %1, %2 offset:%3" : "=v"(dst) : "v"(voff), "s"(sbase), "i"(OFF) : "memory");
}
template <int OFF> DI void gload16a(u32x4& dst, const void* sbase, unsigned voff) {
  asm volatile("global_load_dwordx4 %0, %1, %2 offset:%3" : "=a"(dst) : "v"(voff), "s"(sbase), "i"(OFF) : "memory");
}
DI void l2touch(const void* sbase, unsigned voff, unsigned ldsdummy) {
  asm volatile("s_mov_b32 m0, %2\n\ts_nop 0\n\tglobal_load_lds_dword %0, %1" :: "v"(voff), "s"(sbase), "s"(ldsdummy) : "memory");
}
DI void text_range(const char*& base, unsigned& len) {
  const char* pc; int d0, d1;
  asm volatile("s_getpc_b64 %0\n.Lfkpc%=:\n\ts_mov_b32 %1, .Lfk_text_begin-.Lfkpc%=\n\ts_mov_b32 %2, .Lfk_text_end-.Lfkpc%=" : "=s"(pc), "=s"(d0), "=s"(d1));
  base = pc + d0; len = (unsigned)(d1 - d0);
}
template <class T> DI void pina(T& v) { asm volatile("" : "+a"(v)); }
template <int MODE> DI void store16(void* p, u32x4 v) {
  if constexpr (MODE == 0) *(u32x4*)p = v;
  else if constexpr (MODE == 1) __builtin_nontemporal_store(v, (u32x4*)p);
  else if constexpr (MODE == 2) asm volatile("global_store_dwordx4 %0, %1, off sc1\n\ts_nop 1" :: "v"(p), "v"(v) : "memory");
  else if constexpr (MODE == 3) asm volatile("global_store_dwordx4 %0, %1, off sc0 sc1\n\ts_nop 1" :: "v"(p), "v"(v) : "memory");
  else if constexpr (MODE == 4) asm volatile("global_store_dwordx4 %0, %1, off sc1 nt\n\ts_nop 1" :: "v"(p), "v"(v) : "memory");
  else asm volatile("" :: "v"(v));
}
DI float bf2f(u16 v) { return __uint_as_float(((unsigned)v) << 16); }
DI float bflo(unsigned v) { return __uint_as_float(v << 16); }
DI float bfhi(unsigned v) { return __uint_as_float(v & 0xffff0000u); }
DI unsigned pk2(float lo, float hi) {
  bf16x2v r; r[0] = (__bf16)lo; r[1] = (__bf16)hi; return __builtin_bit_cast(unsigned, r);
}
DI float rbf(float v) { return bflo(pk2(v, 0.f)); }
DI float ln_rstd(float var) { return __builtin_amdgcn_rcpf(rbf(__builtin_amdgcn_sqrtf(rbf(rbf(fmaxf(var, 0.f)) + 1e-5f)))); }
DI s16x4 tr_read(unsigned lds_addr) {
  return __builtin_amdgcn_ds_read_tr16_b64_v4i16((LAS s16x4*)(size_t)lds_addr);
}
DI f32x16 mfma32(u32x4 a, u32x4 b, f32x16 c) { return __builtin_amdgcn_mfma_f32_32x32x16_bf16(__builtin_bit_cast(bf16x8, a), __builtin_bit_cast(bf16x8, b), c, 0, 0, 0); }
DI f32x4 mfma16(u32x4 a, u32x4 b, f32x4 c) { return __builtin_amdgcn_mfma_f32_16x16x32_bf16(__builtin_bit_cast(bf16x8, a), __builtin_bit_cast(bf16x8, b), c, 0, 0, 0); }
#define TLSCOPE(k)
DI void tlmark(int) {}
DI void ckmark(int) {}
DI int uni(int v) { return __builtin_amdgcn_readfirstlane(v); }
}
namespace fk {
DI bf16x2v as_bf2(unsigned v) { return __builtin_bit_cast(bf16x2v, v); }
DI float dot2_ones(unsigned v, float c) { return __builtin_amdgcn_fdot2_f32_bf16(as_bf2(v), as_bf2(0x3f803f80u), c, false); }

constexpr size_t WS_HF   = 0;
constexpr size_t SZ_HF   = (size_t)S_ * D_ * 2;
constexpr size_t WS_HST  = WS_HF + SZ_HF;
constexpr size_t SZ_HST  = (size_t)S_ * 16 * 8;
constexpr size_t WS_WGU  = WS_HST + SZ_HST;
constexpr size_t SZ_WGU  = (size_t)288 * 36 * 1024;
constexpr int    WD_STREAM = 168;
constexpr size_t WS_WD   = WS_WGU + SZ_WGU + 16384;
constexpr size_t SZ_WD   = (size_t)32 * WD_STREAM * 1024;
constexpr size_t WS_ACT  = WS_WD + SZ_WD;
constexpr size_t SZ_ACT  = (size_t)S_ * F_ * 2;
constexpr size_t WS_PART = WS_ACT + SZ_ACT;
constexpr size_t SZ_PART = (size_t)4 * S_ * D_ * 4;
constexpr size_t WS_R2   = WS_PART + SZ_PART;
constexpr size_t WS_MEAN = WS_R2 + S_ * 4;
constexpr size_t WS_CNT  = WS_MEAN + S_ * 4;
constexpr size_t WS_END_FFN = WS_CNT + 512 * 128;
__host__ __device__ constexpr int k4_act(int v) { return v < 10 ? (v / 5) * 9 + (v % 5 < 4 ? v % 5 : 8) : ((v - 10) / 4) * 9 + 4 + (v - 10) % 4; }
DI int k4_virt(int ks) { const int j = ks / 9, p = ks % 9; return p < 4 ? j * 5 + p : (p == 8 ? j * 5 + 4 : 10 + j * 4 + p - 4); }

DI void pack_gu_load(const u16* wg, const u16* wu, int lc, int kp, unsigned ldsb, int lane) {
  const unsigned voff = (unsigned)(((kp * 64 + (lane >> 3)) * F_ + lc * 64 + (lane & 7) * 8) * 2);
  sfor<16>([&](auto I) {
    constexpr int i = decltype(I)::value, s = i >> 3, mat = (i >> 2) & 1, r8 = s * 4 + (i & 3);
    glds16((const char*)(mat ? wu : wg) + (size_t)r8 * 8 * F_ * 2, voff, ldsb + mat * 8192 + r8 * 1024);
  });
}
template <int ST, bool SPLITWAIT> DI void pack_gu_store(char* ws, int lc, int kp, unsigned ldsb, int lane) {
  const int g = lane >> 4;
  const unsigned rbase = ldsb + (16 * (g >> 1) + 4 * (g & 1) + ((lane >> 2) & 3)) * 128 + (lane & 3) * 8;
  char* dst = ws + WS_WGU + ((size_t)(lc * 4) * 18 + kp * 2) * 2048 + lane * 16;
  sfor<2>([&](auto I) {
    constexpr int s = decltype(I)::value;
    if constexpr (SPLITWAIT) wait_vm<8>();
    u32x4 v[8];
    sfor<8>([&](auto J) {
      constexpr int t = decltype(J)::value >> 1, mat = decltype(J)::value & 1;
      u32x2 a = __builtin_bit_cast(u32x2, tr_read(rbase + s * 4096 + mat * 8192 + t * 32)), b = __builtin_bit_cast(u32x2, tr_read(rbase + s * 4096 + mat * 8192 + t * 32 + 1024));
      v[decltype(J)::value] = (u32x4){a[0], a[1], b[0], b[1]};
    });
    sfor<8>([&](auto J) {
      constexpr int t = decltype(J)::value >> 1, mat = decltype(J)::value & 1;
      store16<ST>(dst + (t * 18 + s) * 2048 + mat * 1024, v[decltype(J)::value]);
    });
  });
}
template <int ST> DI void pack_gu_unit(const u16* wg, const u16* wu, char* ws, int lc, int kp, unsigned ldsb, int lane) {
  pack_gu_load(wg, wu, lc, kp, ldsb, lane); pack_gu_store<ST, true>(ws, lc, kp, ldsb, lane);
}
DI void pack_wd_load(const u16* wd, int S, int c2, unsigned ldsb, int lane) {
  const char* base = (const char*)wd + ((size_t)S * 32 * D_ + c2 * 288) * 2;
  sfor<9>([&](auto I) {
    constexpr int n = decltype(I)::value;
    const int c = n * 64 + lane, row = c / 36, cc = c % 36;
    const unsigned vo = (unsigned)(row * D_ * 2 + cc * 16);
    glds16(base, vo, ldsb + n * 1024);
    glds16(base + 16 * D_ * 2, vo, ldsb + 9216 + n * 1024);
  });
}
template <int ST> DI void pack_wd_store(char* ws, int S, int c2, unsigned ldsb, int lane) {
  const int g = lane >> 4, q = (lane >> 2) & 3, p = lane & 3;
  const unsigned rb = ldsb + (16 * (g >> 1) + 4 * (g & 1) + q) * 576;
  const int ksl = S / 36, kh = (S % 36) / 18, ks = S % 18;
  char* dst = ws + WS_WD + ((size_t)((c2 * 2 * 4 + ksl) * 2 + kh) * WD_STREAM + k4_virt(ks) * 9) * 1024 + lane * 16;
  u32x4 v[18];
  sfor<18>([&](auto I) {
    constexpr int ngl = decltype(I)::value / 9, t = decltype(I)::value % 9;
    const unsigned a0 = rb + ngl * 288 + (t < 8 ? 64 * (t >> 1) + 16 * p + 8 * (t & 1) : 256 + 8 * p);
    u32x2 a = __builtin_bit_cast(u32x2, tr_read(a0)), b = __builtin_bit_cast(u32x2, tr_read(a0 + 8 * 576));
    v[decltype(I)::value] = (u32x4){a[0], a[1], b[0], b[1]};
  });
  sfor<18>([&](auto I) {
    constexpr int ngl = decltype(I)::value / 9, t = decltype(I)::value % 9;
    store16<ST>(dst + (size_t)ngl * 8 * WD_STREAM * 1024 + t * 1024, v[decltype(I)::value]);
  });
}
template <int ST> DI void pack_wd_unit(const u16* wd, char* ws, int S, int c2, unsigned ldsb, int lane) {
  pack_wd_load(wd, S, c2, ldsb, lane); wait_vm<0>(); pack_wd_store<ST>(ws, S, c2, ldsb, lane);
}

DI u32x4 load16_sc1(const void* p) { u32x4 v; asm volatile("global_load_dwordx4 %0, %1, off sc1" : "=v"(v) : "v"(p) : "memory"); return v; }
DI u32x2 load8_sc1(const void* p) { u32x2 v; asm volatile("global_load_dwordx2 %0, %1, off sc1" : "=v"(v) : "v"(p) : "memory"); return v; }
DI unsigned load4_sc1(const void* p) { unsigned v; asm volatile("global_load_dword %0, %1, off sc1" : "=v"(v) : "v"(p) : "memory"); return v; }
DI void store8_sc1(void* p, u32x2 v) { asm volatile("global_store_dwordx2 %0, %1, off sc1\n\ts_nop 1" :: "v"(p), "v"(v) : "memory"); }

DI void sig_add(unsigned* p) {
  unsigned long long keep; unsigned one = 1u;
  asm volatile("s_mov_b64 %0, exec\n\ts_mov_b64 exec, 1\n\tglobal_atomic_add %1, %2, off\n\ts_mov_b64 exec, %0" : "=&s"(keep) : "v"(p), "v"(one) : "memory");
}
DI void spin_ge(unsigned& v, const unsigned* p, unsigned need) {
  unsigned t, n;
  asm volatile("s_mov_b32 %2, 0x400000\n\ts_nop 0\n"
               "1:\n\tv_readfirstlane_b32 %1, %0\n\ts_nop 3\n\ts_cmp_ge_u32 %1, %4\n\ts_cbranch_scc1 2f\n\t"
               "s_sub_u32 %2, %2, 1\n\ts_cbranch_scc1 2f\n\ts_sleep 1\n\t"
               "global_load_dword %0, %3, off sc1\n\ts_waitcnt vmcnt(0)\n\ts_branch 1b\n"
               "2:" : "+v"(v), "=&s"(t), "=&s"(n) : "v"(p), "s"(need) : "memory", "scc");
}
template <int OFF> DI void gload16_sc1(u32x4& dst, const void* sbase, unsigned voff) {
  asm volatile("global_load_dwordx4 %0, %1, %2 offset:%3 sc1" : "=v"(dst) : "v"(voff), "s"(sbase), "i"(OFF) : "memory");
}
constexpr int K3_LDS = 144 * 1024 + 512;
#define K3_P 4
constexpr int K3_Q = K3_P + 1;
DI void k3_wload(u32x4 (&dst)[4], const char* base, int s, int rot, unsigned voff) {
  const int sa = s + rot >= 9 ? s + rot - 9 : s + rot;
  sfor<4>([&](auto FI) { constexpr int f = decltype(FI)::value; gload16<f * 1024>(dst[f], base + sa * 4096, voff); });
}
template <int NTK, bool FIRST, bool NEXT, int QB, int NST, class HL>
DI void k3_unit(f32x4 (&acc)[2][8], u32x4 (&wq)[K3_Q][4], const char* cur, const char* nxt, const unsigned (&lb)[3], int rot, unsigned voff, HL&& hload) {
  constexpr int NG = NTK / 4;
  u32x4 af[2][4];
  auto aread = [&](auto II) {
    constexpr int it = decltype(II)::value, ks = it / NG, gp = it % NG;
#pragma unroll
    for (int tk = 0; tk < 4; ++tk) af[it & 1][tk] = *(const LAS u32x4*)(size_t)(lb[ks / 6] + ((ks % 6) * 8 + gp * 4 + tk) * 1024);
  };
  aread(std::integral_constant<int, 0>{});
  sfor<9>([&](auto SI) {
    constexpr int s = decltype(SI)::value;
    if constexpr (FIRST) {
      if constexpr (s == 1) { wait_vm<4>(); bar(); }
      if constexpr (s + 2 < 9) k3_wload(wq[(QB + s + 2) % K3_Q], cur, s + 2, rot, voff);
      if constexpr (s < 2) sfor<6>([&](auto HI) { hload(std::integral_constant<int, 6 + 6 * s + decltype(HI)::value>{}); });
      constexpr int n0 = s < 6 ? 0 : (K3_P * (s - 6)) / 3, n1 = s < 6 ? 0 : (K3_P * (s - 5)) / 3;
      constexpr int m0 = K3_P == 2 ? (s < 7 ? 0 : s - 7) : n0, m1 = K3_P == 2 ? (s < 7 ? 0 : s - 6) : n1;
      sfor<m1 - m0>([&](auto JI) { constexpr int j = m0 + decltype(JI)::value; static_assert(9 + j - K3_Q < s, "slot busy"); k3_wload(wq[(QB + 9 + j) % K3_Q], nxt, j, rot, voff); });
      if constexpr (s == 0) wait_vm<2 + 4 + 4 + 6>();
      else if constexpr (s >= 3) wait_vm<(s <= 6 ? 8 : (s == 7 ? 4 : 0)) + 4 * m1>();
      if constexpr (s == 3) bar();
    } else {
      if constexpr (s + K3_P < 9) k3_wload(wq[(QB + s + K3_P) % K3_Q], cur, s + K3_P, rot, voff);
      else if constexpr (NEXT) k3_wload(wq[(QB + s + K3_P) % K3_Q], nxt, s + K3_P - 9, rot, voff);
      constexpr int ny = NEXT ? K3_P : ((8 - s) < K3_P ? (8 - s) : K3_P);
      wait_vm<4 * ny + (s < K3_P ? NST : 0)>();
      if constexpr (!NEXT && s == K3_P) hload(std::integral_constant<int, 0>{});
    }
    sfor<4>([&](auto FI) { pin(wq[(QB + s) % K3_Q][decltype(FI)::value]); });
    sfor<2 * NG>([&](auto QI) {
      constexpr int q = decltype(QI)::value, it = s * 2 * NG + q, ks = it / NG, gp = it % NG, kq = ks & 1;
      if constexpr (it + 1 < 18 * NG) aread(std::integral_constant<int, it + 1>{});
      sb0();
#pragma unroll
      for (int f = 0; f < 2; ++f)
#pragma unroll
        for (int tk = 0; tk < 4; ++tk)
          acc[f][gp * 4 + tk] = mfma16(wq[(QB + s) % K3_Q][kq * 2 + f], af[it & 1][tk], ks == 0 ? (f32x4){} : acc[f][gp * 4 + tk]);
      sb0();
    });
  });
}

template <int HALF>
DI void k3_body(const char* __restrict__ ws_in, char* __restrict__ ws_out, char* lds, int w) {
  const int tid = threadIdx.x, lane = tid & 63;
  const int b = blockIdx.x, xcd = b & 7, idx = b >> 3;
  const int rt = (xcd & 1) * 8 + (idx & 7), fgp = (xcd >> 1) * 4 + (idx >> 3);
  const unsigned ldsb = (unsigned)(size_t)(LAS char*)lds;
  const unsigned voff = lane * 16;
  const int g = lane >> 4, l15 = lane & 15;
  const unsigned avoff = (g >> 1) * 1024 + (g & 1) * 512 + l15 * 16;
  const char* hsrc = ws_in + WS_HF + (size_t)(rt * 4 + (w >> 1)) * 36 * 1024 + (w & 1) * 256;
#define K3_ROT 0
  const int rot = K3_ROT;
  auto hload = [&](auto I) { constexpr int i = decltype(I)::value; const int ia = i + 2 * rot >= 18 ? i + 2 * rot - 18 : i + 2 * rot;
    glds16(hsrc + ia * 2048, avoff, ldsb + (i * 8 + w) * 1024); };
  auto nohl = [&](auto) {};
  const int t0 = w, t1 = w + 8, t2 = 16 + ((w >> 1) & 1);
  const char* wgu = ws_in + WS_WGU + (size_t)fgp * 18 * 36 * 1024;
  const char* sA = wgu + (size_t)t0 * 36 * 1024;
  const char* sB = wgu + (size_t)t1 * 36 * 1024;
  const char* sC = wgu + (size_t)t2 * 36 * 1024;
  u32x4 wq[K3_Q][4];
  u32x4 stv[2];
  {
    const char* sp = ws_in + WS_HST + (size_t)(rt * 128 + w * 16) * 128;
    gload16<0>(stv[0], sp, l15 * 128 + g * 32); gload16<16>(stv[1], sp, l15 * 128 + g * 32);
  }
  sfor<4>([&](auto I) { hload(I); });
  k3_wload(wq[0], sA, 0, rot, voff);
  hload(std::integral_constant<int, 4>{}); hload(std::integral_constant<int, 5>{});
  k3_wload(wq[1], sA, 1, rot, voff);
  wait_vm<10>();
  float* ctab = (float*)(lds + 144 * 1024);
  {
    pin(stv[0]); pin(stv[1]);
    float sm = (__uint_as_float(stv[0][0]) + __uint_as_float(stv[0][2])) + (__uint_as_float(stv[1][0]) + __uint_as_float(stv[1][2]));
    float sq = (__uint_as_float(stv[0][1]) + __uint_as_float(stv[0][3])) + (__uint_as_float(stv[1][1]) + __uint_as_float(stv[1][3]));
    sm += __shfl_xor(sm, 16); sq += __shfl_xor(sq, 16);
    sm += __shfl_xor(sm, 32); sq += __shfl_xor(sq, 32);
    const float mean = sm * (1.f / D_);
    const float rstd = ln_rstd(sq * (1.f / D_) - mean * mean);
    if (lane < 16) {
      ctab[w * 16 + lane] = -1.4426950408889634f * rstd;
    }
  }
  wait_lgkm0();
  bar();
  float c1[8];
#pragma unroll
  for (int tk = 0; tk < 8; ++tk) c1[tk] = ctab[tk * 16 + l15];
  tlmark(24); ckmark(0);
  unsigned lb[3] = {ldsb + voff, ldsb + voff + 49152, ldsb + voff + 98304};
  unsigned lh[3] = {lb[0] + (w & 1) * 4096, lb[1] + (w & 1) * 4096, lb[2] + (w & 1) * 4096};
  asm volatile("" : "+v"(lb[0]), "+v"(lb[1]), "+v"(lb[2]), "+v"(lh[0]), "+v"(lh[1]), "+v"(lh[2]));
  f32x4 acc[2][8];
  auto swiglu = [&](const f32x4& a, const f32x4& u, float c) -> u32x2 {
    float r[4];
#pragma unroll
    for (int j = 0; j < 4; ++j) { float e = __builtin_amdgcn_exp2f(a[j] * c); float pr = a[j] * u[j]; pin(pr); r[j] = pr * __builtin_amdgcn_rcpf(1.f + e); pin(r[j]); }
    return (u32x2){pk2(r[0], r[1]), pk2(r[2], r[3])};
  };
  char* actp = ws_out + WS_ACT + ((size_t)rt * 4 * 288 + fgp * 18) * 1024 + ((g & 1) * 32 + (g >> 1) * 16 + l15) * 16;
  auto epi2 = [&](int T, int tile32, const f32x4& g0, const f32x4& u0, float ca, const f32x4& g1, const f32x4& u1, float cb) {
    const u32x2 x0 = swiglu(g0, u0, ca), x1 = swiglu(g1, u1, cb);
    auto r0 = __builtin_amdgcn_permlane32_swap(x0[0], x1[0], false, false);
    auto r1 = __builtin_amdgcn_permlane32_swap(x0[1], x1[1], false, false);
    store16<2>(actp + ((size_t)tile32 * 288 + T) * 1024, (u32x4){r0[0], r1[0], r0[1], r1[1]});
  };
  auto epi_full = [&](int T) {
#pragma unroll
    for (int i = 0; i < 4; ++i) epi2(T, i, acc[0][2 * i], acc[1][2 * i], c1[2 * i], acc[0][2 * i + 1], acc[1][2 * i + 1], c1[2 * i + 1]);
  };
  k3_unit<8, true, true, 0, 0>(acc, wq, sA, HALF == 0 ? sC : sB, lb, rot, voff, hload);
  tlmark(25); ckmark(1);
  epi_full(t0);
  ckmark(2);
  if constexpr (HALF == 0) {
    k3_unit<4, false, true, 9 % K3_Q, 4>(acc, wq, sC, sB, lh, rot, voff, nohl);
    const bool hi = w & 1;
#pragma unroll
    for (int i = 0; i < 2; ++i) {
      float a0 = c1[2 * i], a1 = c1[4 + 2 * i], b0 = c1[2 * i + 1], b1 = c1[5 + 2 * i]; pin(a0); pin(a1); pin(b0); pin(b1);
      const float ca = hi ? a1 : a0, cb = hi ? b1 : b0;
      epi2(t2, (w & 1) * 2 + i, acc[0][2 * i], acc[1][2 * i], ca, acc[0][2 * i + 1], acc[1][2 * i + 1], cb);
    }
  }
  unsigned* cnt = (unsigned*)(ws_out + WS_CNT);
  auto sig1 = [&](auto) { sig_add(cnt + (256 + rt * 8 + (fgp >> 1)) * 32); };
  k3_unit<8, false, false, (HALF == 0 ? 18 : 9) % K3_Q, HALF == 0 ? 2 : 4>(acc, wq, sB, sB, lb, rot, voff, sig1);
  tlmark(29); ckmark(3);
  const unsigned* c1p = cnt + (256 + rt * 8 + (fgp >> 2) * 2 + HALF) * 32;
  unsigned pv = load4_sc1(c1p);
  epi_full(t1);
  wait_vm<4>();
  spin_ge(pv, c1p, 16u);
  ckmark(4);
}

#define K4_LA 4
#define K4_LW 4
constexpr int K4_R = 8, K4_D = 4;
constexpr int K4_LDS = K4_R * 16384;
constexpr int K4_NST = 21;
struct K4Sched {
  int aslot[18], sslot[K4_NST], posA[18], posS[K4_NST], cntBar[K4_NST], cntSlot[162];
  constexpr K4Sched() : aslot{}, sslot{}, posA{}, posS{}, cntBar{}, cntSlot{} {
    for (int a = 0; a < 18; ++a) aslot[a] = a < K4_LA ? -1 : 9 * (a - K4_LA) + 4;
    for (int s = 0; s < K4_NST; ++s) sslot[s] = s < K4_LW ? -1 : s - K4_LW;
    int c = 0;
    for (int s = 0; s < (K4_LW > K4_LA ? K4_LW : K4_LA); ++s) {
      if (s < K4_LW) { c += 2; posS[s] = c; }
      if (s < K4_LA) { c += 2; posA[s] = c; }
    }
    for (int n = -K4_D; n < 162; ++n) {
      if ((n + K4_D) % 8 == 0 && (n + K4_D) < 162) {
        int i = (n + K4_D) / 8; cntBar[i] = c;
        for (int s = 0; s < K4_NST; ++s) if (sslot[s] == i) { c += 2; posS[s] = c; }
      }
      if (n >= 0) {
        for (int q = 0; q < 18; ++q) if (aslot[q] == n) { c += 2; posA[q] = c; }
        cntSlot[n] = c;
      }
    }
  }
};
constexpr K4Sched k4s{};
#define K4_PISS 5
constexpr int K4_PCHK = 7;
static_assert(8 * K4_PCHK - K4_D <= k4s.aslot[10], "late activations must be requested behind the check");
DI void k4_body(const char* __restrict__ ws_in, char* __restrict__ ws_out, u16* __restrict__ out, char* lds, int w) {
  const int tid = threadIdx.x, lane = tid & 63;
  bar();
  tlmark(22);
  const int b = blockIdx.x, xcd = b & 7, idx = b >> 3;
  const int ksl = xcd >> 1, mt = (xcd & 1) * 8 + (idx & 7), ng = idx >> 3;
  const int tg = w & 3, kh = w >> 2;
  const int tt = mt * 4 + tg;
  const unsigned ldsb = (unsigned)(size_t)(LAS char*)lds;
  const char* wsrc = ws_in + WS_WD + (size_t)((ng * 4 + ksl) * 2 + kh) * WD_STREAM * 1024 + tg * 2048;
  const char* asrc = ws_in + WS_ACT + ((size_t)tt * 288 + ksl * 72 + kh * 36) * 1024;
  const unsigned voff = lane * 16;
  const int g = lane >> 4;
  const unsigned avoff0 = (g >> 1) * 1024 + (g & 1) * 512 + (lane & 15) * 16 + kh * 256, avoff1 = avoff0 ^ 256;
  const int th = w & 1, a = w >> 1, ott = mt * 4 + ksl, tok = th * 16 + (lane & 15);
  const int col = 32 * a + 8 * g, col8 = 128 + 4 * g;
  u32x2 h0, h1, h2; unsigned mub;
  {
    const char* hfp = ws_in + WS_HF + ((size_t)ott * 36 + ng * 9) * 1024;
    auto ho = [&](int c) -> unsigned { return (c >> 4) * 1024 + (((c >> 2) & 1) * 32 + tok) * 16 + ((c >> 3) & 1) * 8; };
    gload8<0>(h0, hfp, ho(col)); gload8<512>(h1, hfp, ho(col)); gload8<0>(h2, hfp, ho(col8));
    gload4<0>(mub, ws_in + WS_MEAN + (size_t)ott * 128, tok * 4);
  }
  u32x4 af[K4_LA + 1][2];
  auto issue_stage = [&](auto SI) {
    constexpr int s = decltype(SI)::value;
    glds16x2(wsrc + (size_t)s * 8192, voff, ldsb + (s % K4_R) * 16384 + kh * 8192 + tg * 2048);
  };
  auto issue_a = [&](auto AI) {
    constexpr int a = decltype(AI)::value;
    constexpr int ka = k4_act(a);
    gload16_sc1<(ka % 2) * 2048>(af[a % (K4_LA + 1)][0], asrc + (ka / 2) * 4096, avoff0);
    gload16_sc1<(ka % 2) * 2048>(af[a % (K4_LA + 1)][1], asrc + (ka / 2) * 4096, avoff1);
  };
  sfor<(K4_LW > K4_LA ? K4_LW : K4_LA)>([&](auto SI) {
    constexpr int s = decltype(SI)::value;
    if constexpr (s < K4_LW) issue_stage(SI);
    if constexpr (s < K4_LA) issue_a(SI);
    if constexpr (s == 0) bar();
  });
  f32x4 acc[9][2];
  u32x4 wf[K4_D + 1];
  unsigned* cntb = (unsigned*)(ws_out + WS_CNT);
  const unsigned* c2p = cntb + (384 + mt * 8 + ksl * 2 + kh) * 32;
  unsigned pv2 = 0;
  unsigned lrd0 = ldsb + kh * 8192 + lane * 16, lrd1 = lrd0 + 65536;
  unsigned xw = ldsb + w * 1024 + lane * 16, xw5 = xw + 5 * 16384;
  asm volatile("" : "+v"(lrd0), "+v"(lrd1), "+v"(xw), "+v"(xw5));
  auto xad = [&](unsigned lo, unsigned hi, int t) -> unsigned { return t < 6 ? hi + t * 8192 : lo + (t - 6) * 8192; };
  auto rd = [&](auto NI) {
    constexpr int n = decltype(NI)::value;
    if constexpr (n < 162) {
      constexpr int st = n / 8;
      if constexpr (n % 8 == 0) {
        wait_vm<k4s.cntBar[st] - k4s.posS[st]>();
        if constexpr (st == 0) {
          sig_add(cntb + (384 + mt * 8 + ksl * 2 + (ng >> 1)) * 32);
        }
        if constexpr (st == K4_PCHK) {
          wait_vm<k4s.cntBar[K4_PCHK] - k4s.cntBar[K4_PISS]>();
          spin_ge(pv2, c2p, 16u);
        }
        bar();
        if constexpr (st == 0) { tlmark(26); ckmark(5); }
        if constexpr (st == K4_PISS) pv2 = load4_sc1(c2p);
        sfor<K4_NST>([&](auto SI) { if constexpr (k4s.sslot[decltype(SI)::value] == st) issue_stage(SI); });
      }
      constexpr int off = (st % K4_R) * 16384 + (n % 8) * 1024;
      wf[n % (K4_D + 1)] = *(const LAS u32x4*)(size_t)((off < 65536 ? lrd0 : lrd1) + (off & 65535));
    }
  };
  sfor<K4_D>([&](auto I) { rd(I); });
  sfor<162>([&](auto NI) {
    constexpr int n = decltype(NI)::value;
    constexpr int ks = n / 9, t = n % 9, ab = ks % (K4_LA + 1);
    rd(std::integral_constant<int, n + K4_D>{});
    sfor<18>([&](auto AI) { if constexpr (k4s.aslot[decltype(AI)::value] == n) issue_a(AI); });
    if constexpr (t == 0) {
      wait_vm<k4s.cntSlot[n] - k4s.posA[ks]>();
      pin(af[ab][0]); pin(af[ab][1]);
    }
    acc[t][0] = mfma16(wf[n % (K4_D + 1)], af[ab][0], ks == 0 ? (f32x4){} : acc[t][0]);
    acc[t][1] = mfma16(wf[n % (K4_D + 1)], af[ab][1], ks == 0 ? (f32x4){} : acc[t][1]);
    if constexpr (n >= 155) *(LAS f32x4*)(size_t)xad(xw, xw5, n - 155) = acc[n - 155][1];
    sb0();
  });
  tlmark(27); ckmark(6);
  *(LAS f32x4*)(size_t)xad(xw, xw5, 7) = acc[7][1]; *(LAS f32x4*)(size_t)xad(xw, xw5, 8) = acc[8][1];
  wait_lgkm0(); bar();
  tlmark(20);
  const unsigned xr = ldsb + (w ^ 4) * 1024 + lane * 16, xr5 = xr + 5 * 16384;
  f32x4 v[9];
#pragma unroll
  for (int t = 0; t < 9; ++t) v[t] = *(const LAS f32x4*)(size_t)xad(xr, xr5, t) + acc[t][0];
  if (tg == ksl) {
#pragma unroll
    for (int t = 0; t < 9; ++t) *(LAS f32x4*)(size_t)xad(xr, xr5, t) = v[t];
  } else {
    char* pp = ws_out + WS_PART + ((((size_t)ksl * 64 + tt) * 4 + ng) * 2 + kh) * 5120;
#pragma unroll
    for (int i = 0; i < 4; ++i)
      store16<2>(pp + i * 1024 + lane * 16, (u32x4){pk2(v[2 * i][0], v[2 * i][1]), pk2(v[2 * i][2], v[2 * i][3]), pk2(v[2 * i + 1][0], v[2 * i + 1][1]), pk2(v[2 * i + 1][2], v[2 * i + 1][3])});
    store8_sc1(pp + 4096 + lane * 8, (u32x2){pk2(v[8][0], v[8][1]), pk2(v[8][2], v[8][3])});
    wait_vm<0>();
    if (lane == 0) __hip_atomic_fetch_add((unsigned*)(ws_out + WS_CNT) + ((mt * 4 + ng) * 4 + tg) * 32, 1u, __ATOMIC_RELAXED, __HIP_MEMORY_SCOPE_AGENT);
  }
  tlmark(21);
  if (w == ksl) {
    const unsigned* cnt = (const unsigned*)(ws_out + WS_CNT) + ((mt * 4 + ng) * 4 + ksl) * 32;
    unsigned spins = 0;
    while (__hip_atomic_load(cnt, __ATOMIC_RELAXED, __HIP_MEMORY_SCOPE_AGENT) < 6u) {
      __builtin_amdgcn_s_sleep(1);
      if (++spins > (1u << 22)) break;
    }
  }
  wait_lgkm0();
  bar();
  tlmark(28);
  const unsigned ox = ldsb + ((th ^ 1) * 4 + ksl) * 1024 + lane * 16, oa = ox + (a < 3 ? 5 * 16384 + a * 16384 : 0);
  f32x4 s0 = *(const LAS f32x4*)(size_t)oa, s1 = *(const LAS f32x4*)(size_t)(oa + 8192), s2 = *(const LAS f32x4*)(size_t)(ox + 16384);
  const char* pb = ws_in + WS_PART + (((size_t)ott * 4 + ng) * 2 + th) * 5120;
  constexpr size_t KS = (size_t)64 * 4 * 2 * 5120;
  u32x4 pv[3]; unsigned pw[3][2];
#pragma unroll
  for (int k = 0; k < 3; ++k) {
    const char* q = pb + (size_t)((ksl + 1 + k) & 3) * KS;
    pv[k] = load16_sc1(q + a * 1024 + lane * 16);
    const char* q8 = q + (a == 0 ? 4096 + lane * 8 : a * 1024);
    pw[k][0] = load4_sc1(q8); pw[k][1] = load4_sc1(q8 + 4);
  }
  wait_vm<0>();
#pragma unroll
  for (int k = 0; k < 3; ++k) { pin(pv[k]); pin(pw[k][0]); pin(pw[k][1]); }
#pragma unroll
  for (int k = 0; k < 3; ++k) {
    const unsigned a0 = pv[k][0], a1 = pv[k][1], a2 = pv[k][2], a3 = pv[k][3], b0 = pw[k][0], b1 = pw[k][1];
    s0 += (f32x4){bflo(a0), bfhi(a0), bflo(a1), bfhi(a1)}; s1 += (f32x4){bflo(a2), bfhi(a2), bflo(a3), bfhi(a3)}; s2 += (f32x4){bflo(b0), bfhi(b0), bflo(b1), bfhi(b1)};
  }
  pin(h0); pin(h1); pin(h2); pin(mub);
  const float rs = ((const float*)(lds + 144 * 1024))[ksl * 32 + tok] * 0.6931471805599453f, r2 = rs * rs, mu = __uint_as_float(mub);
  u16* orow = out + (size_t)(ott * 32 + tok) * D_ + ng * 144;
  s0 = s0 * r2 + mu; s1 = s1 * r2 + mu; s2 = s2 * r2 + mu;
  u32x4 o;
  o[0] = pk2(s0[0] + bflo(h0[0]), s0[1] + bfhi(h0[0])); o[1] = pk2(s0[2] + bflo(h0[1]), s0[3] + bfhi(h0[1]));
  o[2] = pk2(s1[0] + bflo(h1[0]), s1[1] + bfhi(h1[0])); o[3] = pk2(s1[2] + bflo(h1[1]), s1[3] + bfhi(h1[1]));
  *(u32x4*)(orow + col) = o;
  if (a == 0) *(u32x2*)(orow + col8) = (u32x2){pk2(s2[0] + bflo(h2[0]), s2[1] + bfhi(h2[0])), pk2(s2[2] + bflo(h2[1]), s2[3] + bfhi(h2[1]))};
}
__global__ __launch_bounds__(512, 2) void k34_ffn(const char* __restrict__ ws_in, char* __restrict__ ws_out, u16* __restrict__ out) {
  asm volatile(".Lfk_text_begin:");
  TLSCOPE(3);
  extern __shared__ __attribute__((aligned(16))) char lds[];
  const int w = uni(threadIdx.x >> 6);
  if (w < 4) k3_body<0>(ws_in, ws_out, lds, w); else k3_body<1>(ws_in, ws_out, lds, w);
  k4_body(ws_in, ws_out, out, lds, w);
}
}
#define OUT_ST 2
#define OUT2_ST 0
#define GU_ST 2
#define PACK_ST 2
namespace fk {
constexpr size_t WS_PQ   = (WS_END_FFN + 4095) / 4096 * 4096;
constexpr size_t WS_KVP  = WS_PQ + SZ_HF;
constexpr size_t SZ_KVP  = (size_t)16 * 8 * 16 * 1024;
constexpr size_t WS_KVF  = WS_KVP + SZ_KVP;
constexpr size_t SZ_KVF  = 64 * 1024;
constexpr size_t WS_WOP  = WS_KVF + SZ_KVF;
constexpr size_t SZ_WOP  = (size_t)4 * 4 * 9 * 5 * 1024;
constexpr size_t WS_MX   = WS_WOP + SZ_WOP;
constexpr size_t WS_END  = WS_MX + S_ * 4;

DI float phi_f(float t) { return (t > 0.f ? t + 1.f : __builtin_amdgcn_exp2f(t * 1.4426950408889634f)) + 1e-6f; }

DI void pack_wo_task(const u16* wo, char* ws, int id) {
  const int lane = id & 63, fi = id >> 6, i = fi % 5, u = (fi / 5) % 9, w = (fi / 45) & 3, ng = fi / 180;
  const int row = lane & 31, h = lane >> 5;
  if (i == 4 && row >= 16) {
    if (row >= 18) *(u32x4*)(ws + WS_WOP + (size_t)id * 16) = (u32x4){0u, 0u, 0u, 0u};
    return;
  }
  const int n = ng * 144 + i * 32 + row;
  unsigned v[8];
#pragma unroll
  for (int j = 0; j < 8; ++j) {
    const int e = u < 8 ? 18 * (8 * w + u) + 8 * (j >> 2) + 4 * h + (j & 3) : 18 * (8 * w + 4 * h + (j >> 1)) + 16 + (j & 1);
    v[j] = wo[e * D_ + n];
  }
  store16<OUT_ST>(ws + WS_WOP + (size_t)id * 16, (u32x4){v[0] | (v[1] << 16), v[2] | (v[3] << 16), v[4] | (v[5] << 16), v[6] | (v[7] << 16)});
}
DI void pack_worow_task(const u16* wo, char* ws, int e, int lane) {
  float s = 0.f;
#pragma unroll
  for (int i = 0; i < 9; ++i) s += bf2f(wo[e * D_ + i * 64 + lane]);
#pragma unroll
  for (int o = 32; o; o >>= 1) s += __shfl_xor(s, o);
  const unsigned hi = pk2(s, 0.f) & 0xffffu; const unsigned lo = pk2(s - bflo(hi), 0.f) & 0xffffu;
  const int hh = e / 18, el = e % 18, w = hh >> 3, ul = hh & 7;
  int u, h, j;
  if (el < 16) { u = ul; h = (el >> 2) & 1; j = (el >> 3) * 4 + (el & 3); } else { u = 8; h = ul >> 2; j = (ul & 3) * 2 + (el - 16); }
  if (lane < 8) {
    const int ng = lane >> 1, part = lane & 1;
    u16* p = (u16*)(ws + WS_WOP) + ((((size_t)(ng * 4 + w) * 9 + u) * 5 + 4) * 64 + h * 32 + 16 + part) * 8 + j;
    *p = (u16)(part ? lo : hi);
  }
}

constexpr int K1_WST = 18432, K1_STG = K1_WST + 16384, K1_R = 4;
constexpr int K1_TAB = K1_STG * K1_R;
constexpr int K1_LDS = 8 * 18432;
static_assert(K1_TAB + 4 * 144 * 4 + 1024 <= K1_LDS, "lds");
#define K1_PORD 1
#define K1_NGU 64
#define K1_NOPACK 0
#define K1_DD 3
constexpr int K1_D = K1_DD;
DI float phi3(float u, float rs, float rl) { return fmaxf(fmaf(u, rs, 1e-6f), 1e-6f) + __builtin_amdgcn_exp2f(fminf(u * rl, 0.f)); }
template <bool ISQ, int NH> constexpr int k1_tile(int j) {
  if (ISQ) return NH ? 5 + j : j;
  if (NH == 0) return j < 2 ? j : (j == 2 ? 4 : j + 2);
  return j < 2 ? 2 + j : 5 + j;
}
template <bool ISQ> constexpr int k1_toff(int t) { return ISQ ? 32 * t : (t < 4 ? 32 * t : 144 + 32 * (t - 5)); }

__host__ __device__ constexpr int k1_img(int t) { return t < 8 ? t * 4096 : 3 * K1_STG + (t - 8) * 4096; }
template <bool ISQ, int NH, class ISSUE, class TAIL>
DI void k1_body(char* lds, unsigned ldsb, int lane, int tg, int rt, int grp, char* ws, ISSUE&& issue, TAIL&& tail) {
  constexpr int NT = NH ? 4 : 5, NWF = NT + K1_D;
  const int g = lane >> 4, l15 = lane & 15, q4 = (lane >> 2) & 3, p4 = lane & 3;
  const unsigned rrow = ldsb + (16 * (g >> 1) + 4 * (g & 1) + q4) * 288;
  unsigned rbA[2], rbB[2], rbX[2][2];
  rbA[0] = rrow + p4 * 8;
  rbB[0] = rrow + (p4 < 2 ? 128 + 8 * p4 : 272 + 8 * (p4 - 2));
  rbA[1] = rbA[0] + 2 * K1_STG; rbB[1] = rbB[0] + 2 * K1_STG;
  const unsigned xb = ldsb + K1_WST + tg * 4096 + l15 * 128;
#pragma unroll
  for (int kh = 0; kh < 2; ++kh) { rbX[kh][0] = xb + (((4 * kh + g) ^ ((l15 >> 1) & 7)) * 16); rbX[kh][1] = rbX[kh][0] + 2 * K1_STG; }
  asm volatile("" : "+v"(rbA[0]), "+v"(rbA[1]), "+v"(rbB[0]), "+v"(rbB[1]), "+v"(rbX[0][0]), "+v"(rbX[0][1]), "+v"(rbX[1][0]), "+v"(rbX[1][1]));
  u32x4 ones = {0x3f803f80u, 0x3f803f80u, 0x3f803f80u, 0x3f803f80u};
  pin(ones);
  f32x4 acc[NT][2], accs[NT], sst[2][2];
#pragma unroll
  for (int t = 0; t < NT; ++t) accs[t] = (f32x4){};
  u32x4 xf[2][2], wf[NWF];
  auto rd = [&](auto NI) {
    constexpr int n = decltype(NI)::value;
    if constexpr (n < 18 * NT) {
      constexpr int ks = n / NT, j = n % NT, st = ks >> 1, kh = ks & 1, slot = st % K1_R, hb = slot >> 1, so = (slot & 1) * K1_STG;
      constexpr int t = k1_tile<ISQ, NH>(j);
      constexpr int pos = n % (2 * NT);
      constexpr int nxt = st == 0 ? 3 : (st >= 2 && st + 2 < 9 ? st + 2 : -1);
      if constexpr (pos == 0) {
        constexpr int upto = st == 0 ? 2 : (st + 1 > 3 ? (st + 1 < 8 ? st + 1 : 8) : 3);
        if constexpr (NH == 1 && st == 8) tail(std::integral_constant<int, 0>{});
        else wait_vm<4 * (upto - st)>();
        bar();
        if constexpr (st == 0) tlmark(17);
        if constexpr (NH == 1 && st >= 7) tail(std::integral_constant<int, st>{});
      }
      if constexpr (j == 0) {
        xf[kh][0] = *(const LAS u32x4*)(size_t)(rbX[kh][hb] + so);
        xf[kh][1] = *(const LAS u32x4*)(size_t)(rbX[kh][hb] + so + 2048);
      }
      const unsigned a = ((!ISQ && t == 4) ? rbB[hb] : rbA[hb] + k1_toff<ISQ>(t)) + so + kh * 9216;
      u32x2 lo = __builtin_bit_cast(u32x2, tr_read(a)), hi = __builtin_bit_cast(u32x2, tr_read(a + 8 * 288));
      wf[n % NWF] = (u32x4){lo[0], lo[1], hi[0], hi[1]};
      if constexpr (nxt >= 0 && (pos & 1) == 1 && pos / 2 < 4) issue(std::integral_constant<int, nxt>{}, std::integral_constant<int, pos / 2>{});
    }
  };
  sfor<K1_D>([&](auto I) { rd(I); });
  sfor<18 * NT>([&](auto NI) {
    constexpr int n = decltype(NI)::value, ks = n / NT, j = n % NT, kh = ks & 1;
    rd(std::integral_constant<int, n + K1_D>{});
    const u32x4 wv = wf[n % NWF];
    sfor<2>([&](auto HI) {
      constexpr int th = decltype(HI)::value;
      const f32x4 c = ks == 0 ? (f32x4){} : acc[j][th];
      acc[j][th] = ISQ ? mfma16(wv, xf[kh][th], c) : mfma16(xf[kh][th], wv, c);
    });
    if constexpr (NH == 1 && j < 2) {
      sst[j][0] = mfma16(ones, xf[kh][j], ks == 0 ? (f32x4){} : sst[j][0]);
      sst[j][1] = mfma16(xf[kh][j], xf[kh][j], ks == 0 ? (f32x4){} : sst[j][1]);
    }
    if constexpr (j == NT - 1) {
      if (tg == (ks & 3)) {
        sfor<NT>([&](auto JI) { constexpr int jj = decltype(JI)::value; accs[jj] = mfma16(ones, wf[(ks * NT + jj) % NWF], accs[jj]); });
      }
    }
    sb0();
  });
  tlmark(16);
  float* tab = (float*)(lds + K1_TAB);
  float* stt = tab + 4 * 144;
  if (g == 0) {
    sfor<NT>([&](auto JI) { constexpr int jj = decltype(JI)::value; tab[tg * 144 + k1_tile<ISQ, NH>(jj) * 16 + l15] = accs[jj][0]; });
  }
  if constexpr (NH == 1) {
    if (g == (l15 >> 2)) {
#pragma unroll
      for (int th = 0; th < 2; ++th) {
        const int r = l15 & 3;
        const float q = r == 0 ? sst[th][1][0] : (r == 1 ? sst[th][1][1] : (r == 2 ? sst[th][1][2] : sst[th][1][3]));
        const float mean = sst[th][0][0] * (1.f / D_);
        stt[tg * 32 + th * 16 + l15] = rbf(mean);
        if (ISQ && grp == 0) ((float*)(ws + WS_MX))[rt * 128 + tg * 32 + th * 16 + l15] = mean;
        stt[128 + tg * 32 + th * 16 + l15] = ln_rstd(q * (1.f / D_) - mean * mean);
      }
    }
  }
  wait_lgkm0(); bar();
  constexpr float L2E = 1.4426950408889634f;
  if constexpr (ISQ) {
    const int tt = rt * 4 + tg;
    float nm[2], rs[2], rl[2];
#pragma unroll
    for (int th = 0; th < 2; ++th) { nm[th] = -stt[tg * 32 + th * 16 + l15]; rs[th] = stt[128 + tg * 32 + th * 16 + l15]; rl[th] = rs[th] * L2E; }
    char* pq = ws + WS_PQ + ((size_t)tt * 36 + grp * 9) * 1024 + ((g & 1) * 32 + (g >> 1) * 16 + l15) * 16;
    sfor<NT>([&](auto JI) {
      constexpr int j = decltype(JI)::value, t = k1_tile<ISQ, NH>(j);
      const float* cp = tab + t * 16 + g * 4;
      const f32x4 cs = (*(const f32x4*)cp + *(const f32x4*)(cp + 144)) + (*(const f32x4*)(cp + 288) + *(const f32x4*)(cp + 432));
      unsigned pk[2][2];
#pragma unroll
      for (int th = 0; th < 2; ++th) {
        float v[4];
#pragma unroll
        for (int e = 0; e < 4; ++e) v[e] = phi3(fmaf(nm[th], cs[e], acc[j][th][e]), rs[th], rl[th]);
        pk[th][0] = pk2(v[0], v[1]); pk[th][1] = pk2(v[2], v[3]);
      }
      auto r0 = __builtin_amdgcn_permlane32_swap(pk[0][0], pk[1][0], false, false);
      auto r1 = __builtin_amdgcn_permlane32_swap(pk[0][1], pk[1][1], false, false);
      store16<OUT_ST>(pq + t * 1024, (u32x4){r0[0], r1[0], r0[1], r1[1]});
    });
  } else {
    f32x4 mn[2], rs[2], rl[2];
#pragma unroll
    for (int th = 0; th < 2; ++th) { mn[th] = *(const f32x4*)(stt + tg * 32 + th * 16 + g * 4); rs[th] = *(const f32x4*)(stt + 128 + tg * 32 + th * 16 + g * 4); rl[th] = rs[th] * L2E; }
    char* ob = lds + (tg * 64 + lane) * 16;
    sfor<NT>([&](auto JI) {
      constexpr int j = decltype(JI)::value, t = k1_tile<ISQ, NH>(j);
      const float* cp = tab + t * 16 + l15;
      const float ncs = -((cp[0] + cp[144]) + (cp[288] + cp[432]));
      u32x4 pv;
#pragma unroll
      for (int th = 0; th < 2; ++th) {
        float v[4];
#pragma unroll
        for (int e = 0; e < 4; ++e) {
          const float u = fmaf(mn[th][e], ncs, acc[j][th][e]);
          if (t < 4) v[e] = phi3(u, rs[th][e], rl[th][e]);
          else if (t > 4) v[e] = u * rs[th][e];
          else v[e] = l15 < 8 ? phi3(u, rs[th][e], rl[th][e]) : u * rs[th][e];
        }
        pv[2 * th] = pk2(v[0], v[1]); pv[2 * th + 1] = pk2(v[2], v[3]);
      }
      *(u32x4*)(ob + k1_img(t)) = pv;
      if constexpr (t == 4) *(u32x4*)(ob + k1_img(9)) = l15 < 8 ? ones : pv;
    });
    wait_lgkm0(); bar();

    const int w = NH * 4 + tg;
#pragma unroll
    for (int pi = 0; pi < 2; ++pi) {
      const int p = 2 * w + pi;
      int a, bt;
      if (p < 10) { a = (p + 1) / 3; bt = 5 + a + ((p + 1) % 3) - 1; } else if (p == 10) { a = 4; bt = 8; } else { a = p - 11; bt = 9; }
      f32x4 c = {};
#pragma unroll
      for (int i = 0; i < 4; ++i) {
        const u32x4 av = *(const u32x4*)(lds + a * 4096 + (i * 64 + lane) * 16);
        const u32x4 bv = *(const u32x4*)(lds + k1_img(bt) + (i * 64 + lane) * 16);
        c = mfma16(av, bv, c);
      }
      if constexpr (OUT_ST != 0) asm volatile("s_nop 9" : "+v"(c));
      store16<OUT_ST>(ws + WS_KVP + (((size_t)rt * 8 + grp) * 16 + p) * 1024 + lane * 16, __builtin_bit_cast(u32x4, c));
    }
  }
}

__global__ __launch_bounds__(512, 2) void k1_qkv(const u16* __restrict__ x, const u16* __restrict__ wqkv, const u16* __restrict__ wo,
                                                  const u16* __restrict__ wg, const u16* __restrict__ wu, const u16* __restrict__ wd, char* __restrict__ ws) {
  TLSCOPE(0);
  extern __shared__ __attribute__((aligned(16))) char lds[];
  const int tid = threadIdx.x, lane = tid & 63, w = uni(tid >> 6);
  const int b = blockIdx.x, xcd = b & 7, idx = b >> 3;
  const unsigned ldsb = (unsigned)(size_t)(LAS char*)lds;
  if (idx >= 24) {
    {
      const char* c0; unsigned len; text_range(c0, len);
      const unsigned o = (unsigned)((((idx - 24) * 8 + w) * 64 + lane) * 128);
      if ((unsigned)(((idx - 24) * 8 + w) * 8192) < len) l2touch(c0, o < len ? o : 0u, ldsb);
    }
    const int pw = ((idx - 24) * 8 + xcd) * 8 + w;
    if (pw < 8) ((unsigned*)(ws + WS_CNT))[(pw * 64 + lane) * 32] = 0u;
    const int u = (idx - 24) * 8 + w;
    pack_gu_load(wg, wu, xcd * 9 + u % 9, u / 9, ldsb + w * 16384 + 256, lane);
    for (int id = pw * 64 + lane; id < 4 * 4 * 9 * 5 * 64; id += 512 * 64) pack_wo_task(wo, ws, id);
    for (int e = pw; e < D_; e += 512) pack_worow_task(wo, ws, e, lane);
    tlmark(18);
    wait_vm<0>();
    pack_gu_store<GU_ST, false>(ws, xcd * 9 + u % 9, u / 9, ldsb + w * 16384 + 256, lane);
    tlmark(19);
    return;
  }
  const int rt = (xcd & 3) * 4 + (idx & 3), cgl = idx >> 2;
  const int ch = xcd >> 2;
  const bool isq = cgl < 2;
  const int grp = isq ? ch * 2 + cgl : ch * 4 + (cgl - 2);
  auto wsrc_off = [&](int n) -> unsigned {
    const int c = n * 64 + lane, rho = c / 18, cc = c % 18;
    const int k = (rho & ~12) | ((rho & 4) << 1) | ((rho & 8) >> 1);
    const int col = isq ? 144 * grp + 8 * cc : (cc < 9 ? 576 + 72 * grp + 8 * cc : 1152 + 72 * grp + 8 * (cc - 9));
    return (unsigned)((k * 1728 + col) * 2);
  };
  const char* xsrc = (const char*)x + (size_t)(rt * 128 + w * 16) * 1152;
  const unsigned xoff0 = (lane >> 3) * 1152 + (((lane & 7) ^ (lane >> 4)) * 16);
  const unsigned xoff1 = ((lane >> 3) + 8) * 1152 + (((lane & 7) ^ (4 + (lane >> 4))) * 16);
#define K1_ROT (((idx & 3) * 2 + cgl) % 9)
  const int rot = K1_ROT;
  glds16(xsrc + rot * 128, xoff0, ldsb + K1_WST + w * 2048);
  glds16(xsrc + rot * 128, xoff1, ldsb + K1_WST + w * 2048 + 1024);
  const unsigned woffA = wsrc_off(w), woffB = wsrc_off(w + 8), woffC = wsrc_off(16 + (w & 1));
  auto issue = [&](auto SI, auto PI) {
    constexpr int st = decltype(SI)::value, slot = st % K1_R, pc = decltype(PI)::value;
    const int sa = st + rot >= 9 ? st + rot - 9 : st + rot;
    const char* wb = (const char*)wqkv + (size_t)sa * 64 * 3456;
    const unsigned sb = ldsb + slot * K1_STG;
    if constexpr (pc == 0) glds16(xsrc + sa * 128, xoff0, sb + K1_WST + w * 2048);
    if constexpr (pc == 1) glds16(xsrc + sa * 128, xoff1, sb + K1_WST + w * 2048 + 1024);
    if constexpr (pc == 2) { if (w < 2) glds16(wb, woffC, sb + (16 + w) * 1024); glds16(wb, woffA, sb + w * 1024); }
    if constexpr (pc == 3) glds16(wb, woffB, sb + (w + 8) * 1024);
  };
  issue(std::integral_constant<int, 0>{}, std::integral_constant<int, 2>{});
  issue(std::integral_constant<int, 0>{}, std::integral_constant<int, 3>{});
  bar();
  sfor<4>([&](auto PI) { issue(std::integral_constant<int, 1>{}, PI); });
  sfor<4>([&](auto PI) { issue(std::integral_constant<int, 2>{}, PI); });
  const int tg = w & 3;
  const int pslot = w == 5 ? idx : (w == 6 ? (idx < 21 ? 24 + idx : 99) : ((w == 7 && isq) ? 45 + idx : 99));
  const unsigned pl = ldsb + (w == 6 ? 2 * K1_STG : K1_STG + (w == 7 ? 18432 : 0));
  auto tail = [&](auto SI) {
    constexpr int st = decltype(SI)::value;
    if constexpr (st == 0) { if (w == 5) wait_vm<18>(); else if (pslot < 99 && w == 7) wait_vm<16>(); else wait_vm<0>(); }
    else if constexpr (st == 9) {
      wait_vm<0>();
      if (pslot < 36) pack_wd_store<PACK_ST>(ws, xcd * 18 + (pslot >> 1), pslot & 1, pl, lane);
      else if (pslot < 53) pack_gu_store<GU_ST, false>(ws, xcd * 9 + (K1_NGU + pslot - 36) % 9, (K1_NGU + pslot - 36) / 9, pl, lane);
    } else if ((w == 6) == (st == 8)) {
      if (pslot < 36) pack_wd_load(wd, xcd * 18 + (pslot >> 1), pslot & 1, pl, lane);
      else if (pslot < 53) pack_gu_load(wg, wu, xcd * 9 + (K1_NGU + pslot - 36) % 9, (K1_NGU + pslot - 36) / 9, pl, lane);
    }
  };
  static_assert(K1_NGU == 64, "tail slot map");
  if (isq) { if (w < 4) k1_body<true, 0>(lds, ldsb, lane, tg, rt, grp, ws, issue, tail); else k1_body<true, 1>(lds, ldsb, lane, tg, rt, grp, ws, issue, tail); }
  else     { if (w < 4) k1_body<false, 0>(lds, ldsb, lane, tg, rt, grp, ws, issue, tail); else k1_body<false, 1>(lds, ldsb, lane, tg, rt, grp, ws, issue, tail); }
  tlmark(30);
  tail(std::integral_constant<int, 9>{});
}

constexpr int K1B_LDS = 4 * 18432;
__global__ __launch_bounds__(256) void k1b_reduce(const u16* __restrict__ wg, const u16* __restrict__ wu, const u16* __restrict__ wd, char* __restrict__ ws) {
  TLSCOPE(1);
  extern __shared__ __attribute__((aligned(16))) char lds[];
  const int tid = threadIdx.x;
  const int b = blockIdx.x;
  const int hh = b, s = tid >> 7, l = (tid >> 1) & 63, r = tid & 1;
  const int row = l & 31, h = l >> 5;
  const int T = (18 * hh) / 16 + s;
  const int d0 = 16 * T + 8 * r + 4 * h;
  const int G = hh >> 2, hl = hh & 3;
  int el = -1; bool isz = false;
  if (row < 16) el = row;
  else if (row == 18 || row == 22) isz = true;
  else if ((hh & 7) < 4 ? (row == 16 || row == 17) : (row == 20 || row == 21)) el = 16 + (row & 1);
  const int dlo = 18 * hh, dhi = dlo + 18;
  f32x4 sum = {};
  if ((el >= 0 || isz) && d0 + 3 >= dlo && d0 < dhi) {
    const int dk = d0 - 72 * G, a = dk >> 4, dr = dk & 15;
    int p, col;
    if (isz) { p = 11 + a; col = 0; }
    else {
      const int ev = 18 * hl + el;
      if (ev >= 64) { p = 11 + a; col = 8 + ev - 64; }
      else { const int vt = ev >> 4; col = ev & 15; p = a == 4 ? 10 : 3 * a + (vt - a); }
    }
    const char* src = ws + WS_KVP + ((size_t)G * 16 + p) * 1024 + ((dr >> 2) * 16 + col) * 16;
    f32x4 v[16];
#pragma unroll
    for (int i = 0; i < 16; ++i) v[i] = *(const f32x4*)(src + (size_t)i * 8 * 16 * 1024);
#pragma unroll
    for (int i = 0; i < 16; ++i) sum += v[i];
#pragma unroll
    for (int j = 0; j < 4; ++j) if (d0 + j < dlo || d0 + j >= dhi) sum[j] = 0.f;
  }
  *(u32x2*)(ws + WS_KVF + ((size_t)(hh * 2 + s) * 64 + l) * 16 + r * 8) = (u32x2){pk2(sum[0], sum[1]), pk2(sum[2], sum[3])};
}

#define K2_WIN 16
constexpr int K2_LDS = 4 * 19 * 1024;
__global__ __launch_bounds__(256, 1) void k2_attn_out(const u16* __restrict__ x, const char* __restrict__ ws_in, char* __restrict__ ws) {
  TLSCOPE(2);
  extern __shared__ __attribute__((aligned(16))) char lds[];
  const int tid = threadIdx.x, lane = tid & 63, w = uni(tid >> 6);
  const int b = blockIdx.x, xcd = b & 7, idx = b >> 3;
  const int tt = xcd * 8 + (idx & 7), ng = idx >> 3;
  const int tok = lane & 31, h = lane >> 5;
  const char* pqp = ws_in + WS_PQ + ((size_t)tt * 36 + w * 9) * 1024;
  const char* kvp = ws_in + WS_KVF + (size_t)w * 16 * 1024;
  const char* wop = ws_in + WS_WOP + (size_t)(ng * 4 + w) * 45 * 1024;
  const unsigned voff = lane * 16, voffc = (tok < 24 ? lane : (lane & 32)) * 16;
  u32x4 pq[9], kvf[16], wf[45];
  auto issue = [&](auto II) {
    constexpr int i = decltype(II)::value;
    if constexpr (i == 0) gload16<0>(pq[0], pqp, voff);
    else if constexpr (i < 70) {
      constexpr int u = (i - 1) / 8, k = (i - 1) % 8;
      if constexpr (u < 8 && k < 2) gload16<((2 * u + k) % 4) * 1024>(kvf[2 * u + k], kvp + ((2 * u + k) / 4) * 4096, voffc);
      else if constexpr (u < 8 && k == 2) gload16<((u + 1) % 4) * 1024>(pq[u + 1], pqp + ((u + 1) / 4) * 4096, voff);
      else { constexpr int n = u < 8 ? 5 * u + k - 3 : 40 + k; gload16<(n % 4) * 1024>(wf[n], wop + (n / 4) * 4096, n % 5 == 4 ? voffc : voff); }
    }
  };
  const int u0 = w == 0 ? 0 : 2 * w + 1;
  u32x4 xr[3]; unsigned mxb;
  sfor<4>([&](auto I) { issue(I); });
  {
    const char* xb0 = (const char*)x + (size_t)tt * 32 * 1152 + ng * 288 + u0 * 32;
    const unsigned xo = tok * 1152 + h * 16;
    gload16<0>(xr[0], xb0, xo); gload16<32>(xr[1], xb0, xo);
    gload16<0>(xr[2], xb0 + (w == 0 ? 64 : 0), xo);
    gload4<0>(mxb, ws_in + WS_MX + (size_t)tt * 128, tok * 4);
  }
  sfor<K2_WIN - 4>([&](auto I) { issue(std::integral_constant<int, 4 + decltype(I)::value>{}); });
  u32x4 ofr; unsigned lo[8];
  f32x16 acc[5], c;
  auto ostep = [&](auto UI) {
    constexpr int u = decltype(UI)::value;
    if constexpr (u == 0) pin(pq[0]);
    pin(kvf[2 * u]); pin(kvf[2 * u + 1]); pin(pq[u + 1]);
    c = mfma32(kvf[2 * u], pq[u], (f32x16){});
    c = mfma32(kvf[2 * u + 1], pq[u + 1], c);
  };
  wait_vm<K2_WIN - 4 + 4>();
  ostep(std::integral_constant<int, 0>{});
  sfor<9>([&](auto UI) {
    constexpr int u = decltype(UI)::value;
    if constexpr (u < 8) {
      const float r = __builtin_amdgcn_rcpf(c[10] + 1e-6f);
      ofr = (u32x4){pk2(c[0] * r, c[1] * r), pk2(c[2] * r, c[3] * r), pk2(c[4] * r, c[5] * r), pk2(c[6] * r, c[7] * r)};
      lo[u] = pk2(c[8] * r, c[9] * r);
    } else {
      sfor<4>([&](auto J) { constexpr int j = decltype(J)::value; unsigned a = lo[j], d = lo[4 + j]; pin(a); pin(d); ofr[j] = h ? d : a; });
    }
    sfor<8>([&](auto KI) { issue(std::integral_constant<int, K2_WIN + 8 * u + decltype(KI)::value>{}); });
    constexpr int issued = (K2_WIN + 8 * (u + 1)) < 70 ? (K2_WIN + 8 * (u + 1)) : 70;
    constexpr int need = u < 7 ? 8 * (u + 1) + 4 : (u == 7 ? 65 : 70);
    wait_vm<issued - need>();
    if constexpr (u < 7) ostep(std::integral_constant<int, u + 1>{});
    sfor<5>([&](auto NI) {
      constexpr int i = decltype(NI)::value, n = 5 * u + i;
      pin(wf[n]);
      acc[i] = mfma32(wf[n], ofr, u == 0 ? (f32x16){} : acc[i]);
    });
    sb0();
  });
#pragma unroll
  for (int i = 0; i < 3; ++i) pin(xr[i]);
  pin(mxb);
  const float mx = __uint_as_float(mxb);
  f32x4* xb = (f32x4*)lds;
#pragma unroll
  for (int q = 0; q < 19; ++q) {
    const int i = q >> 2, rb = (q & 3) * 4;
    xb[(w * 19 + q) * 64 + lane] = (f32x4){acc[i][rb], acc[i][rb + 1], acc[i][rb + 2], acc[i][rb + 3]};
  }
  tlmark(31);
#pragma unroll
  for (int i = 0; i < 3; ++i) {
    auto r0 = __builtin_amdgcn_permlane32_swap(xr[i][0], xr[i][2], false, false);
    auto r1 = __builtin_amdgcn_permlane32_swap(xr[i][1], xr[i][3], false, false);
    xr[i] = (u32x4){r0[0], r1[0], r0[1], r1[1]};
  }
  wait_lgkm0(); bar();
  float ms = 0.f;
#pragma unroll
  for (int k = 0; k < 4; ++k) { const f32x4 v = xb[(k * 19 + 18) * 64 + tok]; ms += v[0] + v[1]; }
  const float mean = rbf(mx + ms * (1.f / D_));
  if (ng == 0 && w == 0 && h == 0) ((float*)(ws + WS_MEAN))[tt * 32 + tok] = mean;
  float s1 = 0.f, s2 = 0.f;
#pragma unroll
  for (int i = 0; i < 3; ++i) {
    if (i < 2 || w == 0) {
      const int u = u0 + i;
      f32x4 a = {}, d = {};
#pragma unroll
      for (int k = 0; k < 4; ++k) { a += xb[(k * 19 + 2 * u) * 64 + lane]; d += xb[(k * 19 + 2 * u + 1) * 64 + lane]; }
      const unsigned at[4] = {pk2(a[0], a[1]), pk2(a[2], a[3]), pk2(d[0], d[1]), pk2(d[2], d[3])};
      u32x4 o;
#pragma unroll
      for (int j = 0; j < 4; ++j) {
        const unsigned xv = xr[i][j];
        const unsigned hb = pk2(bflo(xv) + bflo(at[j]), bfhi(xv) + bfhi(at[j]));
        s1 = dot2_ones(hb, s1); s2 = __builtin_amdgcn_fdot2_f32_bf16(as_bf2(hb), as_bf2(hb), s2, false);
        o[j] = pk2(bflo(hb) - mean, bfhi(hb) - mean);
      }
      store16<OUT2_ST>(ws + WS_HF + (((size_t)tt * 36 + ng * 9 + u) * 64 + lane) * 16, o);
    }
  }
  s1 += __shfl_xor(s1, 32); s2 += __shfl_xor(s2, 32);
  if (h == 0) *(f32x2*)(ws + WS_HST + ((size_t)(tt * 32 + tok) * 16 + ng * 4 + w) * 8) = (f32x2){s1, s2};
  asm volatile(".Lfk_text_end:");
}
}
extern "C" void kernel_launch(void* const* d_in, const int* in_sizes, int n_in, void* d_out, int out_size, void* d_ws, size_t ws_size, hipStream_t st) {
  using namespace fk;
  const u16* x = (const u16*)d_in[0]; const u16* wqkv = (const u16*)d_in[1]; const u16* wo = (const u16*)d_in[2];
  const u16* wg = (const u16*)d_in[4]; const u16* wu = (const u16*)d_in[5]; const u16* wd = (const u16*)d_in[6];
  char* ws = (char*)d_ws;
  (void)hipFuncSetAttribute((const void*)k1_qkv, hipFuncAttributeMaxDynamicSharedMemorySize, K1_LDS);
  (void)hipFuncSetAttribute((const void*)k1b_reduce, hipFuncAttributeMaxDynamicSharedMemorySize, K1B_LDS);
  (void)hipFuncSetAttribute((const void*)k2_attn_out, hipFuncAttributeMaxDynamicSharedMemorySize, K2_LDS);
  (void)hipFuncSetAttribute((const void*)k34_ffn, hipFuncAttributeMaxDynamicSharedMemorySize, K3_LDS);
  k1_qkv<<<256, 512, K1_LDS, st>>>(x, wqkv, wo, wg, wu, wd, ws);

  k1b_reduce<<<32, 256, 0, st>>>(wg, wu, wd, ws);
  k2_attn_out<<<256, 256, K2_LDS, st>>>(x, ws, ws);
  k34_ffn<<<256, 512, K3_LDS, st>>>(ws, ws, (u16*)d_out);
}
```

```cpp
#include <hip/hip_runtime.h>
#include <hip/hip_bf16.h>
#include <stdint.h>
#include <type_traits>
#include <utility>

namespace fk {
typedef unsigned short u16;
typedef short bf16x8 __attribute__((ext_vector_type(8)));
typedef short s16x4 __attribute__((ext_vector_type(4)));
typedef float f32x4 __attribute__((ext_vector_type(4)));
typedef float f32x2 __attribute__((ext_vector_type(2)));
typedef float f32x16 __attribute__((ext_vector_type(16)));
typedef unsigned u32x4 __attribute__((ext_vector_type(4)));
typedef unsigned u32x2 __attribute__((ext_vector_type(2)));
typedef __bf16 bf16x2v __attribute__((ext_vector_type(2)));
#define LAS __attribute__((address_space(3)))
#define DI __device__ __forceinline__

constexpr int S_ = 2048, D_ = 576, H_ = 32, DH_ = 18, F_ = 4608;

template <int N, class Fn, int... I> DI void sfor_impl(Fn&& f, std::integer_sequence<int, I...>) { (f(std::integral_constant<int, I>{}), ...); }
template <int N, class Fn> DI void sfor(Fn&& f) { sfor_impl<N>(f, std::make_integer_sequence<int, N>{}); }

template <int N> DI void wait_vm() { asm volatile("s_waitcnt vmcnt(%0)" ::"i"(N) : "memory"); }
DI void wait_lgkm0() { asm volatile("s_waitcnt lgkmcnt(0)" ::: "memory"); }
DI void bar() { asm volatile("" ::: "memory"); __builtin_amdgcn_s_barrier(); asm volatile("" ::: "memory"); }
template <class T> DI void pin(T& v) { asm volatile("" : "+v"(v)); }

DI void glds16(const void* sbase, unsigned voff, unsigned ldsdst) {
  asm volatile("s_mov_b32 m0, %2\n\ts_nop 0\n\tglobal_load_lds_dwordx4 %0, %1" :: "v"(voff), "s"(sbase), "s"(ldsdst) : "memory");
}
DI void glds16x2(const void* sbase, unsigned voff, unsigned ldsdst) {
  asm volatile("s_mov_b32 m0, %2\n\ts_nop 0\n\tglobal_load_lds_dwordx4 %0, %1\n\tglobal_load_lds_dwordx4 %0, %1 offset:1024" :: "v"(voff), "s"(sbase), "s"(ldsdst) : "memory");
}
DI void glds16v(const void* gptr, unsigned ldsdst) {
  asm volatile("s_mov_b32 m0, %1\n\ts_nop 0\n\tglobal_load_lds_dwordx4 %0, off" :: "v"(gptr), "s"(ldsdst) : "memory");
}
DI void sb0() { __builtin_amdgcn_sched_barrier(0); }
template <int OFF> DI void gload16(u32x4& dst, const void* sbase, unsigned voff) {
  asm volatile("global_load_dwordx4 %0, %1, %2 offset:%3" : "=v"(dst) : "v"(voff), "s"(sbase), "i"(OFF) : "memory");
}
template <int OFF> DI void gload8(u32x2& dst, const void* sbase, unsigned voff) {
  asm volatile("global_load_dwordx2 %0, %1, %2 offset:%3" : "=v"(dst) : "v"(voff), "s"(sbase), "i"(OFF) : "memory");
}
template <int OFF> DI void gload4(unsigned& dst, const void* sbase, unsigned voff) {
  asm volatile("global_load_dword %0, %1, %2 offset:%3" : "=v"(dst) : "v"(voff), "s"(sbase), "i"(OFF) : "memory");
}
template <int OFF> DI void gload16a(u32x4& dst, const void* sbase, unsigned voff) {
  asm volatile("global_load_dwordx4 %0, %1, %2 offset:%3" : "=a"(dst) : "v"(voff), "s"(sbase), "i"(OFF) : "memory");
}
DI void l2touch(const void* sbase, unsigned voff, unsigned ldsdummy) {
  asm volatile("s_mov_b32 m0, %2\n\ts_nop 0\n\tglobal_load_lds_dword %0, %1" :: "v"(voff), "s"(sbase), "s"(ldsdummy) : "memory");
}
DI void text_range(const char*& base, unsigned& len) {
  const char* pc; int d0, d1;
  asm volatile("s_getpc_b64 %0\n.Lfkpc%=:\n\ts_mov_b32 %1, .Lfk_text_begin-.Lfkpc%=\n\ts_mov_b32 %2, .Lfk_text_end-.Lfkpc%=" : "=s"(pc), "=s"(d0), "=s"(d1));
  base = pc + d0; len = (unsigned)(d1 - d0);
}
template <class T> DI void pina(T& v) { asm volatile("" : "+a"(v)); }
template <int MODE> DI void store16(void* p, u32x4 v) {
  if constexpr (MODE == 0) *(u32x4*)p = v;
  else if constexpr (MODE == 1) __builtin_nontemporal_store(v, (u32x4*)p);
  else if constexpr (MODE == 2) asm volatile("global_store_dwordx4 %0, %1, off sc1\n\ts_nop 1" :: "v"(p), "v"(v) : "memory");
  else if constexpr (MODE == 3) asm volatile("global_store_dwordx4 %0, %1, off sc0 sc1\n\ts_nop 1" :: "v"(p), "v"(v) : "memory");
  else if constexpr (MODE == 4) asm volatile("global_store_dwordx4 %0, %1, off sc1 nt\n\ts_nop 1" :: "v"(p), "v"(v) : "memory");
  else asm volatile("" :: "v"(v));
}
DI float bf2f(u16 v) { return __uint_as_float(((unsigned)v) << 16); }
DI float bflo(unsigned v) { return __uint_as_float(v << 16); }
DI float bfhi(unsigned v) { return __uint_as_float(v & 0xffff0000u); }
DI unsigned pk2(float lo, float hi) {
  bf16x2v r; r[0] = (__bf16)lo; r[1] = (__bf16)hi; return __builtin_bit_cast(unsigned, r);
}
DI float rbf(float v) { return bflo(pk2(v, 0.f)); }
DI float ln_rstd(float var) { return __builtin_amdgcn_rcpf(rbf(__builtin_amdgcn_sqrtf(rbf(rbf(fmaxf(var, 0.f)) + 1e-5f)))); }
DI s16x4 tr_read(unsigned lds_addr) {
  return __builtin_amdgcn_ds_read_tr16_b64_v4i16((LAS s16x4*)(size_t)lds_addr);
}
DI f32x16 mfma32(u32x4 a, u32x4 b, f32x16 c) { return __builtin_amdgcn_mfma_f32_32x32x16_bf16(__builtin_bit_cast(bf16x8, a), __builtin_bit_cast(bf16x8, b), c, 0, 0, 0); }
DI f32x4 mfma16(u32x4 a, u32x4 b, f32x4 c) { return __builtin_amdgcn_mfma_f32_16x16x32_bf16(__builtin_bit_cast(bf16x8, a), __builtin_bit_cast(bf16x8, b), c, 0, 0, 0); }
#define TLSCOPE(k)
DI void tlmark(int) {}
DI void ckmark(int) {}
DI int uni(int v) { return __builtin_amdgcn_readfirstlane(v); }
}
namespace fk {
DI bf16x2v as_bf2(unsigned v) { return __builtin_bit_cast(bf16x2v, v); }
DI float dot2_ones(unsigned v, float c) { return __builtin_amdgcn_fdot2_f32_bf16(as_bf2(v), as_bf2(0x3f803f80u), c, false); }

constexpr size_t WS_HF   = 0;
constexpr size_t SZ_HF   = (size_t)S_ * D_ * 2;
constexpr size_t WS_HST  = WS_HF + SZ_HF;
constexpr size_t SZ_HST  = (size_t)S_ * 16 * 8;
constexpr size_t WS_WGU  = WS_HST + SZ_HST;
constexpr size_t SZ_WGU  = (size_t)288 * 36 * 1024;
constexpr int    WD_STREAM = 168;
constexpr size_t WS_WD   = WS_WGU + SZ_WGU + 16384;
constexpr size_t SZ_WD   = (size_t)32 * WD_STREAM * 1024;
constexpr size_t WS_ACT  = WS_WD + SZ_WD;
constexpr size_t SZ_ACT  = (size_t)S_ * F_ * 2;
constexpr size_t WS_PART = WS_ACT + SZ_ACT;
constexpr size_t SZ_PART = (size_t)4 * S_ * D_ * 4;
constexpr size_t WS_R2   = WS_PART + SZ_PART;
constexpr size_t WS_MEAN = WS_R2 + S_ * 4;
constexpr size_t WS_CNT  = WS_MEAN + S_ * 4;
constexpr size_t WS_END_FFN = WS_CNT + 512 * 128;
__host__ __device__ constexpr int k4_act(int v) { return v < 10 ? (v / 5) * 9 + (v % 5 < 4 ? v % 5 : 8) : ((v - 10) / 4) * 9 + 4 + (v - 10) % 4; }
DI int k4_virt(int ks) { const int j = ks / 9, p = ks % 9; return p < 4 ? j * 5 + p : (p == 8 ? j * 5 + 4 : 10 + j * 4 + p - 4); }

DI void pack_gu_load(const u16* wg, const u16* wu, int lc, int kp, unsigned ldsb, int lane) {
  const unsigned voff = (unsigned)(((kp * 64 + (lane >> 3)) * F_ + lc * 64 + (lane & 7) * 8) * 2);
  sfor<16>([&](auto I) {
    constexpr int i = decltype(I)::value, s = i >> 3, mat = (i >> 2) & 1, r8 = s * 4 + (i & 3);
    glds16((const char*)(mat ? wu : wg) + (size_t)r8 * 8 * F_ * 2, voff, ldsb + mat * 8192 + r8 * 1024);
  });
}
template <int ST, bool SPLITWAIT> DI void pack_gu_store(char* ws, int lc, int kp, unsigned ldsb, int lane) {
  const int g = lane >> 4;
  const unsigned rbase = ldsb + (16 * (g >> 1) + 4 * (g & 1) + ((lane >> 2) & 3)) * 128 + (lane & 3) * 8;
  char* dst = ws + WS_WGU + ((size_t)(lc * 4) * 18 + kp * 2) * 2048 + lane * 16;
  sfor<2>([&](auto I) {
    constexpr int s = decltype(I)::value;
    if constexpr (SPLITWAIT) wait_vm<8>();
    u32x4 v[8];
    sfor<8>([&](auto J) {
      constexpr int t = decltype(J)::value >> 1, mat = decltype(J)::value & 1;
      u32x2 a = __builtin_bit_cast(u32x2, tr_read(rbase + s * 4096 + mat * 8192 + t * 32)), b = __builtin_bit_cast(u32x2, tr_read(rbase + s * 4096 + mat * 8192 + t * 32 + 1024));
      v[decltype(J)::value] = (u32x4){a[0], a[1], b[0], b[1]};
    });
    sfor<8>([&](auto J) {
      constexpr int t = decltype(J)::value >> 1, mat = decltype(J)::value & 1;
      store16<ST>(dst + (t * 18 + s) * 2048 + mat * 1024, v[decltype(J)::value]);
    });
  });
}
template <int ST> DI void pack_gu_unit(const u16* wg, const u16* wu, char* ws, int lc, int kp, unsigned ldsb, int lane) {
  pack_gu_load(wg, wu, lc, kp, ldsb, lane); pack_gu_store<ST, true>(ws, lc, kp, ldsb, lane);
}
DI void pack_wd_load(const u16* wd, int S, int c2, unsigned ldsb, int lane) {
  const char* base = (const char*)wd + ((size_t)S * 32 * D_ + c2 * 288) * 2;
  sfor<9>([&](auto I) {
    constexpr int n = decltype(I)::value;
    const int c = n * 64 + lane, row = c / 36, cc = c % 36;
    const unsigned vo = (unsigned)(row * D_ * 2 + cc * 16);
    glds16(base, vo, ldsb + n * 1024);
    glds16(base + 16 * D_ * 2, vo, ldsb + 9216 + n * 1024);
  });
}
template <int ST> DI void pack_wd_store(char* ws, int S, int c2, unsigned ldsb, int lane) {
  const int g = lane >> 4, q = (lane >> 2) & 3, p = lane & 3;
  const unsigned rb = ldsb + (16 * (g >> 1) + 4 * (g & 1) + q) * 576;
  const int ksl = S / 36, kh = (S % 36) / 18, ks = S % 18;
  char* dst = ws + WS_WD + ((size_t)((c2 * 2 * 4 + ksl) * 2 + kh) * WD_STREAM + k4_virt(ks) * 9) * 1024 + lane * 16;
  u32x4 v[18];
  sfor<18>([&](auto I) {
    constexpr int ngl = decltype(I)::value / 9, t = decltype(I)::value % 9;
    const unsigned a0 = rb + ngl * 288 + (t < 8 ? 64 * (t >> 1) + 16 * p + 8 * (t & 1) : 256 + 8 * p);
    u32x2 a = __builtin_bit_cast(u32x2, tr_read(a0)), b = __builtin_bit_cast(u32x2, tr_read(a0 + 8 * 576));
    v[decltype(I)::value] = (u32x4){a[0], a[1], b[0], b[1]};
  });
  sfor<18>([&](auto I) {
    constexpr int ngl = decltype(I)::value / 9, t = decltype(I)::value % 9;
    store16<ST>(dst + (size_t)ngl * 8 * WD_STREAM * 1024 + t * 1024, v[decltype(I)::value]);
  });
}
template <int ST> DI void pack_wd_unit(const u16* wd, char* ws, int S, int c2, unsigned ldsb, int lane) {
  pack_wd_load(wd, S, c2, ldsb, lane); wait_vm<0>(); pack_wd_store<ST>(ws, S, c2, ldsb, lane);
}

DI u32x4 load16_sc1(const void* p) { u32x4 v; asm volatile("global_load_dwordx4 %0, %1, off sc1" : "=v"(v) : "v"(p) : "memory"); return v; }
DI u32x2 load8_sc1(const void* p) { u32x2 v; asm volatile("global_load_dwordx2 %0, %1, off sc1" : "=v"(v) : "v"(p) : "memory"); return v; }
DI unsigned load4_sc1(const void* p) { unsigned v; asm volatile("global_load_dword %0, %1, off sc1" : "=v"(v) : "v"(p) : "memory"); return v; }
DI void store8_sc1(void* p, u32x2 v) { asm volatile("global_store_dwordx2 %0, %1, off sc1\n\ts_nop 1" :: "v"(p), "v"(v) : "memory"); }

DI void sig_add(unsigned* p) {
  unsigned long long keep; unsigned one = 1u;
  asm volatile("s_mov_b64 %0, exec\n\ts_mov_b64 exec, 1\n\tglobal_atomic_add %1, %2, off\n\ts_mov_b64 exec, %0" : "=&s"(keep) : "v"(p), "v"(one) : "memory");
}
DI void spin_ge(unsigned& v, const unsigned* p, unsigned need) {
  unsigned t, n;
  asm volatile("s_mov_b32 %2, 0x400000\n\ts_nop 0\n"
               "1:\n\tv_readfirstlane_b32 %1, %0\n\ts_nop 3\n\ts_cmp_ge_u32 %1, %4\n\ts_cbranch_scc1 2f\n\t"
               "s_sub_u32 %2, %2, 1\n\ts_cbranch_scc1 2f\n\ts_sleep 1\n\t"
               "global_load_dword %0, %3, off sc1\n\ts_waitcnt vmcnt(0)\n\ts_branch 1b\n"
               "2:" : "+v"(v), "=&s"(t), "=&s"(n) : "v"(p), "s"(need) : "memory", "scc");
}
template <int OFF> DI void gload16_sc1(u32x4& dst, const void* sbase, unsigned voff) {
  asm volatile("global_load_dwordx4 %0, %1, %2 offset:%3 sc1" : "=v"(dst) : "v"(voff), "s"(sbase), "i"(OFF) : "memory");
}
constexpr int K3_LDS = 160 * 1024;
#define K3_P 4
constexpr int K3_Q = K3_P + 1;
DI void k3_wload(u32x4 (&dst)[4], const char* base, int s, int rot, unsigned voff) {
  const int sa = s + rot >= 9 ? s + rot - 9 : s + rot;
  sfor<4>([&](auto FI) { constexpr int f = decltype(FI)::value; gload16<f * 1024>(dst[f], base + sa * 4096, voff); });
}
template <int NTK, bool FIRST, bool NEXT, int QB, int NST, class HL>
DI void k3_unit(f32x4 (&acc)[2][8], u32x4 (&wq)[K3_Q][4], const char* cur, const char* nxt, const unsigned (&lb)[3], int rot, unsigned voff, HL&& hload) {
  constexpr int NG = NTK / 4;
  u32x4 af[2][4];
  auto aread = [&](auto II) {
    constexpr int it = decltype(II)::value, ks = it / NG, gp = it % NG;
#pragma unroll
    for (int tk = 0; tk < 4; ++tk) af[it & 1][tk] = *(const LAS u32x4*)(size_t)(lb[ks / 6] + ((ks % 6) * 8 + gp * 4 + tk) * 1024);
  };
  aread(std::integral_constant<int, 0>{});
  sfor<9>([&](auto SI) {
    constexpr int s = decltype(SI)::value;
    if constexpr (FIRST) {
      if constexpr (s == 1) { wait_vm<4>(); bar(); }
      if constexpr (s + 2 < 9) k3_wload(wq[(QB + s + 2) % K3_Q], cur, s + 2, rot, voff);
      if constexpr (s < 2) sfor<6>([&](auto HI) { hload(std::integral_constant<int, 6 + 6 * s + decltype(HI)::value>{}); });
      constexpr int n0 = s < 6 ? 0 : (K3_P * (s - 6)) / 3, n1 = s < 6 ? 0 : (K3_P * (s - 5)) / 3;
      constexpr int m0 = K3_P == 2 ? (s < 7 ? 0 : s - 7) : n0, m1 = K3_P == 2 ? (s < 7 ? 0 : s - 6) : n1;
      sfor<m1 - m0>([&](auto JI) { constexpr int j = m0 + decltype(JI)::value; static_assert(9 + j - K3_Q < s, "slot busy"); k3_wload(wq[(QB + 9 + j) % K3_Q], nxt, j, rot, voff); });
      if constexpr (s == 0) wait_vm<2 + 4 + 4 + 6>();
      else if constexpr (s >= 3) wait_vm<(s <= 6 ? 8 : (s == 7 ? 4 : 0)) + 4 * m1>();
      if constexpr (s == 3) bar();
    } else {
      if constexpr (s + K3_P < 9) k3_wload(wq[(QB + s + K3_P) % K3_Q], cur, s + K3_P, rot, voff);
      else if constexpr (NEXT) k3_wload(wq[(QB + s + K3_P) % K3_Q], nxt, s + K3_P - 9, rot, voff);
      constexpr int ny = NEXT ? K3_P : ((8 - s) < K3_P ? (8 - s) : K3_P);
      wait_vm<4 * ny + (s < K3_P ? NST : 0)>();
      if constexpr (!NEXT && s == K3_P) hload(std::integral_constant<int, 0>{});
      if constexpr (!NEXT && s == 6) hload(std::integral_constant<int, 1>{});
    }
    sfor<4>([&](auto FI) { pin(wq[(QB + s) % K3_Q][decltype(FI)::value]); });
    sfor<2 * NG>([&](auto QI) {
      constexpr int q = decltype(QI)::value, it = s * 2 * NG + q, ks = it / NG, gp = it % NG, kq = ks & 1;
      if constexpr (it + 1 < 18 * NG) aread(std::integral_constant<int, it + 1>{});
      sb0();
#pragma unroll
      for (int f = 0; f < 2; ++f)
#pragma unroll
        for (int tk = 0; tk < 4; ++tk)
          acc[f][gp * 4 + tk] = mfma16(wq[(QB + s) % K3_Q][kq * 2 + f], af[it & 1][tk], ks == 0 ? (f32x4){} : acc[f][gp * 4 + tk]);
      sb0();
    });
  });
}

#define K4_LA 4
#define K4_LW 4
constexpr int K4_R = 8, K4_D = 4;
constexpr int K4_LDS = K4_R * 16384;
constexpr int K4_NST = 21;
struct K4Sched {
  int aslot[18], sslot[K4_NST], posA[18], posS[K4_NST], cntBar[K4_NST], cntSlot[162];
  constexpr K4Sched() : aslot{}, sslot{}, posA{}, posS{}, cntBar{}, cntSlot{} {
    for (int a = 0; a < 18; ++a) aslot[a] = a < K4_LA ? -1 : 9 * (a - K4_LA) + 4;
    for (int s = 0; s < K4_NST; ++s) sslot[s] = s == 0 ? -1 : (s < K4_LW ? 0 : s - K4_LW);
    int c = 0;
    for (int s = 0; s < K4_LA; ++s) { c += 2; posA[s] = c; }
    c += 4;
    for (int n = -K4_D; n < 162; ++n) {
      if ((n + K4_D) % 8 == 0 && (n + K4_D) < 162) {
        int i = (n + K4_D) / 8; cntBar[i] = c;
        for (int s = 0; s < K4_NST; ++s) if (sslot[s] == i) { c += 2; posS[s] = c; }
      }
      if (n >= 0) {
        for (int q = 0; q < 18; ++q) if (aslot[q] == n) { c += 2; posA[q] = c; }
        cntSlot[n] = c;
      }
    }
  }
};
constexpr K4Sched k4s{};
#define K4_PISS 5
constexpr int K4_PCHK = 7;
static_assert(8 * K4_PCHK - K4_D <= k4s.aslot[10], "late activations must be requested behind the check");
template <class EPI>
DI void k4_body(const char* __restrict__ ws_in, char* __restrict__ ws_out, u16* __restrict__ out, char* lds, int w, float rs, EPI&& last_epilogue) {
  const int tid = threadIdx.x, lane = tid & 63;
  const int b = blockIdx.x, xcd = b & 7, idx = b >> 3;
  const int ksl = xcd >> 1, mt = (xcd & 1) * 8 + (idx & 7), ng = idx >> 3;
  const int tg = w & 3, kh = w >> 2;
  const int tt = mt * 4 + tg;
  const unsigned ldsb = (unsigned)(size_t)(LAS char*)lds;
  const char* wsrc = ws_in + WS_WD + (size_t)((ng * 4 + ksl) * 2 + kh) * WD_STREAM * 1024 + tg * 2048;
  const char* asrc = ws_in + WS_ACT + ((size_t)tt * 288 + ksl * 72 + kh * 36) * 1024;
  const unsigned voff = lane * 16;
  const int g = lane >> 4;
  const unsigned avoff0 = (g >> 1) * 1024 + (g & 1) * 512 + (lane & 15) * 16 + kh * 256, avoff1 = avoff0 ^ 256;
  const int th = w & 1, a = w >> 1, ott = mt * 4 + ksl, tok = th * 16 + (lane & 15);
  const int col = 32 * a + 8 * g, col8 = 128 + 4 * g;
  u32x2 h0, h1, h2; unsigned mub;
  u32x4 af[K4_LA + 1][2];
  auto issue_stage = [&](auto SI) {
    constexpr int s = decltype(SI)::value;
    glds16x2(wsrc + (size_t)s * 8192, voff, ldsb + (s % K4_R) * 16384 + kh * 8192 + tg * 2048);
  };
  auto issue_a = [&](auto AI) {
    constexpr int a = decltype(AI)::value;
    constexpr int ka = k4_act(a);
    gload16_sc1<(ka % 2) * 2048>(af[a % (K4_LA + 1)][0], asrc + (ka / 2) * 4096, avoff0);
    gload16_sc1<(ka % 2) * 2048>(af[a % (K4_LA + 1)][1], asrc + (ka / 2) * 4096, avoff1);
  };
  sfor<K4_LA>([&](auto AI) { issue_a(AI); });
  last_epilogue();
  f32x4 acc[9][2];
  u32x4 wf[K4_D + 1];
  unsigned* cntb = (unsigned*)(ws_out + WS_CNT);
  const unsigned* c2p = cntb + (384 + mt * 8 + ksl * 2 + kh) * 32;
  unsigned pv2 = 0;
  unsigned lrd0 = ldsb + kh * 8192 + lane * 16, lrd1 = lrd0 + 65536;
  unsigned xw = ldsb + w * 1024 + lane * 16, xw5 = xw + 5 * 16384;
  asm volatile("" : "+v"(lrd0), "+v"(lrd1), "+v"(xw), "+v"(xw5));
  auto xad = [&](unsigned lo, unsigned hi, int t) -> unsigned { return t < 6 ? hi + t * 8192 : lo + (t - 6) * 8192; };
  auto rd = [&](auto NI) {
    constexpr int n = decltype(NI)::value;
    if constexpr (n < 162) {
      constexpr int st = n / 8;
      if constexpr (n % 8 == 0) {
        if constexpr (st > 0) wait_vm<k4s.cntBar[st] - k4s.posS[st]>();
        if constexpr (st == 1) {
          sig_add(cntb + (384 + mt * 8 + ksl * 2 + (ng >> 1)) * 32);
        }
        if constexpr (st == K4_PCHK) {
          wait_vm<k4s.cntBar[K4_PCHK] - k4s.cntBar[K4_PISS]>();
          spin_ge(pv2, c2p, 16u);
        }
        bar();
        if constexpr (st == 0) { tlmark(26); ckmark(5); }
        if constexpr (st == K4_PISS) pv2 = load4_sc1(c2p);
        if constexpr (st == 12) {
          const char* hfp = ws_in + WS_HF + ((size_t)ott * 36 + ng * 9) * 1024;
          auto ho = [&](int c) -> unsigned { return (c >> 4) * 1024 + (((c >> 2) & 1) * 32 + tok) * 16 + ((c >> 3) & 1) * 8; };
          gload8<0>(h0, hfp, ho(col)); gload8<512>(h1, hfp, ho(col)); gload8<0>(h2, hfp, ho(col8));
          gload4<0>(mub, ws_in + WS_MEAN + (size_t)ott * 128, tok * 4);
        }
        sfor<K4_NST>([&](auto SI) { if constexpr (k4s.sslot[decltype(SI)::value] == st) issue_stage(SI); });
      }
      constexpr int off = (st % K4_R) * 16384 + (n % 8) * 1024;
      if constexpr (st == 0) wf[n % (K4_D + 1)] = *(const LAS u32x4*)(size_t)(lrd1 + 81920 + n * 1024);
      else wf[n % (K4_D + 1)] = *(const LAS u32x4*)(size_t)((off < 65536 ? lrd0 : lrd1) + (off & 65535));
    }
  };
  sfor<K4_D>([&](auto I) { rd(I); });
  sfor<162>([&](auto NI) {
    constexpr int n = decltype(NI)::value;
    constexpr int ks = n / 9, t = n % 9, ab = ks % (K4_LA + 1);
    rd(std::integral_constant<int, n + K4_D>{});
    sfor<18>([&](auto AI) { if constexpr (k4s.aslot[decltype(AI)::value] == n) issue_a(AI); });
    if constexpr (t == 0) {
      wait_vm<k4s.cntSlot[n] - k4s.posA[ks]>();
      pin(af[ab][0]); pin(af[ab][1]);
    }
    acc[t][0] = mfma16(wf[n % (K4_D + 1)], af[ab][0], ks == 0 ? (f32x4){} : acc[t][0]);
    acc[t][1] = mfma16(wf[n % (K4_D + 1)], af[ab][1], ks == 0 ? (f32x4){} : acc[t][1]);
    if constexpr (n >= 155) *(LAS f32x4*)(size_t)xad(xw, xw5, n - 155) = acc[n - 155][1];
    sb0();
  });
  tlmark(27); ckmark(6);
  *(LAS f32x4*)(size_t)xad(xw, xw5, 7) = acc[7][1]; *(LAS f32x4*)(size_t)xad(xw, xw5, 8) = acc[8][1];
  wait_lgkm0(); bar();
  tlmark(20);
  const unsigned xr = ldsb + (w ^ 4) * 1024 + lane * 16, xr5 = xr + 5 * 16384;
  f32x4 v[9];
#pragma unroll
  for (int t = 0; t < 9; ++t) v[t] = *(const LAS f32x4*)(size_t)xad(xr, xr5, t) + acc[t][0];
  if (tg == ksl) {
#pragma unroll
    for (int t = 0; t < 9; ++t) *(LAS f32x4*)(size_t)xad(xr, xr5, t) = v[t];
  } else {
    char* pp = ws_out + WS_PART + ((((size_t)ksl * 64 + tt) * 4 + ng) * 2 + kh) * 5120;
#pragma unroll
    for (int i = 0; i < 4; ++i)
      store16<2>(pp + i * 1024 + lane * 16, (u32x4){pk2(v[2 * i][0], v[2 * i][1]), pk2(v[2 * i][2], v[2 * i][3]), pk2(v[2 * i + 1][0], v[2 * i + 1][1]), pk2(v[2 * i + 1][2], v[2 * i + 1][3])});
    store8_sc1(pp + 4096 + lane * 8, (u32x2){pk2(v[8][0], v[8][1]), pk2(v[8][2], v[8][3])});
    wait_vm<0>();
    if (lane == 0) __hip_atomic_fetch_add((unsigned*)(ws_out + WS_CNT) + ((mt * 4 + ng) * 4 + tg) * 32, 1u, __ATOMIC_RELAXED, __HIP_MEMORY_SCOPE_AGENT);
  }
  tlmark(21);
  if (w == ksl) {
    const unsigned* cnt = (const unsigned*)(ws_out + WS_CNT) + ((mt * 4 + ng) * 4 + ksl) * 32;
    unsigned spins = 0;
    while (__hip_atomic_load(cnt, __ATOMIC_RELAXED, __HIP_MEMORY_SCOPE_AGENT) < 6u) {
      __builtin_amdgcn_s_sleep(1);
      if (++spins > (1u << 22)) break;
    }
  }
  wait_lgkm0();
  bar();
  tlmark(28);
  const unsigned ox = ldsb + ((th ^ 1) * 4 + ksl) * 1024 + lane * 16, oa = ox + (a < 3 ? 5 * 16384 + a * 16384 : 0);
  f32x4 s0 = *(const LAS f32x4*)(size_t)oa, s1 = *(const LAS f32x4*)(size_t)(oa + 8192), s2 = *(const LAS f32x4*)(size_t)(ox + 16384);
  const char* pb = ws_in + WS_PART + (((size_t)ott * 4 + ng) * 2 + th) * 5120;
  constexpr size_t KS = (size_t)64 * 4 * 2 * 5120;
  u32x4 pv[3]; unsigned pw[3][2];
#pragma unroll
  for (int k = 0; k < 3; ++k) {
    const char* q = pb + (size_t)((ksl + 1 + k) & 3) * KS;
    pv[k] = load16_sc1(q + a * 1024 + lane * 16);
    const char* q8 = q + (a == 0 ? 4096 + lane * 8 : a * 1024);
    pw[k][0] = load4_sc1(q8); pw[k][1] = load4_sc1(q8 + 4);
  }
  wait_vm<0>();
#pragma unroll
  for (int k = 0; k < 3; ++k) { pin(pv[k]); pin(pw[k][0]); pin(pw[k][1]); }
#pragma unroll
  for (int k = 0; k < 3; ++k) {
    const unsigned a0 = pv[k][0], a1 = pv[k][1], a2 = pv[k][2], a3 = pv[k][3], b0 = pw[k][0], b1 = pw[k][1];
    s0 += (f32x4){bflo(a0), bfhi(a0), bflo(a1), bfhi(a1)}; s1 += (f32x4){bflo(a2), bfhi(a2), bflo(a3), bfhi(a3)}; s2 += (f32x4){bflo(b0), bfhi(b0), bflo(b1), bfhi(b1)};
  }
  pin(h0); pin(h1); pin(h2); pin(mub);
  const float r2 = rs * rs, mu = __uint_as_float(mub);
  u16* orow = out + (size_t)(ott * 32 + tok) * D_ + ng * 144;
  s0 = s0 * r2 + mu; s1 = s1 * r2 + mu; s2 = s2 * r2 + mu;
  u32x4 o;
  o[0] = pk2(s0[0] + bflo(h0[0]), s0[1] + bfhi(h0[0])); o[1] = pk2(s0[2] + bflo(h0[1]), s0[3] + bfhi(h0[1]));
  o[2] = pk2(s1[0] + bflo(h1[0]), s1[1] + bfhi(h1[0])); o[3] = pk2(s1[2] + bflo(h1[1]), s1[3] + bfhi(h1[1]));
  *(u32x4*)(orow + col) = o;
  if (a == 0) *(u32x2*)(orow + col8) = (u32x2){pk2(s2[0] + bflo(h2[0]), s2[1] + bfhi(h2[0])), pk2(s2[2] + bflo(h2[1]), s2[3] + bfhi(h2[1]))};
}
template <int HALF>
DI void k3_body(const char* __restrict__ ws_in, char* __restrict__ ws_out, u16* __restrict__ out, char* lds, int w) {
  const int tid = threadIdx.x, lane = tid & 63;
  const int b = blockIdx.x, xcd = b & 7, idx = b >> 3;
  const int rt = (xcd & 1) * 8 + (idx & 7), fgp = (xcd >> 1) * 4 + (idx >> 3);
  const unsigned ldsb = (unsigned)(size_t)(LAS char*)lds;
  const unsigned voff = lane * 16;
  const int g = lane >> 4, l15 = lane & 15;
  const unsigned avoff = (g >> 1) * 1024 + (g & 1) * 512 + l15 * 16;
  const char* hsrc = ws_in + WS_HF + (size_t)(rt * 4 + (w >> 1)) * 36 * 1024 + (w & 1) * 256;
#define K3_ROT 0
  const int rot = K3_ROT;
  auto hload = [&](auto I) { constexpr int i = decltype(I)::value; const int ia = i + 2 * rot >= 18 ? i + 2 * rot - 18 : i + 2 * rot;
    glds16(hsrc + ia * 2048, avoff, ldsb + (i * 8 + w) * 1024); };
  auto nohl = [&](auto) {};
  const int t0 = w, t1 = w + 8, t2 = 16 + ((w >> 1) & 1);
  const char* wgu = ws_in + WS_WGU + (size_t)fgp * 18 * 36 * 1024;
  const char* sA = wgu + (size_t)t0 * 36 * 1024;
  const char* sB = wgu + (size_t)t1 * 36 * 1024;
  const char* sC = wgu + (size_t)t2 * 36 * 1024;
  u32x4 wq[K3_Q][4];
  u32x4 stv[2];
  {
    const char* sp = ws_in + WS_HST + (size_t)(rt * 128 + w * 16) * 128;
    gload16<0>(stv[0], sp, l15 * 128 + g * 32); gload16<16>(stv[1], sp, l15 * 128 + g * 32);
  }
  sfor<4>([&](auto I) { hload(I); });
  k3_wload(wq[0], sA, 0, rot, voff);
  hload(std::integral_constant<int, 4>{}); hload(std::integral_constant<int, 5>{});
  k3_wload(wq[1], sA, 1, rot, voff);
  wait_vm<10>();
  float* ctab = (float*)(lds + 144 * 1024);
  {
    pin(stv[0]); pin(stv[1]);
    float sm = (__uint_as_float(stv[0][0]) + __uint_as_float(stv[0][2])) + (__uint_as_float(stv[1][0]) + __uint_as_float(stv[1][2]));
    float sq = (__uint_as_float(stv[0][1]) + __uint_as_float(stv[0][3])) + (__uint_as_float(stv[1][1]) + __uint_as_float(stv[1][3]));
    sm += __shfl_xor(sm, 16); sq += __shfl_xor(sq, 16);
    sm += __shfl_xor(sm, 32); sq += __shfl_xor(sq, 32);
    const float mean = sm * (1.f / D_);
    const float rstd = ln_rstd(sq * (1.f / D_) - mean * mean);
    if (lane < 16) {
      ctab[w * 16 + lane] = -1.4426950408889634f * rstd;
    }
  }
  wait_lgkm0();
  bar();
  float c1[8];
#pragma unroll
  for (int tk = 0; tk < 8; ++tk) c1[tk] = ctab[tk * 16 + l15];
  const float rs_out = ctab[(xcd >> 1) * 32 + (w & 1) * 16 + l15] * 0.6931471805599453f;
  tlmark(24); ckmark(0);
  unsigned lb[3] = {ldsb + voff, ldsb + voff + 49152, ldsb + voff + 98304};
  unsigned lh[3] = {lb[0] + (w & 1) * 4096, lb[1] + (w & 1) * 4096, lb[2] + (w & 1) * 4096};
  asm volatile("" : "+v"(lb[0]), "+v"(lb[1]), "+v"(lb[2]), "+v"(lh[0]), "+v"(lh[1]), "+v"(lh[2]));
  f32x4 acc[2][8];
  auto swiglu = [&](const f32x4& a, const f32x4& u, float c) -> u32x2 {
    float r[4];
#pragma unroll
    for (int j = 0; j < 4; ++j) { float e = __builtin_amdgcn_exp2f(a[j] * c); float pr = a[j] * u[j]; pin(pr); r[j] = pr * __builtin_amdgcn_rcpf(1.f + e); pin(r[j]); }
    return (u32x2){pk2(r[0], r[1]), pk2(r[2], r[3])};
  };
  char* actp = ws_out + WS_ACT + ((size_t)rt * 4 * 288 + fgp * 18) * 1024 + ((g & 1) * 32 + (g >> 1) * 16 + l15) * 16;
  auto epi2 = [&](int T, int tile32, const f32x4& g0, const f32x4& u0, float ca, const f32x4& g1, const f32x4& u1, float cb) {
    const u32x2 x0 = swiglu(g0, u0, ca), x1 = swiglu(g1, u1, cb);
    auto r0 = __builtin_amdgcn_permlane32_swap(x0[0], x1[0], false, false);
    auto r1 = __builtin_amdgcn_permlane32_swap(x0[1], x1[1], false, false);
    store16<2>(actp + ((size_t)tile32 * 288 + T) * 1024, (u32x4){r0[0], r1[0], r0[1], r1[1]});
  };
  auto epi_full = [&](int T) {
#pragma unroll
    for (int i = 0; i < 4; ++i) epi2(T, i, acc[0][2 * i], acc[1][2 * i], c1[2 * i], acc[0][2 * i + 1], acc[1][2 * i + 1], c1[2 * i + 1]);
  };
  k3_unit<8, true, true, 0, 0>(acc, wq, sA, HALF == 0 ? sC : sB, lb, rot, voff, hload);
  tlmark(25); ckmark(1);
  epi_full(t0);
  ckmark(2);
  if constexpr (HALF == 0) {
    k3_unit<4, false, true, 9 % K3_Q, 4>(acc, wq, sC, sB, lh, rot, voff, nohl);
    const bool hi = w & 1;
#pragma unroll
    for (int i = 0; i < 2; ++i) {
      float a0 = c1[2 * i], a1 = c1[4 + 2 * i], b0 = c1[2 * i + 1], b1 = c1[5 + 2 * i]; pin(a0); pin(a1); pin(b0); pin(b1);
      const float ca = hi ? a1 : a0, cb = hi ? b1 : b0;
      epi2(t2, (w & 1) * 2 + i, acc[0][2 * i], acc[1][2 * i], ca, acc[0][2 * i + 1], acc[1][2 * i + 1], cb);
    }
  }
  unsigned* cnt = (unsigned*)(ws_out + WS_CNT);
  const unsigned* c1p = cnt + (256 + rt * 8 + (fgp >> 2) * 2 + HALF) * 32;
  unsigned pv = 0;
  auto hook = [&](auto I) {
    if constexpr (decltype(I)::value == 0) sig_add(cnt + (256 + rt * 8 + (fgp >> 1)) * 32);
    else {
      glds16x2(ws_in + WS_WD + (size_t)(((idx >> 3) * 4 + (xcd >> 1)) * 2 + HALF) * WD_STREAM * 1024 + (w & 3) * 2048, voff, ldsb + 144 * 1024 + HALF * 8192 + (w & 3) * 2048);
      pv = load4_sc1(c1p);
    }
  };
  k3_unit<8, false, false, (HALF == 0 ? 18 : 9) % K3_Q, HALF == 0 ? 2 : 4>(acc, wq, sB, sB, lb, rot, voff, hook);
  tlmark(29); ckmark(3);
  wait_vm<0>();
  spin_ge(pv, c1p, 16u);
  k4_body(ws_in, ws_out, out, lds, w, rs_out, [&]() { epi_full(t1); ckmark(4); });
}

__global__ __launch_bounds__(512, 2) void k34_ffn(const char* __restrict__ ws_in, char* __restrict__ ws_out, u16* __restrict__ out) {
  asm volatile(".Lfk_text_begin:");
  TLSCOPE(3);
  extern __shared__ __attribute__((aligned(16))) char lds[];
  const int w = uni(threadIdx.x >> 6);
  if (w < 4) k3_body<0>(ws_in, ws_out, out, lds, w); else k3_body<1>(ws_in, ws_out, out, lds, w);
}
}
#define OUT_ST 2
#define OUT2_ST 0
#define GU_ST 2
#define PACK_ST 2
namespace fk {
constexpr size_t WS_PQ   = (WS_END_FFN + 4095) / 4096 * 4096;
constexpr size_t WS_KVP  = WS_PQ + SZ_HF;
constexpr size_t SZ_KVP  = (size_t)16 * 8 * 16 * 1024;
constexpr size_t WS_KVF  = WS_KVP + SZ_KVP;
constexpr size_t SZ_KVF  = 64 * 1024;
constexpr size_t WS_WOP  = WS_KVF + SZ_KVF;
constexpr size_t SZ_WOP  = (size_t)4 * 4 * 9 * 5 * 1024;
constexpr size_t WS_MX   = WS_WOP + SZ_WOP;
constexpr size_t WS_END  = WS_MX + S_ * 4;

DI float phi_f(float t) { return (t > 0.f ? t + 1.f : __builtin_amdgcn_exp2f(t * 1.4426950408889634f)) + 1e-6f; }

DI void pack_wo_task(const u16* wo, char* ws, int id) {
  const int lane = id & 63, fi = id >> 6, i = fi % 5, u = (fi / 5) % 9, w = (fi / 45) & 3, ng = fi / 180;
  const int row = lane & 31, h = lane >> 5;
  if (i == 4 && row >= 16) {
    if (row >= 18) *(u32x4*)(ws + WS_WOP + (size_t)id * 16) = (u32x4){0u, 0u, 0u, 0u};
    return;
  }
  const int n = ng * 144 + i * 32 + row;
  unsigned v[8];
#pragma unroll
  for (int j = 0; j < 8; ++j) {
    const int e = u < 8 ? 18 * (8 * w + u) + 8 * (j >> 2) + 4 * h + (j & 3) : 18 * (8 * w + 4 * h + (j >> 1)) + 16 + (j & 1);
    v[j] = wo[e * D_ + n];
  }
  store16<OUT_ST>(ws + WS_WOP + (size_t)id * 16, (u32x4){v[0] | (v[1] << 16), v[2] | (v[3] << 16), v[4] | (v[5] << 16), v[6] | (v[7] << 16)});
}
DI void pack_worow_task(const u16* wo, char* ws, int e, int lane) {
  float s = 0.f;
#pragma unroll
  for (int i = 0; i < 9; ++i) s += bf2f(wo[e * D_ + i * 64 + lane]);
#pragma unroll
  for (int o = 32; o; o >>= 1) s += __shfl_xor(s, o);
  const unsigned hi = pk2(s, 0.f) & 0xffffu; const unsigned lo = pk2(s - bflo(hi), 0.f) & 0xffffu;
  const int hh = e / 18, el = e % 18, w = hh >> 3, ul = hh & 7;
  int u, h, j;
  if (el < 16) { u = ul; h = (el >> 2) & 1; j = (el >> 3) * 4 + (el & 3); } else { u = 8; h = ul >> 2; j = (ul & 3) * 2 + (el - 16); }
  if (lane < 8) {
    const int ng = lane >> 1, part = lane & 1;
    u16* p = (u16*)(ws + WS_WOP) + ((((size_t)(ng * 4 + w) * 9 + u) * 5 + 4) * 64 + h * 32 + 16 + part) * 8 + j;
    *p = (u16)(part ? lo : hi);
  }
}

constexpr int K1_WST = 18432, K1_STG = K1_WST + 16384, K1_R = 4;
constexpr int K1_TAB = K1_STG * K1_R;
constexpr int K1_LDS = 8 * 18432;
static_assert(K1_TAB + 4 * 144 * 4 + 1024 <= K1_LDS, "lds");
#define K1_PORD 1
#define K1_NGU 64
#define K1_NOPACK 0
#define K1_DD 3
constexpr int K1_D = K1_DD;
DI float phi3(float u, float rs, float rl) { return fmaxf(fmaf(u, rs, 1e-6f), 1e-6f) + __builtin_amdgcn_exp2f(fminf(u * rl, 0.f)); }
template <bool ISQ, int NH> constexpr int k1_tile(int j) {
  if (ISQ) return NH ? 5 + j : j;
  if (NH == 0) return j < 2 ? j : (j == 2 ? 4 : j + 2);
  return j < 2 ? 2 + j : 5 + j;
}
template <bool ISQ> constexpr int k1_toff(int t) { return ISQ ? 32 * t : (t < 4 ? 32 * t : 144 + 32 * (t - 5)); }

__host__ __device__ constexpr int k1_img(int t) { return t < 8 ? t * 4096 : 3 * K1_STG + (t - 8) * 4096; }
template <bool ISQ, int NH, class ISSUE, class TAIL>
DI void k1_body(char* lds, unsigned ldsb, int lane, int tg, int rt, int grp, char* ws, ISSUE&& issue, TAIL&& tail) {
  constexpr int NT = NH ? 4 : 5, NWF = NT + K1_D;
  const int g = lane >> 4, l15 = lane & 15, q4 = (lane >> 2) & 3, p4 = lane & 3;
  const unsigned rrow = ldsb + (16 * (g >> 1) + 4 * (g & 1) + q4) * 288;
  unsigned rbA[2], rbB[2], rbX[2][2];
  rbA[0] = rrow + p4 * 8;
  rbB[0] = rrow + (p4 < 2 ? 128 + 8 * p4 : 272 + 8 * (p4 - 2));
  rbA[1] = rbA[0] + 2 * K1_STG; rbB[1] = rbB[0] + 2 * K1_STG;
  const unsigned xb = ldsb + K1_WST + tg * 4096 + l15 * 128;
#pragma unroll
  for (int kh = 0; kh < 2; ++kh) { rbX[kh][0] = xb + (((4 * kh + g) ^ ((l15 >> 1) & 7)) * 16); rbX[kh][1] = rbX[kh][0] + 2 * K1_STG; }
  asm volatile("" : "+v"(rbA[0]), "+v"(rbA[1]), "+v"(rbB[0]), "+v"(rbB[1]), "+v"(rbX[0][0]), "+v"(rbX[0][1]), "+v"(rbX[1][0]), "+v"(rbX[1][1]));
  u32x4 ones = {0x3f803f80u, 0x3f803f80u, 0x3f803f80u, 0x3f803f80u};
  pin(ones);
  f32x4 acc[NT][2], accs[NT], sst[2][2];
#pragma unroll
  for (int t = 0; t < NT; ++t) accs[t] = (f32x4){};
  u32x4 xf[2][2], wf[NWF];
  auto rd = [&](auto NI) {
    constexpr int n = decltype(NI)::value;
    if constexpr (n < 18 * NT) {
      constexpr int ks = n / NT, j = n % NT, st = ks >> 1, kh = ks & 1, slot = st % K1_R, hb = slot >> 1, so = (slot & 1) * K1_STG;
      constexpr int t = k1_tile<ISQ, NH>(j);
      constexpr int pos = n % (2 * NT);
      constexpr int nxt = st == 0 ? 3 : (st >= 2 && st + 2 < 9 ? st + 2 : -1);
      if constexpr (pos == 0) {
        constexpr int upto = st == 0 ? 2 : (st + 1 > 3 ? (st + 1 < 8 ? st + 1 : 8) : 3);
        if constexpr (NH == 1 && st == 8) tail(std::integral_constant<int, 0>{});
        else wait_vm<4 * (upto - st)>();
        bar();
        if constexpr (st == 0) tlmark(17);
        if constexpr (NH == 1 && st >= 7) tail(std::integral_constant<int, st>{});
      }
      if constexpr (j == 0) {
        xf[kh][0] = *(const LAS u32x4*)(size_t)(rbX[kh][hb] + so);
        xf[kh][1] = *(const LAS u32x4*)(size_t)(rbX[kh][hb] + so + 2048);
      }
      const unsigned a = ((!ISQ && t == 4) ? rbB[hb] : rbA[hb] + k1_toff<ISQ>(t)) + so + kh * 9216;
      u32x2 lo = __builtin_bit_cast(u32x2, tr_read(a)), hi = __builtin_bit_cast(u32x2, tr_read(a + 8 * 288));
      wf[n % NWF] = (u32x4){lo[0], lo[1], hi[0], hi[1]};
      if constexpr (nxt >= 0 && (pos & 1) == 1 && pos / 2 < 4) issue(std::integral_constant<int, nxt>{}, std::integral_constant<int, pos / 2>{});
    }
  };
  sfor<K1_D>([&](auto I) { rd(I); });
  sfor<18 * NT>([&](auto NI) {
    constexpr int n = decltype(NI)::value, ks = n / NT, j = n % NT, kh = ks & 1;
    rd(std::integral_constant<int, n + K1_D>{});
    const u32x4 wv = wf[n % NWF];
    sfor<2>([&](auto HI) {
      constexpr int th = decltype(HI)::value;
      const f32x4 c = ks == 0 ? (f32x4){} : acc[j][th];
      acc[j][th] = ISQ ? mfma16(wv, xf[kh][th], c) : mfma16(xf[kh][th], wv, c);
    });
    if constexpr (NH == 1 && j < 2) {
      sst[j][0] = mfma16(ones, xf[kh][j], ks == 0 ? (f32x4){} : sst[j][0]);
      sst[j][1] = mfma16(xf[kh][j], xf[kh][j], ks == 0 ? (f32x4){} : sst[j][1]);
    }
    if constexpr (j == NT - 1) {
      if (tg == (ks & 3)) {
        sfor<NT>([&](auto JI) { constexpr int jj = decltype(JI)::value; accs[jj] = mfma16(ones, wf[(ks * NT + jj) % NWF], accs[jj]); });
      }
    }
    sb0();
  });
  tlmark(16);
  float* tab = (float*)(lds + K1_TAB);
  float* stt = tab + 4 * 144;
  if (g == 0) {
    sfor<NT>([&](auto JI) { constexpr int jj = decltype(JI)::value; tab[tg * 144 + k1_tile<ISQ, NH>(jj) * 16 + l15] = accs[jj][0]; });
  }
  if constexpr (NH == 1) {
    if (g == (l15 >> 2)) {
#pragma unroll
      for (int th = 0; th < 2; ++th) {
        const int r = l15 & 3;
        const float q = r == 0 ? sst[th][1][0] : (r == 1 ? sst[th][1][1] : (r == 2 ? sst[th][1][2] : sst[th][1][3]));
        const float mean = sst[th][0][0] * (1.f / D_);
        stt[tg * 32 + th * 16 + l15] = rbf(mean);
        if (ISQ && grp == 0) ((float*)(ws + WS_MX))[rt * 128 + tg * 32 + th * 16 + l15] = mean;
        stt[128 + tg * 32 + th * 16 + l15] = ln_rstd(q * (1.f / D_) - mean * mean);
      }
    }
  }
  wait_lgkm0(); bar();
  constexpr float L2E = 1.4426950408889634f;
  if constexpr (ISQ) {
    const int tt = rt * 4 + tg;
    float nm[2], rs[2], rl[2];
#pragma unroll
    for (int th = 0; th < 2; ++th) { nm[th] = -stt[tg * 32 + th * 16 + l15]; rs[th] = stt[128 + tg * 32 + th * 16 + l15]; rl[th] = rs[th] * L2E; }
    char* pq = ws + WS_PQ + ((size_t)tt * 36 + grp * 9) * 1024 + ((g & 1) * 32 + (g >> 1) * 16 + l15) * 16;
    sfor<NT>([&](auto JI) {
      constexpr int j = decltype(JI)::value, t = k1_tile<ISQ, NH>(j);
      const float* cp = tab + t * 16 + g * 4;
      const f32x4 cs = (*(const f32x4*)cp + *(const f32x4*)(cp + 144)) + (*(const f32x4*)(cp + 288) + *(const f32x4*)(cp + 432));
      unsigned pk[2][2];
#pragma unroll
      for (int th = 0; th < 2; ++th) {
        float v[4];
#pragma unroll
        for (int e = 0; e < 4; ++e) v[e] = phi3(fmaf(nm[th], cs[e], acc[j][th][e]), rs[th], rl[th]);
        pk[th][0] = pk2(v[0], v[1]); pk[th][1] = pk2(v[2], v[3]);
      }
      auto r0 = __builtin_amdgcn_permlane32_swap(pk[0][0], pk[1][0], false, false);
      auto r1 = __builtin_amdgcn_permlane32_swap(pk[0][1], pk[1][1], false, false);
      store16<OUT_ST>(pq + t * 1024, (u32x4){r0[0], r1[0], r0[1], r1[1]});
    });
  } else {
    f32x4 mn[2], rs[2], rl[2];
#pragma unroll
    for (int th = 0; th < 2; ++th) { mn[th] = *(const f32x4*)(stt + tg * 32 + th * 16 + g * 4); rs[th] = *(const f32x4*)(stt + 128 + tg * 32 + th * 16 + g * 4); rl[th] = rs[th] * L2E; }
    char* ob = lds + (tg * 64 + lane) * 16;
    sfor<NT>([&](auto JI) {
      constexpr int j = decltype(JI)::value, t = k1_tile<ISQ, NH>(j);
      const float* cp = tab + t * 16 + l15;
      const float ncs = -((cp[0] + cp[144]) + (cp[288] + cp[432]));
      u32x4 pv;
#pragma unroll
      for (int th = 0; th < 2; ++th) {
        float v[4];
#pragma unroll
        for (int e = 0; e < 4; ++e) {
          const float u = fmaf(mn[th][e], ncs, acc[j][th][e]);
          if (t < 4) v[e] = phi3(u, rs[th][e], rl[th][e]);
          else if (t > 4) v[e] = u * rs[th][e];
          else v[e] = l15 < 8 ? phi3(u, rs[th][e], rl[th][e]) : u * rs[th][e];
        }
        pv[2 * th] = pk2(v[0], v[1]); pv[2 * th + 1] = pk2(v[2], v[3]);
      }
      *(u32x4*)(ob + k1_img(t)) = pv;
      if constexpr (t == 4) *(u32x4*)(ob + k1_img(9)) = l15 < 8 ? ones : pv;
    });
    wait_lgkm0(); bar();

    const int w = NH * 4 + tg;
#pragma unroll
    for (int pi = 0; pi < 2; ++pi) {
      const int p = 2 * w + pi;
      int a, bt;
      if (p < 10) { a = (p + 1) / 3; bt = 5 + a + ((p + 1) % 3) - 1; } else if (p == 10) { a = 4; bt = 8; } else { a = p - 11; bt = 9; }
      f32x4 c = {};
#pragma unroll
      for (int i = 0; i < 4; ++i) {
        const u32x4 av = *(const u32x4*)(lds + a * 4096 + (i * 64 + lane) * 16);
        const u32x4 bv = *(const u32x4*)(lds + k1_img(bt) + (i * 64 + lane) * 16);
        c = mfma16(av, bv, c);
      }
      if constexpr (OUT_ST != 0) asm volatile("s_nop 9" : "+v"(c));
      store16<OUT_ST>(ws + WS_KVP + (((size_t)rt * 8 + grp) * 16 + p) * 1024 + lane * 16, __builtin_bit_cast(u32x4, c));
    }
  }
}

__global__ __launch_bounds__(512, 2) void k1_qkv(const u16* __restrict__ x, const u16* __restrict__ wqkv, const u16* __restrict__ wo,
                                                  const u16* __restrict__ wg, const u16* __restrict__ wu, const u16* __restrict__ wd, char* __restrict__ ws) {
  TLSCOPE(0);
  extern __shared__ __attribute__((aligned(16))) char lds[];
  const int tid = threadIdx.x, lane = tid & 63, w = uni(tid >> 6);
  const int b = blockIdx.x, xcd = b & 7, idx = b >> 3;
  const unsigned ldsb = (unsigned)(size_t)(LAS char*)lds;
  if (idx >= 24) {
    {
      const char* c0; unsigned len; text_range(c0, len);
      const unsigned o = (unsigned)((((idx - 24) * 8 + w) * 64 + lane) * 128);
      if ((unsigned)(((idx - 24) * 8 + w) * 8192) < len) l2touch(c0, o < len ? o : 0u, ldsb);
    }
    const int pw = ((idx - 24) * 8 + xcd) * 8 + w;
    if (pw < 8) ((unsigned*)(ws + WS_CNT))[(pw * 64 + lane) * 32] = 0u;
    const int u = (idx - 24) * 8 + w;
    pack_gu_load(wg, wu, xcd * 9 + u % 9, u / 9, ldsb + w * 16384 + 256, lane);
    for (int id = pw * 64 + lane; id < 4 * 4 * 9 * 5 * 64; id += 512 * 64) pack_wo_task(wo, ws, id);
    for (int e = pw; e < D_; e += 512) pack_worow_task(wo, ws, e, lane);
    tlmark(18);
    wait_vm<0>();
    pack_gu_store<GU_ST, false>(ws, xcd * 9 + u % 9, u / 9, ldsb + w * 16384 + 256, lane);
    tlmark(19);
    return;
  }
  const int rt = (xcd & 3) * 4 + (idx & 3), cgl = idx >> 2;
  const int ch = xcd >> 2;
  const bool isq = cgl < 2;
  const int grp = isq ? ch * 2 + cgl : ch * 4 + (cgl - 2);
  auto wsrc_off = [&](int n) -> unsigned {
    const int c = n * 64 + lane, rho = c / 18, cc = c % 18;
    const int k = (rho & ~12) | ((rho & 4) << 1) | ((rho & 8) >> 1);
    const int col = isq ? 144 * grp + 8 * cc : (cc < 9 ? 576 + 72 * grp + 8 * cc : 1152 + 72 * grp + 8 * (cc - 9));
    return (unsigned)((k * 1728 + col) * 2);
  };
  const char* xsrc = (const char*)x + (size_t)(rt * 128 + w * 16) * 1152;
  const unsigned xoff0 = (lane >> 3) * 1152 + (((lane & 7) ^ (lane >> 4)) * 16);
  const unsigned xoff1 = ((lane >> 3) + 8) * 1152 + (((lane & 7) ^ (4 + (lane >> 4))) * 16);
#define K1_ROT (((idx & 3) * 2 + cgl) % 9)
  const int rot = K1_ROT;
  glds16(xsrc + rot * 128, xoff0, ldsb + K1_WST + w * 2048);
  glds16(xsrc + rot * 128, xoff1, ldsb + K1_WST + w * 2048 + 1024);
  const unsigned woffA = wsrc_off(w), woffB = wsrc_off(w + 8), woffC = wsrc_off(16 + (w & 1));
  auto issue = [&](auto SI, auto PI) {
    constexpr int st = decltype(SI)::value, slot = st % K1_R, pc = decltype(PI)::value;
    const int sa = st + rot >= 9 ? st + rot - 9 : st + rot;
    const char* wb = (const char*)wqkv + (size_t)sa * 64 * 3456;
    const unsigned sb = ldsb + slot * K1_STG;
    if constexpr (pc == 0) glds16(xsrc + sa * 128, xoff0, sb + K1_WST + w * 2048);
    if constexpr (pc == 1) glds16(xsrc + sa * 128, xoff1, sb + K1_WST + w * 2048 + 1024);
    if constexpr (pc == 2) { if (w < 2) glds16(wb, woffC, sb + (16 + w) * 1024); glds16(wb, woffA, sb + w * 1024); }
    if constexpr (pc == 3) glds16(wb, woffB, sb + (w + 8) * 1024);
  };
  issue(std::integral_constant<int, 0>{}, std::integral_constant<int, 2>{});
  issue(std::integral_constant<int, 0>{}, std::integral_constant<int, 3>{});
  bar();
  sfor<4>([&](auto PI) { issue(std::integral_constant<int, 1>{}, PI); });
  sfor<4>([&](auto PI) { issue(std::integral_constant<int, 2>{}, PI); });
  const int tg = w & 3;
  const int pslot = w == 5 ? idx : (w == 6 ? (idx < 21 ? 24 + idx : 99) : ((w == 7 && isq) ? 45 + idx : 99));
  const unsigned pl = ldsb + (w == 6 ? 2 * K1_STG : K1_STG + (w == 7 ? 18432 : 0));
  auto tail = [&](auto SI) {
    constexpr int st = decltype(SI)::value;
    if constexpr (st == 0) { if (w == 5) wait_vm<18>(); else if (pslot < 99 && w == 7) wait_vm<16>(); else wait_vm<0>(); }
    else if constexpr (st == 9) {
      wait_vm<0>();
      if (pslot < 36) pack_wd_store<PACK_ST>(ws, xcd * 18 + (pslot >> 1), pslot & 1, pl, lane);
      else if (pslot < 53) pack_gu_store<GU_ST, false>(ws, xcd * 9 + (K1_NGU + pslot - 36) % 9, (K1_NGU + pslot - 36) / 9, pl, lane);
    } else if ((w == 6) == (st == 8)) {
      if (pslot < 36) pack_wd_load(wd, xcd * 18 + (pslot >> 1), pslot & 1, pl, lane);
      else if (pslot < 53) pack_gu_load(wg, wu, xcd * 9 + (K1_NGU + pslot - 36) % 9, (K1_NGU + pslot - 36) / 9, pl, lane);
    }
  };
  static_assert(K1_NGU == 64, "tail slot map");
  if (isq) { if (w < 4) k1_body<true, 0>(lds, ldsb, lane, tg, rt, grp, ws, issue, tail); else k1_body<true, 1>(lds, ldsb, lane, tg, rt, grp, ws, issue, tail); }
  else     { if (w < 4) k1_body<false, 0>(lds, ldsb, lane, tg, rt, grp, ws, issue, tail); else k1_body<false, 1>(lds, ldsb, lane, tg, rt, grp, ws, issue, tail); }
  tlmark(30);
  tail(std::integral_constant<int, 9>{});
}

constexpr int K1B_LDS = 4 * 18432;
__global__ __launch_bounds__(256) void k1b_reduce(const u16* __restrict__ wg, const u16* __restrict__ wu, const u16* __restrict__ wd, char* __restrict__ ws) {
  TLSCOPE(1);
  extern __shared__ __attribute__((aligned(16))) char lds[];
  const int tid = threadIdx.x;
  const int b = blockIdx.x;
  const int hh = b, s = tid >> 7, l = (tid >> 1) & 63, r = tid & 1;
  const int row = l & 31, h = l >> 5;
  const int T = (18 * hh) / 16 + s;
  const int d0 = 16 * T + 8 * r + 4 * h;
  const int G = hh >> 2, hl = hh & 3;
  int el = -1; bool isz = false;
  if (row < 16) el = row;
  else if (row == 18 || row == 22) isz = true;
  else if ((hh & 7) < 4 ? (row == 16 || row == 17) : (row == 20 || row == 21)) el = 16 + (row & 1);
  const int dlo = 18 * hh, dhi = dlo + 18;
  f32x4 sum = {};
  if ((el >= 0 || isz) && d0 + 3 >= dlo && d0 < dhi) {
    const int dk = d0 - 72 * G, a = dk >> 4, dr = dk & 15;
    int p, col;
    if (isz) { p = 11 + a; col = 0; }
    else {
      const int ev = 18 * hl + el;
      if (ev >= 64) { p = 11 + a; col = 8 + ev - 64; }
      else { const int vt = ev >> 4; col = ev & 15; p = a == 4 ? 10 : 3 * a + (vt - a); }
    }
    const char* src = ws + WS_KVP + ((size_t)G * 16 + p) * 1024 + ((dr >> 2) * 16 + col) * 16;
    f32x4 v[16];
#pragma unroll
    for (int i = 0; i < 16; ++i) v[i] = *(const f32x4*)(src + (size_t)i * 8 * 16 * 1024);
#pragma unroll
    for (int i = 0; i < 16; ++i) sum += v[i];
#pragma unroll
    for (int j = 0; j < 4; ++j) if (d0 + j < dlo || d0 + j >= dhi) sum[j] = 0.f;
  }
  *(u32x2*)(ws + WS_KVF + ((size_t)(hh * 2 + s) * 64 + l) * 16 + r * 8) = (u32x2){pk2(sum[0], sum[1]), pk2(sum[2], sum[3])};
}

#define K2_WIN 16
constexpr int K2_LDS = 4 * 19 * 1024;
__global__ __launch_bounds__(256, 1) void k2_attn_out(const u16* __restrict__ x, const char* __restrict__ ws_in, char* __restrict__ ws) {
  TLSCOPE(2);
  extern __shared__ __attribute__((aligned(16))) char lds[];
  const int tid = threadIdx.x, lane = tid & 63, w = uni(tid >> 6);
  const int b = blockIdx.x, xcd = b & 7, idx = b >> 3;
  const int tt = xcd * 8 + (idx & 7), ng = idx >> 3;
  const int tok = lane & 31, h = lane >> 5;
  const char* pqp = ws_in + WS_PQ + ((size_t)tt * 36 + w * 9) * 1024;
  const char* kvp = ws_in + WS_KVF + (size_t)w * 16 * 1024;
  const char* wop = ws_in + WS_WOP + (size_t)(ng * 4 + w) * 45 * 1024;
  const unsigned voff = lane * 16, voffc = (tok < 24 ? lane : (lane & 32)) * 16;
  u32x4 pq[9], kvf[16], wf[45];
  auto issue = [&](auto II) {
    constexpr int i = decltype(II)::value;
    if constexpr (i == 0) gload16<0>(pq[0], pqp, voff);
    else if constexpr (i < 70) {
      constexpr int u = (i - 1) / 8, k = (i - 1) % 8;
      if constexpr (u < 8 && k < 2) gload16<((2 * u + k) % 4) * 1024>(kvf[2 * u + k], kvp + ((2 * u + k) / 4) * 4096, voffc);
      else if constexpr (u < 8 && k == 2) gload16<((u + 1) % 4) * 1024>(pq[u + 1], pqp + ((u + 1) / 4) * 4096, voff);
      else { constexpr int n = u < 8 ? 5 * u + k - 3 : 40 + k; gload16<(n % 4) * 1024>(wf[n], wop + (n / 4) * 4096, n % 5 == 4 ? voffc : voff); }
    }
  };
  const int u0 = w == 0 ? 0 : 2 * w + 1;
  u32x4 xr[3]; unsigned mxb;
  sfor<4>([&](auto I) { issue(I); });
  {
    const char* xb0 = (const char*)x + (size_t)tt * 32 * 1152 + ng * 288 + u0 * 32;
    const unsigned xo = tok * 1152 + h * 16;
    gload16<0>(xr[0], xb0, xo); gload16<32>(xr[1], xb0, xo);
    gload16<0>(xr[2], xb0 + (w == 0 ? 64 : 0), xo);
    gload4<0>(mxb, ws_in + WS_MX + (size_t)tt * 128, tok * 4);
  }
  sfor<K2_WIN - 4>([&](auto I) { issue(std::integral_constant<int, 4 + decltype(I)::value>{}); });
  u32x4 ofr; unsigned lo[8];
  f32x16 acc[5], c;
  auto ostep = [&](auto UI) {
    constexpr int u = decltype(UI)::value;
    if constexpr (u == 0) pin(pq[0]);
    pin(kvf[2 * u]); pin(kvf[2 * u + 1]); pin(pq[u + 1]);
    c = mfma32(kvf[2 * u], pq[u], (f32x16){});
    c = mfma32(kvf[2 * u + 1], pq[u + 1], c);
  };
  wait_vm<K2_WIN - 4 + 4>();
  ostep(std::integral_constant<int, 0>{});
  sfor<9>([&](auto UI) {
    constexpr int u = decltype(UI)::value;
    if constexpr (u < 8) {
      const float r = __builtin_amdgcn_rcpf(c[10] + 1e-6f);
      ofr = (u32x4){pk2(c[0] * r, c[1] * r), pk2(c[2] * r, c[3] * r), pk2(c[4] * r, c[5] * r), pk2(c[6] * r, c[7] * r)};
      lo[u] = pk2(c[8] * r, c[9] * r);
    } else {
      sfor<4>([&](auto J) { constexpr int j = decltype(J)::value; unsigned a = lo[j], d = lo[4 + j]; pin(a); pin(d); ofr[j] = h ? d : a; });
    }
    sfor<8>([&](auto KI) { issue(std::integral_constant<int, K2_WIN + 8 * u + decltype(KI)::value>{}); });
    constexpr int issued = (K2_WIN + 8 * (u + 1)) < 70 ? (K2_WIN + 8 * (u + 1)) : 70;
    constexpr int need = u < 7 ? 8 * (u + 1) + 4 : (u == 7 ? 65 : 70);
    wait_vm<issued - need>();
    if constexpr (u < 7) ostep(std::integral_constant<int, u + 1>{});
    sfor<5>([&](auto NI) {
      constexpr int i = decltype(NI)::value, n = 5 * u + i;
      pin(wf[n]);
      acc[i] = mfma32(wf[n], ofr, u == 0 ? (f32x16){} : acc[i]);
    });
    sb0();
  });
#pragma unroll
  for (int i = 0; i < 3; ++i) pin(xr[i]);
  pin(mxb);
  const float mx = __uint_as_float(mxb);
  f32x4* xb = (f32x4*)lds;
#pragma unroll
  for (int q = 0; q < 19; ++q) {
    const int i = q >> 2, rb = (q & 3) * 4;
    xb[(w * 19 + q) * 64 + lane] = (f32x4){acc[i][rb], acc[i][rb + 1], acc[i][rb + 2], acc[i][rb + 3]};
  }
  tlmark(31);
#pragma unroll
  for (int i = 0; i < 3; ++i) {
    auto r0 = __builtin_amdgcn_permlane32_swap(xr[i][0], xr[i][2], false, false);
    auto r1 = __builtin_amdgcn_permlane32_swap(xr[i][1], xr[i][3], false, false);
    xr[i] = (u32x4){r0[0], r1[0], r0[1], r1[1]};
  }
  wait_lgkm0(); bar();
  float ms = 0.f;
#pragma unroll
  for (int k = 0; k < 4; ++k) { const f32x4 v = xb[(k * 19 + 18) * 64 + tok]; ms += v[0] + v[1]; }
  const float mean = rbf(mx + ms * (1.f / D_));
  if (ng == 0 && w == 0 && h == 0) ((float*)(ws + WS_MEAN))[tt * 32 + tok] = mean;
  float s1 = 0.f, s2 = 0.f;
#pragma unroll
  for (int i = 0; i < 3; ++i) {
    if (i < 2 || w == 0) {
      const int u = u0 + i;
      f32x4 a = {}, d = {};
#pragma unroll
      for (int k = 0; k < 4; ++k) { a += xb[(k * 19 + 2 * u) * 64 + lane]; d += xb[(k * 19 + 2 * u + 1) * 64 + lane]; }
      const unsigned at[4] = {pk2(a[0], a[1]), pk2(a[2], a[3]), pk2(d[0], d[1]), pk2(d[2], d[3])};
      u32x4 o;
#pragma unroll
      for (int j = 0; j < 4; ++j) {
        const unsigned xv = xr[i][j];
        const unsigned hb = pk2(bflo(xv) + bflo(at[j]), bfhi(xv) + bfhi(at[j]));
        s1 = dot2_ones(hb, s1); s2 = __builtin_amdgcn_fdot2_f32_bf16(as_bf2(hb), as_bf2(hb), s2, false);
        o[j] = pk2(bflo(hb) - mean, bfhi(hb) - mean);
      }
      store16<OUT2_ST>(ws + WS_HF + (((size_t)tt * 36 + ng * 9 + u) * 64 + lane) * 16, o);
    }
  }
  s1 += __shfl_xor(s1, 32); s2 += __shfl_xor(s2, 32);
  if (h == 0) *(f32x2*)(ws + WS_HST + ((size_t)(tt * 32 + tok) * 16 + ng * 4 + w) * 8) = (f32x2){s1, s2};
  asm volatile(".Lfk_text_end:");
}
}
extern "C" void kernel_launch(void* const* d_in, const int* in_sizes, int n_in, void* d_out, int out_size, void* d_ws, size_t ws_size, hipStream_t st) {
  using namespace fk;
  const u16* x = (const u16*)d_in[0]; const u16* wqkv = (const u16*)d_in[1]; const u16* wo = (const u16*)d_in[2];
  const u16* wg = (const u16*)d_in[4]; const u16* wu = (const u16*)d_in[5]; const u16* wd = (const u16*)d_in[6];
  char* ws = (char*)d_ws;
  (void)hipFuncSetAttribute((const void*)k1_qkv, hipFuncAttributeMaxDynamicSharedMemorySize, K1_LDS);
  (void)hipFuncSetAttribute((const void*)k1b_reduce, hipFuncAttributeMaxDynamicSharedMemorySize, K1B_LDS);
  (void)hipFuncSetAttribute((const void*)k2_attn_out, hipFuncAttributeMaxDynamicSharedMemorySize, K2_LDS);
  (void)hipFuncSetAttribute((const void*)k34_ffn, hipFuncAttributeMaxDynamicSharedMemorySize, K3_LDS);
  k1_qkv<<<256, 512, K1_LDS, st>>>(x, wqkv, wo, wg, wu, wd, ws);

  k1b_reduce<<<32, 256, 0, st>>>(wg, wu, wd, ws);
  k2_attn_out<<<256, 256, K2_LDS, st>>>(x, ws, ws);
  k34_ffn<<<256, 512, K3_LDS, st>>>(ws, ws, (u16*)d_out);
}
```

```cpp
#include <hip/hip_runtime.h>
#include <hip/hip_bf16.h>
#include <stdint.h>
#include <type_traits>
#include <utility>

#define LN_SOFT 1.5e-3f
namespace fk {
typedef unsigned short u16;
typedef short bf16x8 __attribute__((ext_vector_type(8)));
typedef short s16x4 __attribute__((ext_vector_type(4)));
typedef float f32x4 __attribute__((ext_vector_type(4)));
typedef float f32x2 __attribute__((ext_vector_type(2)));
typedef float f32x16 __attribute__((ext_vector_type(16)));
typedef unsigned u32x4 __attribute__((ext_vector_type(4)));
typedef unsigned u32x2 __attribute__((ext_vector_type(2)));
typedef __bf16 bf16x2v __attribute__((ext_vector_type(2)));
#define LAS __attribute__((address_space(3)))
#define DI __device__ __forceinline__

constexpr int S_ = 2048, D_ = 576, H_ = 32, DH_ = 18, F_ = 4608;

template <int N, class Fn, int... I> DI void sfor_impl(Fn&& f, std::integer_sequence<int, I...>) { (f(std::integral_constant<int, I>{}), ...); }
template <int N, class Fn> DI void sfor(Fn&& f) { sfor_impl<N>(f, std::make_integer_sequence<int, N>{}); }

template <int N> DI void wait_vm() { asm volatile("s_waitcnt vmcnt(%0)" ::"i"(N) : "memory"); }
DI void wait_lgkm0() { asm volatile("s_waitcnt lgkmcnt(0)" ::: "memory"); }
DI void bar() { asm volatile("" ::: "memory"); __builtin_amdgcn_s_barrier(); asm volatile("" ::: "memory"); }
template <class T> DI void pin(T& v) { asm volatile("" : "+v"(v)); }

DI void glds16(const void* sbase, unsigned voff, unsigned ldsdst) {
  asm volatile("s_mov_b32 m0, %2\n\ts_nop 0\n\tglobal_load_lds_dwordx4 %0, %1" :: "v"(voff), "s"(sbase), "s"(ldsdst) : "memory");
}
DI void glds16nt(const void* sbase, unsigned voff, unsigned ldsdst) {
  asm volatile("s_mov_b32 m0, %2\n\ts_nop 0\n\tglobal_load_lds_dwordx4 %0, %1 nt" :: "v"(voff), "s"(sbase), "s"(ldsdst) : "memory");
}
DI void glds16x2(const void* sbase, unsigned voff, unsigned ldsdst) {
  asm volatile("s_mov_b32 m0, %2\n\ts_nop 0\n\tglobal_load_lds_dwordx4 %0, %1\n\tglobal_load_lds_dwordx4 %0, %1 offset:1024" :: "v"(voff), "s"(sbase), "s"(ldsdst) : "memory");
}
DI void glds16v(const void* gptr, unsigned ldsdst) {
  asm volatile("s_mov_b32 m0, %1\n\ts_nop 0\n\tglobal_load_lds_dwordx4 %0, off" :: "v"(gptr), "s"(ldsdst) : "memory");
}
DI void sb0() { __builtin_amdgcn_sched_barrier(0); }
template <int OFF> DI void gload16(u32x4& dst, const void* sbase, unsigned voff) {
  asm volatile("global_load_dwordx4 %0, %1, %2 offset:%3" : "=v"(dst) : "v"(voff), "s"(sbase), "i"(OFF) : "memory");
}
template <int OFF> DI void gload8(u32x2& dst, const void* sbase, unsigned voff) {
  asm volatile("global_load_dwordx2 %0, %1, %2 offset:%3" : "=v"(dst) : "v"(voff), "s"(sbase), "i"(OFF) : "memory");
}
template <int OFF> DI void gload4(unsigned& dst, const void* sbase, unsigned voff) {
  asm volatile("global_load_dword %0, %1, %2 offset:%3" : "=v"(dst) : "v"(voff), "s"(sbase), "i"(OFF) : "memory");
}
template <int OFF> DI void gload16a(u32x4& dst, const void* sbase, unsigned voff) {
  asm volatile("global_load_dwordx4 %0, %1, %2 offset:%3" : "=a"(dst) : "v"(voff), "s"(sbase), "i"(OFF) : "memory");
}
DI void l2touch(const void* sbase, unsigned voff, unsigned ldsdummy) {
  asm volatile("s_mov_b32 m0, %2\n\ts_nop 0\n\tglobal_load_lds_dword %0, %1" :: "v"(voff), "s"(sbase), "s"(ldsdummy) : "memory");
}
DI void text_range(const char*& base, unsigned& len) {
  const char* pc; int d0, d1;
  asm volatile("s_getpc_b64 %0\n.Lfkpc%=:\n\ts_mov_b32 %1, .Lfk_text_begin-.Lfkpc%=\n\ts_mov_b32 %2, .Lfk_text_end-.Lfkpc%=" : "=s"(pc), "=s"(d0), "=s"(d1));
  base = pc + d0; len = (unsigned)(d1 - d0);
}
template <class T> DI void pina(T& v) { asm volatile("" : "+a"(v)); }
template <int MODE> DI void store16(void* p, u32x4 v) {
  if constexpr (MODE == 0) *(u32x4*)p = v;
  else if constexpr (MODE == 1) __builtin_nontemporal_store(v, (u32x4*)p);
  else if constexpr (MODE == 2) asm volatile("global_store_dwordx4 %0, %1, off sc1\n\ts_nop 1" :: "v"(p), "v"(v) : "memory");
  else if constexpr (MODE == 3) asm volatile("global_store_dwordx4 %0, %1, off sc0 sc1\n\ts_nop 1" :: "v"(p), "v"(v) : "memory");
  else if constexpr (MODE == 4) asm volatile("global_store_dwordx4 %0, %1, off sc1 nt\n\ts_nop 1" :: "v"(p), "v"(v) : "memory");
  else asm volatile("" :: "v"(v));
}
DI float bf2f(u16 v) { return __uint_as_float(((unsigned)v) << 16); }
DI float bflo(unsigned v) { return __uint_as_float(v << 16); }
DI float bfhi(unsigned v) { return __uint_as_float(v & 0xffff0000u); }
DI unsigned pk2(float lo, float hi) {
  bf16x2v r; r[0] = (__bf16)lo; r[1] = (__bf16)hi; return __builtin_bit_cast(unsigned, r);
}
DI float rbf(float v) { return bflo(pk2(v, 0.f)); }
DI float ln_rstd(float var) { return __builtin_amdgcn_rcpf(rbf(__builtin_amdgcn_sqrtf(rbf(rbf(fmaxf(var, 0.f)) + 1e-5f)))); }
DI float ln_den(float var) { return rbf(__builtin_amdgcn_sqrtf(rbf(rbf(var) + 1e-5f))); }
DI float ln_rstd_soft(float var) { var = fmaxf(var, 0.f); return __builtin_amdgcn_rcpf(0.5f * (ln_den(var * (1.f - LN_SOFT)) + ln_den(var * (1.f + LN_SOFT)))); }
DI s16x4 tr_read(unsigned lds_addr) {
  return __builtin_amdgcn_ds_read_tr16_b64_v4i16((LAS s16x4*)(size_t)lds_addr);
}
DI f32x16 mfma32(u32x4 a, u32x4 b, f32x16 c) { return __builtin_amdgcn_mfma_f32_32x32x16_bf16(__builtin_bit_cast(bf16x8, a), __builtin_bit_cast(bf16x8, b), c, 0, 0, 0); }
DI f32x4 mfma16(u32x4 a, u32x4 b, f32x4 c) { return __builtin_amdgcn_mfma_f32_16x16x32_bf16(__builtin_bit_cast(bf16x8, a), __builtin_bit_cast(bf16x8, b), c, 0, 0, 0); }
#define TLSCOPE(k)
DI void tlmark(int) {}
DI void ckmark(int) {}
DI int uni(int v) { return __builtin_amdgcn_readfirstlane(v); }
}
namespace fk {
DI bf16x2v as_bf2(unsigned v) { return __builtin_bit_cast(bf16x2v, v); }
DI float dot2_ones(unsigned v, float c) { return __builtin_amdgcn_fdot2_f32_bf16(as_bf2(v), as_bf2(0x3f803f80u), c, false); }

constexpr size_t WS_HF   = 0;
constexpr size_t SZ_HF   = (size_t)S_ * D_ * 2;
constexpr size_t WS_HST  = WS_HF + SZ_HF;
constexpr size_t SZ_HST  = (size_t)S_ * 16 * 8;
constexpr size_t WS_WGU  = WS_HST + SZ_HST;
constexpr size_t SZ_WGU  = (size_t)288 * 36 * 1024;
constexpr int    WD_STREAM = 168;
constexpr size_t WS_WD   = WS_WGU + SZ_WGU + 16384;
constexpr size_t SZ_WD   = (size_t)32 * WD_STREAM * 1024;
constexpr size_t WS_ACT  = WS_WD + SZ_WD;
constexpr size_t SZ_ACT  = (size_t)S_ * F_ * 2;
constexpr size_t WS_PART = WS_ACT + SZ_ACT;
constexpr size_t SZ_PART = (size_t)4 * S_ * D_ * 4;
constexpr size_t WS_R2   = WS_PART + SZ_PART;
constexpr size_t WS_MEAN = WS_R2 + S_ * 4;
constexpr size_t WS_CNT  = WS_MEAN + S_ * 4;
constexpr size_t WS_END_FFN = WS_CNT + 576 * 128;
__host__ __device__ constexpr int k4_act(int v) { return v < 10 ? (v / 5) * 9 + (v % 5 < 4 ? v % 5 : 8) : ((v - 10) / 4) * 9 + 4 + (v - 10) % 4; }
DI int k4_virt(int ks) { const int j = ks / 9, p = ks % 9; return p < 4 ? j * 5 + p : (p == 8 ? j * 5 + 4 : 10 + j * 4 + p - 4); }

DI void pack_gu_load(const u16* wg, const u16* wu, int lc, int kp, unsigned ldsb, int lane) {
  const unsigned voff = (unsigned)(((kp * 64 + (lane >> 3)) * F_ + lc * 64 + (lane & 7) * 8) * 2);
  sfor<16>([&](auto I) {
    constexpr int i = decltype(I)::value, s = i >> 3, mat = (i >> 2) & 1, r8 = s * 4 + (i & 3);
    glds16nt((const char*)(mat ? wu : wg) + (size_t)r8 * 8 * F_ * 2, voff, ldsb + mat * 8192 + r8 * 1024);
  });
}
template <int ST, bool SPLITWAIT> DI void pack_gu_store(char* ws, int lc, int kp, unsigned ldsb, int lane) {
  const int g = lane >> 4;
  const unsigned rbase = ldsb + (16 * (g >> 1) + 4 * (g & 1) + ((lane >> 2) & 3)) * 128 + (lane & 3) * 8;
  char* dst = ws + WS_WGU + ((size_t)(lc * 4) * 18 + kp * 2) * 2048 + lane * 16;
  sfor<2>([&](auto I) {
    constexpr int s = decltype(I)::value;
    if constexpr (SPLITWAIT) wait_vm<8>();
    u32x4 v[8];
    sfor<8>([&](auto J) {
      constexpr int t = decltype(J)::value >> 1, mat = decltype(J)::value & 1;
      u32x2 a = __builtin_bit_cast(u32x2, tr_read(rbase + s * 4096 + mat * 8192 + t * 32)), b = __builtin_bit_cast(u32x2, tr_read(rbase + s * 4096 + mat * 8192 + t * 32 + 1024));
      v[decltype(J)::value] = (u32x4){a[0], a[1], b[0], b[1]};
    });
    sfor<8>([&](auto J) {
      constexpr int t = decltype(J)::value >> 1, mat = decltype(J)::value & 1;
      store16<ST>(dst + (t * 18 + s) * 2048 + mat * 1024, v[decltype(J)::value]);
    });
  });
}
template <int ST> DI void pack_gu_unit(const u16* wg, const u16* wu, char* ws, int lc, int kp, unsigned ldsb, int lane) {
  pack_gu_load(wg, wu, lc, kp, ldsb, lane); pack_gu_store<ST, true>(ws, lc, kp, ldsb, lane);
}
DI void pack_wd_load(const u16* wd, int S, int c2, unsigned ldsb, int lane) {
  const char* base = (const char*)wd + ((size_t)S * 32 * D_ + c2 * 288) * 2;
  sfor<9>([&](auto I) {
    constexpr int n = decltype(I)::value;
    const int c = n * 64 + lane, row = c / 36, cc = c % 36;
    const unsigned vo = (unsigned)(row * D_ * 2 + cc * 16);
    glds16nt(base, vo, ldsb + n * 1024);
    glds16nt(base + 16 * D_ * 2, vo, ldsb + 9216 + n * 1024);
  });
}
template <int ST> DI void pack_wd_store(char* ws, int S, int c2, unsigned ldsb, int lane) {
  const int g = lane >> 4, q = (lane >> 2) & 3, p = lane & 3;
  const unsigned rb = ldsb + (16 * (g >> 1) + 4 * (g & 1) + q) * 576;
  const int ksl = S / 36, kh = (S % 36) / 18, ks = S % 18;
  char* dst = ws + WS_WD + ((size_t)((c2 * 2 * 4 + ksl) * 2 + kh) * WD_STREAM + k4_virt(ks) * 9) * 1024 + lane * 16;
  u32x4 v[18];
  sfor<18>([&](auto I) {
    constexpr int ngl = decltype(I)::value / 9, t = decltype(I)::value % 9;
    const unsigned a0 = rb + ngl * 288 + (t < 8 ? 64 * (t >> 1) + 16 * p + 8 * (t & 1) : 256 + 8 * p);
    u32x2 a = __builtin_bit_cast(u32x2, tr_read(a0)), b = __builtin_bit_cast(u32x2, tr_read(a0 + 8 * 576));
    v[decltype(I)::value] = (u32x4){a[0], a[1], b[0], b[1]};
  });
  sfor<18>([&](auto I) {
    constexpr int ngl = decltype(I)::value / 9, t = decltype(I)::value % 9;
    store16<ST>(dst + (size_t)ngl * 8 * WD_STREAM * 1024 + t * 1024, v[decltype(I)::value]);
  });
}
template <int ST> DI void pack_wd_unit(const u16* wd, char* ws, int S, int c2, unsigned ldsb, int lane) {
  pack_wd_load(wd, S, c2, ldsb, lane); wait_vm<0>(); pack_wd_store<ST>(ws, S, c2, ldsb, lane);
}

DI u32x4 load16_sc1(const void* p) { u32x4 v; asm volatile("global_load_dwordx4 %0, %1, off sc1" : "=v"(v) : "v"(p) : "memory"); return v; }
DI u32x2 load8_sc1(const void* p) { u32x2 v; asm volatile("global_load_dwordx2 %0, %1, off sc1" : "=v"(v) : "v"(p) : "memory"); return v; }
DI unsigned load4_sc1(const void* p) { unsigned v; asm volatile("global_load_dword %0, %1, off sc1" : "=v"(v) : "v"(p) : "memory"); return v; }
DI void store8_sc1(void* p, u32x2 v) { asm volatile("global_store_dwordx2 %0, %1, off sc1\n\ts_nop 1" :: "v"(p), "v"(v) : "memory"); }

DI void sig_add(unsigned* p) {
  unsigned long long keep; unsigned one = 1u;
  asm volatile("s_mov_b64 %0, exec\n\ts_mov_b64 exec, 1\n\tglobal_atomic_add %1, %2, off\n\ts_mov_b64 exec, %0" : "=&s"(keep) : "v"(p), "v"(one) : "memory");
}
DI void spin_ge(unsigned& v, const unsigned* p, unsigned need) {
  unsigned t, n;
  asm volatile("s_mov_b32 %2, 0x40000\n\ts_nop 0\n"
               "1:\n\tv_readfirstlane_b32 %1, %0\n\ts_nop 3\n\ts_cmp_ge_u32 %1, %4\n\ts_cbranch_scc1 2f\n\t"
               "s_sub_u32 %2, %2, 1\n\ts_cbranch_scc1 2f\n\ts_sleep 1\n\t"
               "global_load_dword %0, %3, off sc1\n\ts_waitcnt vmcnt(0)\n\ts_branch 1b\n"
               "2:" : "+v"(v), "=&s"(t), "=&s"(n) : "v"(p), "s"(need) : "memory", "scc");
}
template <int OFF> DI void gload16_sc1(u32x4& dst, const void* sbase, unsigned voff) {
  asm volatile("global_load_dwordx4 %0, %1, %2 offset:%3 sc1" : "=v"(dst) : "v"(voff), "s"(sbase), "i"(OFF) : "memory");
}
constexpr int K3_LDS = 160 * 1024;
#define K3_P 4
constexpr int K3_Q = K3_P + 1;
DI void k3_wload(u32x4 (&dst)[4], const char* base, int s, int rot, unsigned voff) {
  const int sa = s + rot >= 9 ? s + rot - 9 : s + rot;
  sfor<4>([&](auto FI) { constexpr int f = decltype(FI)::value; gload16<f * 1024>(dst[f], base + sa * 4096, voff); });
}
template <int NTK, bool FIRST, bool NEXT, int QB, int NST, class HL>
DI void k3_unit(f32x4 (&acc)[2][8], u32x4 (&wq)[K3_Q][4], const char* cur, const char* nxt, const unsigned (&lb)[3], int rot, unsigned voff, HL&& hload) {
  constexpr int NG = NTK / 4;
  u32x4 af[2][4];
  auto aread = [&](auto II) {
    constexpr int it = decltype(II)::value, ks = it / NG, gp = it % NG;
#pragma unroll
    for (int tk = 0; tk < 4; ++tk) af[it & 1][tk] = *(const LAS u32x4*)(size_t)(lb[ks / 6] + ((ks % 6) * 8 + gp * 4 + tk) * 1024);
  };
  aread(std::integral_constant<int, 0>{});
  sfor<9>([&](auto SI) {
    constexpr int s = decltype(SI)::value;
    if constexpr (FIRST) {
      if constexpr (s == 1) { wait_vm<4>(); bar(); }
      if constexpr (s + 2 < 9) k3_wload(wq[(QB + s + 2) % K3_Q], cur, s + 2, rot, voff);
      if constexpr (s < 2) sfor<6>([&](auto HI) { hload(std::integral_constant<int, 6 + 6 * s + decltype(HI)::value>{}); });
      constexpr int n0 = s < 6 ? 0 : (K3_P * (s - 6)) / 3, n1 = s < 6 ? 0 : (K3_P * (s - 5)) / 3;
      constexpr int m0 = K3_P == 2 ? (s < 7 ? 0 : s - 7) : n0, m1 = K3_P == 2 ? (s < 7 ? 0 : s - 6) : n1;
      sfor<m1 - m0>([&](auto JI) { constexpr int j = m0 + decltype(JI)::value; static_assert(9 + j - K3_Q < s, "slot busy"); k3_wload(wq[(QB + 9 + j) % K3_Q], nxt, j, rot, voff); });
      if constexpr (s == 0) wait_vm<2 + 4 + 4 + 6>();
      else if constexpr (s >= 3) wait_vm<(s <= 6 ? 8 : (s == 7 ? 4 : 0)) + 4 * m1>();
      if constexpr (s == 3) bar();
    } else {
      if constexpr (s + K3_P < 9) k3_wload(wq[(QB + s + K3_P) % K3_Q], cur, s + K3_P, rot, voff);
      else if constexpr (NEXT) k3_wload(wq[(QB + s + K3_P) % K3_Q], nxt, s + K3_P - 9, rot, voff);
      constexpr int ny = NEXT ? K3_P : ((8 - s) < K3_P ? (8 - s) : K3_P);
      wait_vm<4 * ny + (s < K3_P ? NST : 0)>();
      if constexpr (!NEXT && s == K3_P) hload(std::integral_constant<int, 0>{});
      if constexpr (!NEXT && s == 6) hload(std::integral_constant<int, 1>{});
    }
    sfor<4>([&](auto FI) { pin(wq[(QB + s) % K3_Q][decltype(FI)::value]); });
    sfor<2 * NG>([&](auto QI) {
      constexpr int q = decltype(QI)::value, it = s * 2 * NG + q, ks = it / NG, gp = it % NG, kq = ks & 1;
      sfor<4>([&](auto TI) {
        constexpr int tk = decltype(TI)::value, ks1 = (it + 1) / NG, gp1 = (it + 1) % NG;
        acc[0][gp * 4 + tk] = mfma16(wq[(QB + s) % K3_Q][kq * 2], af[it & 1][tk], ks == 0 ? (f32x4){} : acc[0][gp * 4 + tk]);
        if constexpr (it + 1 < 18 * NG) af[(it + 1) & 1][tk] = *(const LAS u32x4*)(size_t)(lb[ks1 / 6] + ((ks1 % 6) * 8 + gp1 * 4 + tk) * 1024);
        sb0();
        acc[1][gp * 4 + tk] = mfma16(wq[(QB + s) % K3_Q][kq * 2 + 1], af[it & 1][tk], ks == 0 ? (f32x4){} : acc[1][gp * 4 + tk]);
        sb0();
      });
    });
  });
}

#define K4_LA 3
#define K4_LW 3
constexpr int K4_R = 8, K4_D = 4;
constexpr int K4_LDS = K4_R * 16384;
constexpr int K4_NST = 21;
struct K4Sched {
  int aslot[18], sslot[K4_NST], posA[18], posS[K4_NST], cntBar[K4_NST], cntSlot[162], posSt;
  constexpr K4Sched(int late) : aslot{}, sslot{}, posA{}, posS{}, cntBar{}, cntSlot{}, posSt{} {
    for (int a = 0; a < 18; ++a) aslot[a] = a < K4_LA ? -1 : 9 * (a - K4_LA) + 4;
    for (int s = 0; s < K4_NST; ++s) sslot[s] = s == 0 ? -1 : (s < K4_LW ? 0 : s - K4_LW);
    int c = 0;
    for (int s = 0; s < K4_LA; ++s) { c += 2; posA[s] = c; }
    if (!late) { c += 4; posSt = c; }
    for (int n = -K4_D; n < 162; ++n) {
      if ((n + K4_D) % 8 == 0 && (n + K4_D) < 162) {
        int i = (n + K4_D) / 8; cntBar[i] = c;
        for (int s = 0; s < K4_NST; ++s) if (sslot[s] == i) { c += 2; posS[s] = c; }
        if (late && i == 0) { c += 4; posSt = c; }
      }
      if (n >= 0) {
        for (int q = 0; q < 18; ++q) if (aslot[q] == n) { c += 2; posA[q] = c; }
        cntSlot[n] = c;
      }
    }
  }
};
template <int LATE> constexpr K4Sched k4sch{LATE};
#define K4_LATE 1
#define K4_SIGB 3
#define K4_PISS 6
#define K4_PCHK 8
static_assert(8 * K4_PCHK - K4_D <= k4sch<0>.aslot[10], "late activations must be requested behind the check");
template <int LATE, class EPI>
DI void k4_body(const char* __restrict__ ws_in, char* __restrict__ ws_out, u16* __restrict__ out, char* lds, int w, float rs, EPI&& last_epilogue) {
  const int tid = threadIdx.x, lane = tid & 63;
  const int b = blockIdx.x, xcd = b & 7, idx = b >> 3;
  const int ksl = xcd >> 1, mt = (xcd & 1) * 8 + (idx & 7), ng = idx >> 3;
  const int tg = w & 3, kh = w >> 2;
  const int tt = mt * 4 + tg;
  const unsigned ldsb = (unsigned)(size_t)(LAS char*)lds;
  const char* wsrc = ws_in + WS_WD + (size_t)((ng * 4 + ksl) * 2 + kh) * WD_STREAM * 1024 + tg * 2048;
  const char* asrc = ws_in + WS_ACT + ((size_t)tt * 288 + ksl * 72 + kh * 36) * 1024;
  const unsigned voff = lane * 16;
  const int g = lane >> 4;
  const unsigned avoff0 = (g >> 1) * 1024 + (g & 1) * 512 + (lane & 15) * 16 + kh * 256, avoff1 = avoff0 ^ 256;
  const int th = w & 1, a = w >> 1, ott = mt * 4 + ksl, tok = th * 16 + (lane & 15);
  const int col = 32 * a + 8 * g, col8 = 128 + 4 * g;
  u32x2 h0, h1, h2; unsigned mub;
  u32x4 af[K4_LA + 1][2];
  auto issue_stage = [&](auto SI) {
    constexpr int s = decltype(SI)::value;
    glds16x2(wsrc + (size_t)s * 8192, voff, ldsb + (s % K4_R) * 16384 + kh * 8192 + tg * 2048);
  };
  auto issue_a = [&](auto AI) {
    constexpr int a = decltype(AI)::value;
    constexpr int ka = k4_act(a);
    gload16_sc1<(ka % 2) * 2048>(af[a % (K4_LA + 1)][0], asrc + (ka / 2) * 4096, avoff0);
    gload16_sc1<(ka % 2) * 2048>(af[a % (K4_LA + 1)][1], asrc + (ka / 2) * 4096, avoff1);
  };
  sfor<K4_LA>([&](auto AI) { issue_a(AI); });
  if constexpr (!LATE) last_epilogue();
  f32x4 acc[9][2];
  u32x4 wf[K4_D + 1];
  unsigned* cntb = (unsigned*)(ws_out + WS_CNT);
  const unsigned* c2p = cntb + (384 + mt * 8 + ksl * 2 + kh) * 32;
  unsigned pv2 = 0;
  unsigned lrd0 = ldsb + kh * 8192 + lane * 16, lrd1 = lrd0 + 65536;
  unsigned xw = ldsb + w * 1024 + lane * 16, xw5 = xw + 5 * 16384;
  asm volatile("" : "+v"(lrd0), "+v"(lrd1), "+v"(xw), "+v"(xw5));
  auto xad = [&](unsigned lo, unsigned hi, int t) -> unsigned { return t < 6 ? hi + t * 8192 : lo + (t - 6) * 8192; };
  auto rd = [&](auto NI) {
    constexpr int n = decltype(NI)::value;
    if constexpr (n < 162) {
      constexpr int st = n / 8;
      if constexpr (n % 8 == 0) {
        if constexpr (st > 0) wait_vm<k4sch<LATE>.cntBar[st] - k4sch<LATE>.posS[st]>();
        if constexpr (st == (LATE ? K4_SIGB : 1)) {
          wait_vm<k4sch<LATE>.cntBar[st] - k4sch<LATE>.posSt>();
          sig_add(cntb + (384 + mt * 8 + ksl * 2 + (ng >> 1)) * 32);
        }
        if constexpr (st == K4_PCHK) {
          wait_vm<k4sch<LATE>.cntBar[K4_PCHK] - k4sch<LATE>.cntBar[K4_PISS]>();
          spin_ge(pv2, c2p, 16u);
        }
        bar();
        if constexpr (st == 0) { tlmark(26); ckmark(5); }
        if constexpr (st == K4_PISS) pv2 = load4_sc1(c2p);
        if constexpr (st == 12) {
          const char* hfp = ws_in + WS_HF + ((size_t)ott * 36 + ng * 9) * 1024;
          auto ho = [&](int c) -> unsigned { return (c >> 4) * 1024 + (((c >> 2) & 1) * 32 + tok) * 16 + ((c >> 3) & 1) * 8; };
          gload8<0>(h0, hfp, ho(col)); gload8<512>(h1, hfp, ho(col)); gload8<0>(h2, hfp, ho(col8));
          gload4<0>(mub, ws_in + WS_MEAN + (size_t)ott * 128, tok * 4);
        }
        sfor<K4_NST>([&](auto SI) { if constexpr (k4sch<LATE>.sslot[decltype(SI)::value] == st) issue_stage(SI); });
        if constexpr (LATE && st == 0) last_epilogue();
      }
      constexpr int off = (st % K4_R) * 16384 + (n % 8) * 1024;
      if constexpr (st == 0) wf[n % (K4_D + 1)] = *(const LAS u32x4*)(size_t)(lrd1 + 81920 + n * 1024);
      else wf[n % (K4_D + 1)] = *(const LAS u32x4*)(size_t)((off < 65536 ? lrd0 : lrd1) + (off & 65535));
    }
  };
  sfor<K4_D>([&](auto I) { rd(I); });
  sfor<162>([&](auto NI) {
    constexpr int n = decltype(NI)::value;
    constexpr int ks = n / 9, t = n % 9, ab = ks % (K4_LA + 1);
    rd(std::integral_constant<int, n + K4_D>{});
    sfor<18>([&](auto AI) { if constexpr (k4sch<LATE>.aslot[decltype(AI)::value] == n) issue_a(AI); });
    if constexpr (t == 0) {
      wait_vm<k4sch<LATE>.cntSlot[n] - k4sch<LATE>.posA[ks]>();
      pin(af[ab][0]); pin(af[ab][1]);
    }
    acc[t][0] = mfma16(wf[n % (K4_D + 1)], af[ab][0], ks == 0 ? (f32x4){} : acc[t][0]);
    acc[t][1] = mfma16(wf[n % (K4_D + 1)], af[ab][1], ks == 0 ? (f32x4){} : acc[t][1]);
    if constexpr (n >= 155) *(LAS f32x4*)(size_t)xad(xw, xw5, n - 155) = acc[n - 155][1];
    sb0();
  });
  tlmark(27); ckmark(6);
  *(LAS f32x4*)(size_t)xad(xw, xw5, 7) = acc[7][1]; *(LAS f32x4*)(size_t)xad(xw, xw5, 8) = acc[8][1];
  wait_lgkm0(); bar();
  tlmark(20);
  const unsigned xr = ldsb + (w ^ 4) * 1024 + lane * 16, xr5 = xr + 5 * 16384;
  f32x4 v[9];
#pragma unroll
  for (int t = 0; t < 9; ++t) v[t] = *(const LAS f32x4*)(size_t)xad(xr, xr5, t) + acc[t][0];
  if (tg == ksl) {
#pragma unroll
    for (int t = 0; t < 9; ++t) *(LAS f32x4*)(size_t)xad(xr, xr5, t) = v[t];
  } else {
    char* pp = ws_out + WS_PART + ((((size_t)ksl * 64 + tt) * 4 + ng) * 2 + kh) * 5120;
#pragma unroll
    for (int i = 0; i < 4; ++i)
      store16<2>(pp + i * 1024 + lane * 16, (u32x4){pk2(v[2 * i][0], v[2 * i][1]), pk2(v[2 * i][2], v[2 * i][3]), pk2(v[2 * i + 1][0], v[2 * i + 1][1]), pk2(v[2 * i + 1][2], v[2 * i + 1][3])});
    store8_sc1(pp + 4096 + lane * 8, (u32x2){pk2(v[8][0], v[8][1]), pk2(v[8][2], v[8][3])});
    wait_vm<0>();
    if (lane == 0) __hip_atomic_fetch_add((unsigned*)(ws_out + WS_CNT) + ((mt * 4 + ng) * 4 + tg) * 32, 1u, __ATOMIC_RELAXED, __HIP_MEMORY_SCOPE_AGENT);
  }
  tlmark(21);
  if (w == ksl) {
    const unsigned* cnt = (const unsigned*)(ws_out + WS_CNT) + ((mt * 4 + ng) * 4 + ksl) * 32;
    unsigned spins = 0;
    while (__hip_atomic_load(cnt, __ATOMIC_RELAXED, __HIP_MEMORY_SCOPE_AGENT) < 6u) {
      __builtin_amdgcn_s_sleep(1);
      if (++spins > (1u << 22)) break;
    }
  }
  wait_lgkm0();
  bar();
  tlmark(28);
  const unsigned ox = ldsb + ((th ^ 1) * 4 + ksl) * 1024 + lane * 16, oa = ox + (a < 3 ? 5 * 16384 + a * 16384 : 0);
  f32x4 s0 = *(const LAS f32x4*)(size_t)oa, s1 = *(const LAS f32x4*)(size_t)(oa + 8192), s2 = *(const LAS f32x4*)(size_t)(ox + 16384);
  const char* pb = ws_in + WS_PART + (((size_t)ott * 4 + ng) * 2 + th) * 5120;
  constexpr size_t KS = (size_t)64 * 4 * 2 * 5120;
  u32x4 pv[3]; unsigned pw[3][2];
#pragma unroll
  for (int k = 0; k < 3; ++k) {
    const char* q = pb + (size_t)((ksl + 1 + k) & 3) * KS;
    pv[k] = load16_sc1(q + a * 1024 + lane * 16);
    const char* q8 = q + (a == 0 ? 4096 + lane * 8 : a * 1024);
    pw[k][0] = load4_sc1(q8); pw[k][1] = load4_sc1(q8 + 4);
  }
  wait_vm<0>();
#pragma unroll
  for (int k = 0; k < 3; ++k) { pin(pv[k]); pin(pw[k][0]); pin(pw[k][1]); }
#pragma unroll
  for (int k = 0; k < 3; ++k) {
    const unsigned a0 = pv[k][0], a1 = pv[k][1], a2 = pv[k][2], a3 = pv[k][3], b0 = pw[k][0], b1 = pw[k][1];
    s0 += (f32x4){bflo(a0), bfhi(a0), bflo(a1), bfhi(a1)}; s1 += (f32x4){bflo(a2), bfhi(a2), bflo(a3), bfhi(a3)}; s2 += (f32x4){bflo(b0), bfhi(b0), bflo(b1), bfhi(b1)};
  }
  pin(h0); pin(h1); pin(h2); pin(mub);
  const float r2 = rs * rs, mu = __uint_as_float(mub);
  u16* orow = out + (size_t)(ott * 32 + tok) * D_ + ng * 144;
  s0 = s0 * r2 + mu; s1 = s1 * r2 + mu; s2 = s2 * r2 + mu;
  u32x4 o;
  o[0] = pk2(s0[0] + bflo(h0[0]), s0[1] + bfhi(h0[0])); o[1] = pk2(s0[2] + bflo(h0[1]), s0[3] + bfhi(h0[1]));
  o[2] = pk2(s1[0] + bflo(h1[0]), s1[1] + bfhi(h1[0])); o[3] = pk2(s1[2] + bflo(h1[1]), s1[3] + bfhi(h1[1]));
  *(u32x4*)(orow + col) = o;
  if (a == 0) *(u32x2*)(orow + col8) = (u32x2){pk2(s2[0] + bflo(h2[0]), s2[1] + bfhi(h2[0])), pk2(s2[2] + bflo(h2[1]), s2[3] + bfhi(h2[1]))};
}
template <int HALF>
DI void k3_body(const char* __restrict__ ws_in, char* __restrict__ ws_out, u16* __restrict__ out, char* lds, int w) {
  const int tid = threadIdx.x, lane = tid & 63;
  const int b = blockIdx.x, xcd = b & 7, idx = b >> 3;
  const int rt = (xcd & 1) * 8 + (idx & 7), fgp = (xcd >> 1) * 4 + (idx >> 3);
  const unsigned ldsb = (unsigned)(size_t)(LAS char*)lds;
  const unsigned voff = lane * 16;
  const int g = lane >> 4, l15 = lane & 15;
  const unsigned avoff = (g >> 1) * 1024 + (g & 1) * 512 + l15 * 16;
  const char* hsrc = ws_in + WS_HF + (size_t)(rt * 4 + (w >> 1)) * 36 * 1024 + (w & 1) * 256;
#define K3_ROT 0
  const int rot = K3_ROT;
  auto hload = [&](auto I) { constexpr int i = decltype(I)::value; const int ia = i + 2 * rot >= 18 ? i + 2 * rot - 18 : i + 2 * rot;
    glds16(hsrc + ia * 2048, avoff, ldsb + (i * 8 + w) * 1024); };
  auto nohl = [&](auto) {};
  const int t0 = w, t1 = w + 8, t2 = 16 + ((w >> 1) & 1);
  const char* wgu = ws_in + WS_WGU + (size_t)fgp * 18 * 36 * 1024;
  const char* sA = wgu + (size_t)t0 * 36 * 1024;
  const char* sB = wgu + (size_t)t1 * 36 * 1024;
  const char* sC = wgu + (size_t)t2 * 36 * 1024;
  u32x4 wq[K3_Q][4];
  u32x4 stv[2];
  {
    const char* sp = ws_in + WS_HST + (size_t)(rt * 128 + w * 16) * 128;
    gload16<0>(stv[0], sp, l15 * 128 + g * 32); gload16<16>(stv[1], sp, l15 * 128 + g * 32);
  }
  sfor<4>([&](auto I) { hload(I); });
  k3_wload(wq[0], sA, 0, rot, voff);
  hload(std::integral_constant<int, 4>{}); hload(std::integral_constant<int, 5>{});
  k3_wload(wq[1], sA, 1, rot, voff);
  wait_vm<10>();
  float* ctab = (float*)(lds + 144 * 1024);
  {
    pin(stv[0]); pin(stv[1]);
    float sm = (__uint_as_float(stv[0][0]) + __uint_as_float(stv[0][2])) + (__uint_as_float(stv[1][0]) + __uint_as_float(stv[1][2]));
    float sq = (__uint_as_float(stv[0][1]) + __uint_as_float(stv[0][3])) + (__uint_as_float(stv[1][1]) + __uint_as_float(stv[1][3]));
    sm += __shfl_xor(sm, 16); sq += __shfl_xor(sq, 16);
    sm += __shfl_xor(sm, 32); sq += __shfl_xor(sq, 32);
    const float mean = sm * (1.f / D_);
    const float rstd = ln_rstd_soft(sq * (1.f / D_) - mean * mean);
    if (lane < 16) {
      ctab[w * 16 + lane] = -1.4426950408889634f * rstd;
    }
  }
  wait_lgkm0();
  bar();
  float c1[8];
#pragma unroll
  for (int tk = 0; tk < 8; ++tk) c1[tk] = ctab[tk * 16 + l15];
  const float rs_out = ctab[(xcd >> 1) * 32 + (w & 1) * 16 + l15] * 0.6931471805599453f;
  tlmark(24); ckmark(0);
  unsigned lb[3] = {ldsb + voff, ldsb + voff + 49152, ldsb + voff + 98304};
  unsigned lh[3] = {lb[0] + (w & 1) * 4096, lb[1] + (w & 1) * 4096, lb[2] + (w & 1) * 4096};
  asm volatile("" : "+v"(lb[0]), "+v"(lb[1]), "+v"(lb[2]), "+v"(lh[0]), "+v"(lh[1]), "+v"(lh[2]));
  f32x4 acc[2][8];
  auto swiglu = [&](const f32x4& a, const f32x4& u, float c) -> u32x2 {
    float r[4];
#pragma unroll
    for (int j = 0; j < 4; ++j) { float e = __builtin_amdgcn_exp2f(a[j] * c); float pr = a[j] * u[j]; pin(pr); r[j] = pr * __builtin_amdgcn_rcpf(1.f + e); pin(r[j]); }
    return (u32x2){pk2(r[0], r[1]), pk2(r[2], r[3])};
  };
  char* actp = ws_out + WS_ACT + ((size_t)rt * 4 * 288 + fgp * 18) * 1024 + ((g & 1) * 32 + (g >> 1) * 16 + l15) * 16;
  auto epi2 = [&](int T, int tile32, const f32x4& g0, const f32x4& u0, float ca, const f32x4& g1, const f32x4& u1, float cb) {
    const u32x2 x0 = swiglu(g0, u0, ca), x1 = swiglu(g1, u1, cb);
    auto r0 = __builtin_amdgcn_permlane32_swap(x0[0], x1[0], false, false);
    auto r1 = __builtin_amdgcn_permlane32_swap(x0[1], x1[1], false, false);
    store16<2>(actp + ((size_t)tile32 * 288 + T) * 1024, (u32x4){r0[0], r1[0], r0[1], r1[1]});
  };
  auto epi_full = [&](int T) {
#pragma unroll
    for (int i = 0; i < 4; ++i) epi2(T, i, acc[0][2 * i], acc[1][2 * i], c1[2 * i], acc[0][2 * i + 1], acc[1][2 * i + 1], c1[2 * i + 1]);
  };
  k3_unit<8, true, true, 0, 0>(acc, wq, sA, HALF == 0 ? sC : sB, lb, rot, voff, hload);
  tlmark(25); ckmark(1);
  epi_full(t0);
  ckmark(2);
  if constexpr (HALF == 0) {
    k3_unit<4, false, true, 9 % K3_Q, 4>(acc, wq, sC, sB, lh, rot, voff, nohl);
    const bool hi = w & 1;
#pragma unroll
    for (int i = 0; i < 2; ++i) {
      float a0 = c1[2 * i], a1 = c1[4 + 2 * i], b0 = c1[2 * i + 1], b1 = c1[5 + 2 * i]; pin(a0); pin(a1); pin(b0); pin(b1);
      const float ca = hi ? a1 : a0, cb = hi ? b1 : b0;
      epi2(t2, (w & 1) * 2 + i, acc[0][2 * i], acc[1][2 * i], ca, acc[0][2 * i + 1], acc[1][2 * i + 1], cb);
    }
  }
  unsigned* cnt = (unsigned*)(ws_out + WS_CNT);
  const unsigned* c1p = cnt + (256 + rt * 8 + (fgp >> 2) * 2 + HALF) * 32;
  unsigned pv = 0;
  auto hook = [&](auto I) {
    if constexpr (decltype(I)::value == 0) sig_add(cnt + (256 + rt * 8 + (fgp >> 1)) * 32);
    else {
      glds16x2(ws_in + WS_WD + (size_t)(((idx >> 3) * 4 + (xcd >> 1)) * 2 + HALF) * WD_STREAM * 1024 + (w & 3) * 2048, voff, ldsb + 144 * 1024 + HALF * 8192 + (w & 3) * 2048);
      pv = load4_sc1(c1p);
    }
  };
  k3_unit<8, false, false, (HALF == 0 ? 18 : 9) % K3_Q, HALF == 0 ? 2 : 4>(acc, wq, sB, sB, lb, rot, voff, hook);
  tlmark(29); ckmark(3);
  wait_vm<0>();
  spin_ge(pv, c1p, 16u);
  k4_body<(HALF == 1 && K4_LATE)>(ws_in, ws_out, out, lds, w, rs_out, [&]() { epi_full(t1); ckmark(4); });
}

__global__ __launch_bounds__(512, 2) void k34_ffn(const char* __restrict__ ws_in, char* __restrict__ ws_out, u16* __restrict__ out) {
  asm volatile(".Lfk_text_begin:");
  TLSCOPE(3);
  extern __shared__ __attribute__((aligned(16))) char lds[];
  const int w = uni(threadIdx.x >> 6);
  if (w < 4) k3_body<0>(ws_in, ws_out, out, lds, w); else k3_body<1>(ws_in, ws_out, out, lds, w);
}
}
#define OUT_ST 2
#define OUT2_ST 0
#define GU_ST 2
#define PACK_ST 2
namespace fk {
constexpr size_t WS_PQ   = (WS_END_FFN + 4095) / 4096 * 4096;
constexpr size_t WS_KVP  = WS_PQ + SZ_HF;
constexpr size_t SZ_KVP  = (size_t)16 * 8 * 16 * 1024;
constexpr size_t WS_KVF  = WS_KVP + SZ_KVP;
constexpr size_t SZ_KVF  = 64 * 1024;
constexpr size_t WS_WOP  = WS_KVF + SZ_KVF;
constexpr size_t SZ_WOP  = (size_t)4 * 4 * 9 * 5 * 1024;
constexpr size_t WS_MX   = WS_WOP + SZ_WOP;
constexpr size_t WS_END  = WS_MX + S_ * 4;

DI float phi_f(float t) { return (t > 0.f ? t + 1.f : __builtin_amdgcn_exp2f(t * 1.4426950408889634f)) + 1e-6f; }

DI void pack_wo_task(const u16* wo, char* ws, int id) {
  const int lane = id & 63, fi = id >> 6, i = fi % 5, u = (fi / 5) % 9, w = (fi / 45) & 3, ng = fi / 180;
  const int row = lane & 31, h = lane >> 5;
  if (i == 4 && row >= 16) {
    if (row >= 18) store16<OUT_ST>(ws + WS_WOP + (size_t)id * 16, (u32x4){0u, 0u, 0u, 0u});
    return;
  }
  const int n = ng * 144 + i * 32 + row;
  unsigned v[8];
#pragma unroll
  for (int j = 0; j < 8; ++j) {
    const int e = u < 8 ? 18 * (8 * w + u) + 8 * (j >> 2) + 4 * h + (j & 3) : 18 * (8 * w + 4 * h + (j >> 1)) + 16 + (j & 1);
    v[j] = wo[e * D_ + n];
  }
  store16<OUT_ST>(ws + WS_WOP + (size_t)id * 16, (u32x4){v[0] | (v[1] << 16), v[2] | (v[3] << 16), v[4] | (v[5] << 16), v[6] | (v[7] << 16)});
}
DI void pack_worow_task(const u16* wo, char* ws, int e, int lane) {
  float s = 0.f;
#pragma unroll
  for (int i = 0; i < 9; ++i) s += bf2f(wo[e * D_ + i * 64 + lane]);
#pragma unroll
  for (int o = 32; o; o >>= 1) s += __shfl_xor(s, o);
  const unsigned hi = pk2(s, 0.f) & 0xffffu; const unsigned lo = pk2(s - bflo(hi), 0.f) & 0xffffu;
  const int hh = e / 18, el = e % 18, w = hh >> 3, ul = hh & 7;
  int u, h, j;
  if (el < 16) { u = ul; h = (el >> 2) & 1; j = (el >> 3) * 4 + (el & 3); } else { u = 8; h = ul >> 2; j = (ul & 3) * 2 + (el - 16); }
  if (lane < 8) {
    const int ng = lane >> 1, part = lane & 1;
    u16* p = (u16*)(ws + WS_WOP) + ((((size_t)(ng * 4 + w) * 9 + u) * 5 + 4) * 64 + h * 32 + 16 + part) * 8 + j;
    unsigned val = part ? lo : hi;
    asm volatile("global_store_short %0, %1, off sc1" :: "v"(p), "v"(val) : "memory");
  }
}

constexpr int K1_WST = 18432, K1_STG = K1_WST + 16384, K1_R = 4;
constexpr int K1_TAB = K1_STG * K1_R;
constexpr int K1_LDS = 8 * 18432;
static_assert(K1_TAB + 4 * 144 * 4 + 1024 <= K1_LDS, "lds");
#define GLX glds16
#define GLW glds16
#define K1_PORD 1
#define K1_NGU 64
#define K1_NOPACK 0
#define K1_DD 3
constexpr int K1_D = K1_DD;
DI float phi3(float u, float rs, float rl) { return fmaxf(fmaf(u, rs, 1e-6f), 1e-6f) + __builtin_amdgcn_exp2f(fminf(u * rl, 0.f)); }
template <bool ISQ, int NH> constexpr int k1_tile(int j) {
  if (ISQ) return NH ? 5 + j : j;
  if (NH == 0) return j < 2 ? j : (j == 2 ? 4 : j + 2);
  return j < 2 ? 2 + j : 5 + j;
}
template <bool ISQ> constexpr int k1_toff(int t) { return ISQ ? 32 * t : (t < 4 ? 32 * t : 144 + 32 * (t - 5)); }

__host__ __device__ constexpr int k1_img(int t) { return t < 8 ? t * 4096 : 3 * K1_STG + (t - 8) * 4096; }
template <bool ISQ, int NH, class ISSUE, class TAIL>
DI void k1_body(char* lds, unsigned ldsb, int lane, int tg, int rt, int grp, char* ws, ISSUE&& issue, TAIL&& tail) {
  constexpr int NT = NH ? 4 : 5, NWF = NT + K1_D;
  const int g = lane >> 4, l15 = lane & 15, q4 = (lane >> 2) & 3, p4 = lane & 3;
  const unsigned rrow = ldsb + (16 * (g >> 1) + 4 * (g & 1) + q4) * 288;
  unsigned rbA[2], rbB[2], rbX[2][2];
  rbA[0] = rrow + p4 * 8;
  rbB[0] = rrow + (p4 < 2 ? 128 + 8 * p4 : 272 + 8 * (p4 - 2));
  rbA[1] = rbA[0] + 2 * K1_STG; rbB[1] = rbB[0] + 2 * K1_STG;
  const unsigned xb = ldsb + K1_WST + tg * 4096 + l15 * 128;
#pragma unroll
  for (int kh = 0; kh < 2; ++kh) { rbX[kh][0] = xb + (((4 * kh + g) ^ ((l15 >> 1) & 7)) * 16); rbX[kh][1] = rbX[kh][0] + 2 * K1_STG; }
  asm volatile("" : "+v"(rbA[0]), "+v"(rbA[1]), "+v"(rbB[0]), "+v"(rbB[1]), "+v"(rbX[0][0]), "+v"(rbX[0][1]), "+v"(rbX[1][0]), "+v"(rbX[1][1]));
  u32x4 ones = {0x3f803f80u, 0x3f803f80u, 0x3f803f80u, 0x3f803f80u};
  pin(ones);
  f32x4 acc[NT][2], accs[NT], sst[2][2];
#pragma unroll
  for (int t = 0; t < NT; ++t) accs[t] = (f32x4){};
  u32x4 xf[2][2], wf[NWF];
  auto rd = [&](auto NI) {
    constexpr int n = decltype(NI)::value;
    if constexpr (n < 18 * NT) {
      constexpr int ks = n / NT, j = n % NT, st = ks >> 1, kh = ks & 1, slot = st % K1_R, hb = slot >> 1, so = (slot & 1) * K1_STG;
      constexpr int t = k1_tile<ISQ, NH>(j);
      constexpr int pos = n % (2 * NT);
      constexpr int nxt = st == 0 ? 3 : (st >= 2 && st + 2 < 9 ? st + 2 : -1);
      if constexpr (pos == 0) {
        constexpr int upto = st == 0 ? 2 : (st + 1 > 3 ? (st + 1 < 8 ? st + 1 : 8) : 3);
        if constexpr (NH == 1 && st == 8) tail(std::integral_constant<int, 0>{});
        else wait_vm<4 * (upto - st)>();
        bar();
        if constexpr (st == 0) tlmark(17);
        if constexpr (NH == 1 && st >= 7) tail(std::integral_constant<int, st>{});
      }
      if constexpr (j == 0) {
        xf[kh][0] = *(const LAS u32x4*)(size_t)(rbX[kh][hb] + so);
        xf[kh][1] = *(const LAS u32x4*)(size_t)(rbX[kh][hb] + so + 2048);
      }
      const unsigned a = ((!ISQ && t == 4) ? rbB[hb] : rbA[hb] + k1_toff<ISQ>(t)) + so + kh * 9216;
      u32x2 lo = __builtin_bit_cast(u32x2, tr_read(a)), hi = __builtin_bit_cast(u32x2, tr_read(a + 8 * 288));
      wf[n % NWF] = (u32x4){lo[0], lo[1], hi[0], hi[1]};
      if constexpr (nxt >= 0 && (pos & 1) == 1 && pos / 2 < 4) issue(std::integral_constant<int, nxt>{}, std::integral_constant<int, pos / 2>{});
    }
  };
  sfor<K1_D>([&](auto I) { rd(I); });
  sfor<18 * NT>([&](auto NI) {
    constexpr int n = decltype(NI)::value, ks = n / NT, j = n % NT, kh = ks & 1;
    rd(std::integral_constant<int, n + K1_D>{});
    const u32x4 wv = wf[n % NWF];
    sfor<2>([&](auto HI) {
      constexpr int th = decltype(HI)::value;
      const f32x4 c = ks == 0 ? (f32x4){} : acc[j][th];
      acc[j][th] = ISQ ? mfma16(wv, xf[kh][th], c) : mfma16(xf[kh][th], wv, c);
    });
    if constexpr (NH == 1 && j < 2) {
      sst[j][0] = mfma16(ones, xf[kh][j], ks == 0 ? (f32x4){} : sst[j][0]);
      sst[j][1] = mfma16(xf[kh][j], xf[kh][j], ks == 0 ? (f32x4){} : sst[j][1]);
    }
    if constexpr (j == NT - 1) {
      if (tg == (ks & 3)) {
        sfor<NT>([&](auto JI) { constexpr int jj = decltype(JI)::value; accs[jj] = mfma16(ones, wf[(ks * NT + jj) % NWF], accs[jj]); });
      }
    }
    sb0();
  });
  tlmark(16);
  float* tab = (float*)(lds + K1_TAB);
  float* stt = tab + 4 * 144;
  if (g == 0) {
    sfor<NT>([&](auto JI) { constexpr int jj = decltype(JI)::value; tab[tg * 144 + k1_tile<ISQ, NH>(jj) * 16 + l15] = accs[jj][0]; });
  }
  if constexpr (NH == 1) {
    if (g == (l15 >> 2)) {
#pragma unroll
      for (int th = 0; th < 2; ++th) {
        const int r = l15 & 3;
        const float q = r == 0 ? sst[th][1][0] : (r == 1 ? sst[th][1][1] : (r == 2 ? sst[th][1][2] : sst[th][1][3]));
        const float mean = sst[th][0][0] * (1.f / D_);
        stt[tg * 32 + th * 16 + l15] = rbf(mean);
        if (ISQ && grp == 0) ((float*)(ws + WS_MX))[rt * 128 + tg * 32 + th * 16 + l15] = mean;
        stt[128 + tg * 32 + th * 16 + l15] = ln_rstd(q * (1.f / D_) - mean * mean);
      }
    }
  }
  wait_lgkm0(); bar();
  constexpr float L2E = 1.4426950408889634f;
  if constexpr (ISQ) {
    const int tt = rt * 4 + tg;
    float nm[2], rs[2], rl[2];
#pragma unroll
    for (int th = 0; th < 2; ++th) { nm[th] = -stt[tg * 32 + th * 16 + l15]; rs[th] = stt[128 + tg * 32 + th * 16 + l15]; rl[th] = rs[th] * L2E; }
    char* pq = ws + WS_PQ + ((size_t)tt * 36 + grp * 9) * 1024 + ((g & 1) * 32 + (g >> 1) * 16 + l15) * 16;
    sfor<NT>([&](auto JI) {
      constexpr int j = decltype(JI)::value, t = k1_tile<ISQ, NH>(j);
      const float* cp = tab + t * 16 + g * 4;
      const f32x4 cs = (*(const f32x4*)cp + *(const f32x4*)(cp + 144)) + (*(const f32x4*)(cp + 288) + *(const f32x4*)(cp + 432));
      unsigned pk[2][2];
#pragma unroll
      for (int th = 0; th < 2; ++th) {
        float v[4];
#pragma unroll
        for (int e = 0; e < 4; ++e) v[e] = phi3(fmaf(nm[th], cs[e], acc[j][th][e]), rs[th], rl[th]);
        pk[th][0] = pk2(v[0], v[1]); pk[th][1] = pk2(v[2], v[3]);
      }
      auto r0 = __builtin_amdgcn_permlane32_swap(pk[0][0], pk[1][0], false, false);
      auto r1 = __builtin_amdgcn_permlane32_swap(pk[0][1], pk[1][1], false, false);
      store16<OUT_ST>(pq + t * 1024, (u32x4){r0[0], r1[0], r0[1], r1[1]});
    });
  } else {
    f32x4 mn[2], rs[2], rl[2];
#pragma unroll
    for (int th = 0; th < 2; ++th) { mn[th] = *(const f32x4*)(stt + tg * 32 + th * 16 + g * 4); rs[th] = *(const f32x4*)(stt + 128 + tg * 32 + th * 16 + g * 4); rl[th] = rs[th] * L2E; }
    char* ob = lds + (tg * 64 + lane) * 16;
    sfor<NT>([&](auto JI) {
      constexpr int j = decltype(JI)::value, t = k1_tile<ISQ, NH>(j);
      const float* cp = tab + t * 16 + l15;
      const float ncs = -((cp[0] + cp[144]) + (cp[288] + cp[432]));
      u32x4 pv;
#pragma unroll
      for (int th = 0; th < 2; ++th) {
        float v[4];
#pragma unroll
        for (int e = 0; e < 4; ++e) {
          const float u = fmaf(mn[th][e], ncs, acc[j][th][e]);
          if (t < 4) v[e] = phi3(u, rs[th][e], rl[th][e]);
          else if (t > 4) v[e] = u * rs[th][e];
          else v[e] = l15 < 8 ? phi3(u, rs[th][e], rl[th][e]) : u * rs[th][e];
        }
        pv[2 * th] = pk2(v[0], v[1]); pv[2 * th + 1] = pk2(v[2], v[3]);
      }
      *(u32x4*)(ob + k1_img(t)) = pv;
      if constexpr (t == 4) *(u32x4*)(ob + k1_img(9)) = l15 < 8 ? ones : pv;
    });
    wait_lgkm0(); bar();

    const int w = NH * 4 + tg;
#pragma unroll
    for (int pi = 0; pi < 2; ++pi) {
      const int p = 2 * w + pi;
      int a, bt;
      if (p < 10) { a = (p + 1) / 3; bt = 5 + a + ((p + 1) % 3) - 1; } else if (p == 10) { a = 4; bt = 8; } else { a = p - 11; bt = 9; }
      f32x4 c = {};
#pragma unroll
      for (int i = 0; i < 4; ++i) {
        const u32x4 av = *(const u32x4*)(lds + a * 4096 + (i * 64 + lane) * 16);
        const u32x4 bv = *(const u32x4*)(lds + k1_img(bt) + (i * 64 + lane) * 16);
        c = mfma16(av, bv, c);
      }
      if constexpr (OUT_ST != 0) asm volatile("s_nop 9" : "+v"(c));
      store16<OUT_ST>(ws + WS_KVP + (((size_t)rt * 8 + grp) * 16 + p) * 1024 + lane * 16, __builtin_bit_cast(u32x4, c));
    }
  }
}

__global__ __launch_bounds__(512, 2) void k1_qkv(const u16* __restrict__ x, const u16* __restrict__ wqkv, const u16* __restrict__ wo,
                                                  const u16* __restrict__ wg, const u16* __restrict__ wu, const u16* __restrict__ wd, char* __restrict__ ws) {
  TLSCOPE(0);
  extern __shared__ __attribute__((aligned(16))) char lds[];
  const int tid = threadIdx.x, lane = tid & 63, w = uni(tid >> 6);
  const int b = blockIdx.x, xcd = b & 7, idx = b >> 3;
  const unsigned ldsb = (unsigned)(size_t)(LAS char*)lds;
  if (idx >= 24) {
    {
      const char* c0; unsigned len; text_range(c0, len);
      const unsigned o = (unsigned)((((idx - 24) * 8 + w) * 64 + lane) * 128);
      if ((unsigned)(((idx - 24) * 8 + w) * 8192) < len) l2touch(c0, o < len ? o : 0u, ldsb);
    }
    const int pw = ((idx - 24) * 8 + xcd) * 8 + w;
    if (pw < 8) ((unsigned*)(ws + WS_CNT))[(pw * 64 + lane) * 32] = 0u;
    const int u = (idx - 24) * 8 + w;
    pack_gu_load(wg, wu, xcd * 9 + u % 9, u / 9, ldsb + w * 16384 + 256, lane);
    for (int id = pw * 64 + lane; id < 4 * 4 * 9 * 5 * 64; id += 512 * 64) pack_wo_task(wo, ws, id);
    for (int e = pw; e < D_; e += 512) pack_worow_task(wo, ws, e, lane);
    tlmark(18);
    wait_vm<0>();
    pack_gu_store<GU_ST, false>(ws, xcd * 9 + u % 9, u / 9, ldsb + w * 16384 + 256, lane);
    tlmark(19);
    return;
  }
  const int rt = (xcd & 3) * 4 + (idx & 3), cgl = idx >> 2;
  const int ch = xcd >> 2;
  const bool isq = cgl < 2;
  const int grp = isq ? ch * 2 + cgl : ch * 4 + (cgl - 2);
  auto wsrc_off = [&](int n) -> unsigned {
    const int c = n * 64 + lane, rho = c / 18, cc = c % 18;
    const int k = (rho & ~12) | ((rho & 4) << 1) | ((rho & 8) >> 1);
    const int col = isq ? 144 * grp + 8 * cc : (cc < 9 ? 576 + 72 * grp + 8 * cc : 1152 + 72 * grp + 8 * (cc - 9));
    return (unsigned)((k * 1728 + col) * 2);
  };
  const char* xsrc = (const char*)x + (size_t)(rt * 128 + w * 16) * 1152;
  const unsigned xoff0 = (lane >> 3) * 1152 + (((lane & 7) ^ (lane >> 4)) * 16);
  const unsigned xoff1 = ((lane >> 3) + 8) * 1152 + (((lane & 7) ^ (4 + (lane >> 4))) * 16);
#define K1_ROT (((idx & 3) * 2 + cgl) % 9)
  const int rot = K1_ROT;
  glds16(xsrc + rot * 128, xoff0, ldsb + K1_WST + w * 2048);
  glds16(xsrc + rot * 128, xoff1, ldsb + K1_WST + w * 2048 + 1024);
  const unsigned woffA = wsrc_off(w), woffB = wsrc_off(w + 8), woffC = wsrc_off(16 + (w & 1));
  auto issue = [&](auto SI, auto PI) {
    constexpr int st = decltype(SI)::value, slot = st % K1_R, pc = decltype(PI)::value;
    const int sa = st + rot >= 9 ? st + rot - 9 : st + rot;
    const char* wb = (const char*)wqkv + (size_t)sa * 64 * 3456;
    const unsigned sb = ldsb + slot * K1_STG;
    if constexpr (pc == 0) GLX(xsrc + sa * 128, xoff0, sb + K1_WST + w * 2048);
    if constexpr (pc == 1) GLX(xsrc + sa * 128, xoff1, sb + K1_WST + w * 2048 + 1024);
    if constexpr (pc == 2) { if (w < 2) GLW(wb, woffC, sb + (16 + w) * 1024); GLW(wb, woffA, sb + w * 1024); }
    if constexpr (pc == 3) GLW(wb, woffB, sb + (w + 8) * 1024);
  };
  issue(std::integral_constant<int, 0>{}, std::integral_constant<int, 2>{});
  issue(std::integral_constant<int, 0>{}, std::integral_constant<int, 3>{});
  bar();
  sfor<4>([&](auto PI) { issue(std::integral_constant<int, 1>{}, PI); });
  sfor<4>([&](auto PI) { issue(std::integral_constant<int, 2>{}, PI); });
  const int tg = w & 3;
  const int pslot = w == 5 ? idx : (w == 6 ? (idx < 21 ? 24 + idx : 99) : ((w == 7 && isq) ? 45 + idx : 99));
  const unsigned pl = ldsb + (w == 6 ? 2 * K1_STG : K1_STG + (w == 7 ? 18432 : 0));
  auto tail = [&](auto SI) {
    constexpr int st = decltype(SI)::value;
    if constexpr (st == 0) { if (w == 5) wait_vm<18>(); else if (pslot < 99 && w == 7) wait_vm<16>(); else wait_vm<0>(); }
    else if constexpr (st == 9) {
      wait_vm<0>();
      if (pslot < 36) pack_wd_store<PACK_ST>(ws, xcd * 18 + (pslot >> 1), pslot & 1, pl, lane);
      else if (pslot < 53) pack_gu_store<GU_ST, false>(ws, xcd * 9 + (K1_NGU + pslot - 36) % 9, (K1_NGU + pslot - 36) / 9, pl, lane);
    } else if ((w == 6) == (st == 8)) {
      if (pslot < 36) pack_wd_load(wd, xcd * 18 + (pslot >> 1), pslot & 1, pl, lane);
      else if (pslot < 53) pack_gu_load(wg, wu, xcd * 9 + (K1_NGU + pslot - 36) % 9, (K1_NGU + pslot - 36) / 9, pl, lane);
    }
  };
  static_assert(K1_NGU == 64, "tail slot map");
  if (isq) { if (w < 4) k1_body<true, 0>(lds, ldsb, lane, tg, rt, grp, ws, issue, tail); else k1_body<true, 1>(lds, ldsb, lane, tg, rt, grp, ws, issue, tail); }
  else     { if (w < 4) k1_body<false, 0>(lds, ldsb, lane, tg, rt, grp, ws, issue, tail); else k1_body<false, 1>(lds, ldsb, lane, tg, rt, grp, ws, issue, tail); }
  tlmark(30);
  tail(std::integral_constant<int, 9>{});
}

constexpr int K1B_LDS = 4 * 18432;
__global__ __launch_bounds__(256) void k1b_reduce(const u16* __restrict__ wg, const u16* __restrict__ wu, const u16* __restrict__ wd, char* __restrict__ ws) {
  TLSCOPE(1);
  extern __shared__ __attribute__((aligned(16))) char lds[];
  const int tid = threadIdx.x;
  const int b = blockIdx.x;
  const int hh = b, s = tid >> 7, l = (tid >> 1) & 63, r = tid & 1;
  const int row = l & 31, h = l >> 5;
  const int T = (18 * hh) / 16 + s;
  const int d0 = 16 * T + 8 * r + 4 * h;
  const int G = hh >> 2, hl = hh & 3;
  int el = -1; bool isz = false;
  if (row < 16) el = row;
  else if (row == 18 || row == 22) isz = true;
  else if ((hh & 7) < 4 ? (row == 16 || row == 17) : (row == 20 || row == 21)) el = 16 + (row & 1);
  const int dlo = 18 * hh, dhi = dlo + 18;
  f32x4 sum = {};
  if ((el >= 0 || isz) && d0 + 3 >= dlo && d0 < dhi) {
    const int dk = d0 - 72 * G, a = dk >> 4, dr = dk & 15;
    int p, col;
    if (isz) { p = 11 + a; col = 0; }
    else {
      const int ev = 18 * hl + el;
      if (ev >= 64) { p = 11 + a; col = 8 + ev - 64; }
      else { const int vt = ev >> 4; col = ev & 15; p = a == 4 ? 10 : 3 * a + (vt - a); }
    }
    const char* src = ws + WS_KVP + ((size_t)G * 16 + p) * 1024 + ((dr >> 2) * 16 + col) * 16;
    f32x4 v[16];
#pragma unroll
    for (int i = 0; i < 16; ++i) v[i] = *(const f32x4*)(src + (size_t)i * 8 * 16 * 1024);
#pragma unroll
    for (int i = 0; i < 16; ++i) sum += v[i];
#pragma unroll
    for (int j = 0; j < 4; ++j) if (d0 + j < dlo || d0 + j >= dhi) sum[j] = 0.f;
  }
  *(u32x2*)(ws + WS_KVF + ((size_t)(hh * 2 + s) * 64 + l) * 16 + r * 8) = (u32x2){pk2(sum[0], sum[1]), pk2(sum[2], sum[3])};
}

#define K2_WIN 16
constexpr int K2_LDS = 4 * 19 * 1024;
__global__ __launch_bounds__(256, 1) void k2_attn_out(const u16* __restrict__ x, const char* __restrict__ ws_in, char* __restrict__ ws) {
  TLSCOPE(2);
  extern __shared__ __attribute__((aligned(16))) char lds[];
  const int tid = threadIdx.x, lane = tid & 63, w = uni(tid >> 6);
  const int b = blockIdx.x, xcd = b & 7, idx = b >> 3;
  const int tt = xcd * 8 + (idx & 7), ng = idx >> 3;
  const int tok = lane & 31, h = lane >> 5;
  const char* pqp = ws_in + WS_PQ + ((size_t)tt * 36 + w * 9) * 1024;
  const char* kvp = ws_in + WS_KVF + (size_t)w * 16 * 1024;
  const char* wop = ws_in + WS_WOP + (size_t)(ng * 4 + w) * 45 * 1024;
  const unsigned voff = lane * 16, voffc = (tok < 24 ? lane : (lane & 32)) * 16;
  u32x4 pq[9], kvf[16], wf[45];
  auto issue = [&](auto II) {
    constexpr int i = decltype(II)::value;
    if constexpr (i == 0) gload16<0>(pq[0], pqp, voff);
    else if constexpr (i < 70) {
      constexpr int u = (i - 1) / 8, k = (i - 1) % 8;
      if constexpr (u < 8 && k < 2) gload16<((2 * u + k) % 4) * 1024>(kvf[2 * u + k], kvp + ((2 * u + k) / 4) * 4096, voffc);
      else if constexpr (u < 8 && k == 2) gload16<((u + 1) % 4) * 1024>(pq[u + 1], pqp + ((u + 1) / 4) * 4096, voff);
      else { constexpr int n = u < 8 ? 5 * u + k - 3 : 40 + k; gload16<(n % 4) * 1024>(wf[n], wop + (n / 4) * 4096, n % 5 == 4 ? voffc : voff); }
    }
  };
  const int u0 = w == 0 ? 0 : 2 * w + 1;
  u32x4 xr[3]; unsigned mxb;
  sfor<4>([&](auto I) { issue(I); });
  {
    const char* xb0 = (const char*)x + (size_t)tt * 32 * 1152 + ng * 288 + u0 * 32;
    const unsigned xo = tok * 1152 + h * 16;
    gload16<0>(xr[0], xb0, xo); gload16<32>(xr[1], xb0, xo);
    gload16<0>(xr[2], xb0 + (w == 0 ? 64 : 0), xo);
    gload4<0>(mxb, ws_in + WS_MX + (size_t)tt * 128, tok * 4);
  }
  sfor<K2_WIN - 4>([&](auto I) { issue(std::integral_constant<int, 4 + decltype(I)::value>{}); });
  u32x4 ofr; unsigned lo[8];
  f32x16 acc[5], c;
  auto ostep = [&](auto UI) {
    constexpr int u = decltype(UI)::value;
    if constexpr (u == 0) pin(pq[0]);
    pin(kvf[2 * u]); pin(kvf[2 * u + 1]); pin(pq[u + 1]);
    c = mfma32(kvf[2 * u], pq[u], (f32x16){});
    c = mfma32(kvf[2 * u + 1], pq[u + 1], c);
  };
  wait_vm<K2_WIN - 4 + 4>();
  ostep(std::integral_constant<int, 0>{});
  sfor<9>([&](auto UI) {
    constexpr int u = decltype(UI)::value;
    if constexpr (u < 8) {
      const float r = __builtin_amdgcn_rcpf(c[10] + 1e-6f);
      ofr = (u32x4){pk2(c[0] * r, c[1] * r), pk2(c[2] * r, c[3] * r), pk2(c[4] * r, c[5] * r), pk2(c[6] * r, c[7] * r)};
      lo[u] = pk2(c[8] * r, c[9] * r);
    } else {
      sfor<4>([&](auto J) { constexpr int j = decltype(J)::value; unsigned a = lo[j], d = lo[4 + j]; pin(a); pin(d); ofr[j] = h ? d : a; });
    }
    sfor<8>([&](auto KI) { issue(std::integral_constant<int, K2_WIN + 8 * u + decltype(KI)::value>{}); });
    constexpr int issued = (K2_WIN + 8 * (u + 1)) < 70 ? (K2_WIN + 8 * (u + 1)) : 70;
    constexpr int need = u < 7 ? 8 * (u + 1) + 4 : (u == 7 ? 65 : 70);
    wait_vm<issued - need>();
    if constexpr (u < 7) ostep(std::integral_constant<int, u + 1>{});
    sfor<5>([&](auto NI) {
      constexpr int i = decltype(NI)::value, n = 5 * u + i;
      pin(wf[n]);
      acc[i] = mfma32(wf[n], ofr, u == 0 ? (f32x16){} : acc[i]);
    });
    sb0();
  });
#pragma unroll
  for (int i = 0; i < 3; ++i) pin(xr[i]);
  pin(mxb);
  const float mx = __uint_as_float(mxb);
  f32x4* xb = (f32x4*)lds;
#pragma unroll
  for (int q = 0; q < 19; ++q) {
    const int i = q >> 2, rb = (q & 3) * 4;
    xb[(w * 19 + q) * 64 + lane] = (f32x4){acc[i][rb], acc[i][rb + 1], acc[i][rb + 2], acc[i][rb + 3]};
  }
  tlmark(31);
#pragma unroll
  for (int i = 0; i < 3; ++i) {
    auto r0 = __builtin_amdgcn_permlane32_swap(xr[i][0], xr[i][2], false, false);
    auto r1 = __builtin_amdgcn_permlane32_swap(xr[i][1], xr[i][3], false, false);
    xr[i] = (u32x4){r0[0], r1[0], r0[1], r1[1]};
  }
  wait_lgkm0(); bar();
  float ms = 0.f;
#pragma unroll
  for (int k = 0; k < 4; ++k) { const f32x4 v = xb[(k * 19 + 18) * 64 + tok]; ms += v[0] + v[1]; }
  const float mean = rbf(mx + ms * (1.f / D_));
  if (ng == 0 && w == 0 && h == 0) ((float*)(ws + WS_MEAN))[tt * 32 + tok] = mean;
  float s1 = 0.f, s2 = 0.f;
#pragma unroll
  for (int i = 0; i < 3; ++i) {
    if (i < 2 || w == 0) {
      const int u = u0 + i;
      f32x4 a = {}, d = {};
#pragma unroll
      for (int k = 0; k < 4; ++k) { a += xb[(k * 19 + 2 * u) * 64 + lane]; d += xb[(k * 19 + 2 * u + 1) * 64 + lane]; }
      const unsigned at[4] = {pk2(a[0], a[1]), pk2(a[2], a[3]), pk2(d[0], d[1]), pk2(d[2], d[3])};
      u32x4 o;
#pragma unroll
      for (int j = 0; j < 4; ++j) {
        const unsigned xv = xr[i][j];
        const unsigned hb = pk2(bflo(xv) + bflo(at[j]), bfhi(xv) + bfhi(at[j]));
        s1 = dot2_ones(hb, s1); s2 = __builtin_amdgcn_fdot2_f32_bf16(as_bf2(hb), as_bf2(hb), s2, false);
        o[j] = pk2(bflo(hb) - mean, bfhi(hb) - mean);
      }
      store16<OUT2_ST>(ws + WS_HF + (((size_t)tt * 36 + ng * 9 + u) * 64 + lane) * 16, o);
    }
  }
  s1 += __shfl_xor(s1, 32); s2 += __shfl_xor(s2, 32);
  if (h == 0) *(f32x2*)(ws + WS_HST + ((size_t)(tt * 32 + tok) * 16 + ng * 4 + w) * 8) = (f32x2){s1, s2};
  asm volatile(".Lfk_text_end:");
}
}
extern "C" void kernel_launch(void* const* d_in, const int* in_sizes, int n_in, void* d_out, int out_size, void* d_ws, size_t ws_size, hipStream_t st) {
  using namespace fk;
  const u16* x = (const u16*)d_in[0]; const u16* wqkv = (const u16*)d_in[1]; const u16* wo = (const u16*)d_in[2];
  const u16* wg = (const u16*)d_in[4]; const u16* wu = (const u16*)d_in[5]; const u16* wd = (const u16*)d_in[6];
  char* ws = (char*)d_ws;
  (void)hipFuncSetAttribute((const void*)k1_qkv, hipFuncAttributeMaxDynamicSharedMemorySize, K1_LDS);
  (void)hipFuncSetAttribute((const void*)k1b_reduce, hipFuncAttributeMaxDynamicSharedMemorySize, K1B_LDS);
  (void)hipFuncSetAttribute((const void*)k2_attn_out, hipFuncAttributeMaxDynamicSharedMemorySize, K2_LDS);
  (void)hipFuncSetAttribute((const void*)k34_ffn, hipFuncAttributeMaxDynamicSharedMemorySize, K3_LDS);
  k1_qkv<<<256, 512, K1_LDS, st>>>(x, wqkv, wo, wg, wu, wd, ws);

  k1b_reduce<<<32, 256, 0, st>>>(wg, wu, wd, ws);
  k2_attn_out<<<256, 256, K2_LDS, st>>>(x, ws, ws);
  k34_ffn<<<256, 512, K3_LDS, st>>>(ws, ws, (u16*)d_out);
}
```

```cpp
#include <hip/hip_runtime.h>
#include <hip/hip_bf16.h>
#include <stdint.h>
#include <type_traits>
#include <utility>

#define LN_SOFT 1.5e-3f
namespace fk {
typedef unsigned short u16;
typedef short bf16x8 __attribute__((ext_vector_type(8)));
typedef short s16x4 __attribute__((ext_vector_type(4)));
typedef float f32x4 __attribute__((ext_vector_type(4)));
typedef float f32x2 __attribute__((ext_vector_type(2)));
typedef float f32x16 __attribute__((ext_vector_type(16)));
typedef unsigned u32x4 __attribute__((ext_vector_type(4)));
typedef unsigned u32x2 __attribute__((ext_vector_type(2)));
typedef __bf16 bf16x2v __attribute__((ext_vector_type(2)));
#define LAS __attribute__((address_space(3)))
#define DI __device__ __forceinline__

constexpr int S_ = 2048, D_ = 576, H_ = 32, DH_ = 18, F_ = 4608;

template <int N, class Fn, int... I> DI void sfor_impl(Fn&& f, std::integer_sequence<int, I...>) { (f(std::integral_constant<int, I>{}), ...); }
template <int N, class Fn> DI void sfor(Fn&& f) { sfor_impl<N>(f, std::make_integer_sequence<int, N>{}); }

template <int N> DI void wait_vm() { asm volatile("s_waitcnt vmcnt(%0)" ::"i"(N) : "memory"); }
DI void wait_lgkm0() { asm volatile("s_waitcnt lgkmcnt(0)" ::: "memory"); }
DI void bar() { asm volatile("" ::: "memory"); __builtin_amdgcn_s_barrier(); asm volatile("" ::: "memory"); }
template <class T> DI void pin(T& v) { asm volatile("" : "+v"(v)); }

DI void glds16(const void* sbase, unsigned voff, unsigned ldsdst) {
  asm volatile("s_mov_b32 m0, %2\n\ts_nop 0\n\tglobal_load_lds_dwordx4 %0, %1" :: "v"(voff), "s"(sbase), "s"(ldsdst) : "memory");
}
DI void glds16nt(const void* sbase, unsigned voff, unsigned ldsdst) {
  asm volatile("s_mov_b32 m0, %2\n\ts_nop 0\n\tglobal_load_lds_dwordx4 %0, %1 nt" :: "v"(voff), "s"(sbase), "s"(ldsdst) : "memory");
}
DI void glds16x2(const void* sbase, unsigned voff, unsigned ldsdst) {
  asm volatile("s_mov_b32 m0, %2\n\ts_nop 0\n\tglobal_load_lds_dwordx4 %0, %1\n\tglobal_load_lds_dwordx4 %0, %1 offset:1024" :: "v"(voff), "s"(sbase), "s"(ldsdst) : "memory");
}
DI void sb0() { __builtin_amdgcn_sched_barrier(0); }
template <int OFF> DI void gload16(u32x4& dst, const void* sbase, unsigned voff) {
  asm volatile("global_load_dwordx4 %0, %1, %2 offset:%3" : "=v"(dst) : "v"(voff), "s"(sbase), "i"(OFF) : "memory");
}
template <int OFF> DI void gload8(u32x2& dst, const void* sbase, unsigned voff) {
  asm volatile("global_load_dwordx2 %0, %1, %2 offset:%3" : "=v"(dst) : "v"(voff), "s"(sbase), "i"(OFF) : "memory");
}
template <int OFF> DI void gload4(unsigned& dst, const void* sbase, unsigned voff) {
  asm volatile("global_load_dword %0, %1, %2 offset:%3" : "=v"(dst) : "v"(voff), "s"(sbase), "i"(OFF) : "memory");
}
DI void l2touch(const void* sbase, unsigned voff, unsigned ldsdummy) {
  asm volatile("s_mov_b32 m0, %2\n\ts_nop 0\n\tglobal_load_lds_dword %0, %1" :: "v"(voff), "s"(sbase), "s"(ldsdummy) : "memory");
}
DI void text_range(const char*& base, unsigned& len) {
  const char* pc; int d0, d1;
  asm volatile("s_getpc_b64 %0\n.Lfkpc%=:\n\ts_mov_b32 %1, .Lfk_text_begin-.Lfkpc%=\n\ts_mov_b32 %2, .Lfk_text_end-.Lfkpc%=" : "=s"(pc), "=s"(d0), "=s"(d1));
  base = pc + d0; len = (unsigned)(d1 - d0);
}
template <int MODE> DI void store16(void* p, u32x4 v) {
  if constexpr (MODE == 0) *(u32x4*)p = v;
  else asm volatile("global_store_dwordx4 %0, %1, off sc1\n\ts_nop 1" :: "v"(p), "v"(v) : "memory");
}
DI float bf2f(u16 v) { return __uint_as_float(((unsigned)v) << 16); }
DI float bflo(unsigned v) { return __uint_as_float(v << 16); }
DI float bfhi(unsigned v) { return __uint_as_float(v & 0xffff0000u); }
DI unsigned pk2(float lo, float hi) {
  bf16x2v r; r[0] = (__bf16)lo; r[1] = (__bf16)hi; return __builtin_bit_cast(unsigned, r);
}
DI float rbf(float v) { return bflo(pk2(v, 0.f)); }
DI float ln_rstd(float var) { return __builtin_amdgcn_rcpf(rbf(__builtin_amdgcn_sqrtf(rbf(rbf(fmaxf(var, 0.f)) + 1e-5f)))); }
DI float ln_den(float var) { return rbf(__builtin_amdgcn_sqrtf(rbf(rbf(var) + 1e-5f))); }
DI float ln_rstd_soft(float var) { var = fmaxf(var, 0.f); return __builtin_amdgcn_rcpf(0.5f * (ln_den(var * (1.f - LN_SOFT)) + ln_den(var * (1.f + LN_SOFT)))); }
DI s16x4 tr_read(unsigned lds_addr) {
  return __builtin_amdgcn_ds_read_tr16_b64_v4i16((LAS s16x4*)(size_t)lds_addr);
}
DI f32x16 mfma32(u32x4 a, u32x4 b, f32x16 c) { return __builtin_amdgcn_mfma_f32_32x32x16_bf16(__builtin_bit_cast(bf16x8, a), __builtin_bit_cast(bf16x8, b), c, 0, 0, 0); }
DI f32x4 mfma16(u32x4 a, u32x4 b, f32x4 c) { return __builtin_amdgcn_mfma_f32_16x16x32_bf16(__builtin_bit_cast(bf16x8, a), __builtin_bit_cast(bf16x8, b), c, 0, 0, 0); }
DI int uni(int v) { return __builtin_amdgcn_readfirstlane(v); }

DI bf16x2v as_bf2(unsigned v) { return __builtin_bit_cast(bf16x2v, v); }
DI float dot2_ones(unsigned v, float c) { return __builtin_amdgcn_fdot2_f32_bf16(as_bf2(v), as_bf2(0x3f803f80u), c, false); }

constexpr size_t WS_HF   = 0;
constexpr size_t SZ_HF   = (size_t)S_ * D_ * 2;
constexpr size_t WS_HST  = WS_HF + SZ_HF;
constexpr size_t SZ_HST  = (size_t)S_ * 16 * 8;
constexpr size_t WS_WGU  = WS_HST + SZ_HST;
constexpr size_t SZ_WGU  = (size_t)288 * 36 * 1024;
constexpr int    WD_STREAM = 168;
constexpr size_t WS_WD   = WS_WGU + SZ_WGU + 16384;
constexpr size_t SZ_WD   = (size_t)32 * WD_STREAM * 1024;
constexpr size_t WS_ACT  = WS_WD + SZ_WD;
constexpr size_t SZ_ACT  = (size_t)S_ * F_ * 2;
constexpr size_t WS_PART = WS_ACT + SZ_ACT;
constexpr size_t SZ_PART = (size_t)2048 * 5120;
constexpr size_t WS_MEAN = WS_PART + SZ_PART;
constexpr size_t WS_CNT  = WS_MEAN + S_ * 4;
constexpr size_t WS_END_FFN = WS_CNT + 576 * 128;
__host__ __device__ constexpr int k4_act(int v) { return v < 10 ? (v / 5) * 9 + (v % 5 < 4 ? v % 5 : 8) : ((v - 10) / 4) * 9 + 4 + (v - 10) % 4; }
DI int k4_virt(int ks) { const int j = ks / 9, p = ks % 9; return p < 4 ? j * 5 + p : (p == 8 ? j * 5 + 4 : 10 + j * 4 + p - 4); }

DI void pack_gu_load(const u16* wg, const u16* wu, int lc, int kp, unsigned ldsb, int lane) {
  const unsigned voff = (unsigned)(((kp * 64 + (lane >> 3)) * F_ + lc * 64 + (lane & 7) * 8) * 2);
  sfor<16>([&](auto I) {
    constexpr int i = decltype(I)::value, s = i >> 3, mat = (i >> 2) & 1, r8 = s * 4 + (i & 3);
    glds16nt((const char*)(mat ? wu : wg) + (size_t)r8 * 8 * F_ * 2, voff, ldsb + mat * 8192 + r8 * 1024);
  });
}
template <int ST, bool SPLITWAIT> DI void pack_gu_store(char* ws, int lc, int kp, unsigned ldsb, int lane) {
  const int g = lane >> 4;
  const unsigned rbase = ldsb + (16 * (g >> 1) + 4 * (g & 1) + ((lane >> 2) & 3)) * 128 + (lane & 3) * 8;
  char* dst = ws + WS_WGU + ((size_t)(lc * 4) * 18 + kp * 2) * 2048 + lane * 16;
  sfor<2>([&](auto I) {
    constexpr int s = decltype(I)::value;
    if constexpr (SPLITWAIT) wait_vm<8>();
    u32x4 v[8];
    sfor<8>([&](auto J) {
      constexpr int t = decltype(J)::value >> 1, mat = decltype(J)::value & 1;
      u32x2 a = __builtin_bit_cast(u32x2, tr_read(rbase + s * 4096 + mat * 8192 + t * 32)), b = __builtin_bit_cast(u32x2, tr_read(rbase + s * 4096 + mat * 8192 + t * 32 + 1024));
      v[decltype(J)::value] = (u32x4){a[0], a[1], b[0], b[1]};
    });
    sfor<8>([&](auto J) {
      constexpr int t = decltype(J)::value >> 1, mat = decltype(J)::value & 1;
      store16<ST>(dst + (t * 18 + s) * 2048 + mat * 1024, v[decltype(J)::value]);
    });
  });
}
DI void pack_wd_load(const u16* wd, int S, int c2, unsigned ldsb, int lane) {
  const char* base = (const char*)wd + ((size_t)S * 32 * D_ + c2 * 288) * 2;
  sfor<9>([&](auto I) {
    constexpr int n = decltype(I)::value;
    const int c = n * 64 + lane, row = c / 36, cc = c % 36;
    const unsigned vo = (unsigned)(row * D_ * 2 + cc * 16);
    glds16nt(base, vo, ldsb + n * 1024);
    glds16nt(base + 16 * D_ * 2, vo, ldsb + 9216 + n * 1024);
  });
}
template <int ST> DI void pack_wd_store(char* ws, int S, int c2, unsigned ldsb, int lane) {
  const int g = lane >> 4, q = (lane >> 2) & 3, p = lane & 3;
  const unsigned rb = ldsb + (16 * (g >> 1) + 4 * (g & 1) + q) * 576;
  const int ksl = S / 36, kh = (S % 36) / 18, ks = S % 18;
  char* dst = ws + WS_WD + ((size_t)((c2 * 2 * 4 + ksl) * 2 + kh) * WD_STREAM + k4_virt(ks) * 9) * 1024 + lane * 16;
  u32x4 v[18];
  sfor<18>([&](auto I) {
    constexpr int ngl = decltype(I)::value / 9, t = decltype(I)::value % 9;
    const unsigned a0 = rb + ngl * 288 + (t < 8 ? 64 * (t >> 1) + 16 * p + 8 * (t & 1) : 256 + 8 * p);
    u32x2 a = __builtin_bit_cast(u32x2, tr_read(a0)), b = __builtin_bit_cast(u32x2, tr_read(a0 + 8 * 576));
    v[decltype(I)::value] = (u32x4){a[0], a[1], b[0], b[1]};
  });
  sfor<18>([&](auto I) {
    constexpr int ngl = decltype(I)::value / 9, t = decltype(I)::value % 9;
    store16<ST>(dst + (size_t)ngl * 8 * WD_STREAM * 1024 + t * 1024, v[decltype(I)::value]);
  });
}

DI u32x4 load16_sc1(const void* p) { u32x4 v; asm volatile("global_load_dwordx4 %0, %1, off sc1" : "=v"(v) : "v"(p) : "memory"); return v; }
DI unsigned load4_sc1(const void* p) { unsigned v; asm volatile("global_load_dword %0, %1, off sc1" : "=v"(v) : "v"(p) : "memory"); return v; }
DI void store8_sc1(void* p, u32x2 v) { asm volatile("global_store_dwordx2 %0, %1, off sc1\n\ts_nop 1" :: "v"(p), "v"(v) : "memory"); }

DI void sig_add(unsigned* p) {
  unsigned long long keep; unsigned one = 1u;
  asm volatile("s_mov_b64 %0, exec\n\ts_mov_b64 exec, 1\n\tglobal_atomic_add %1, %2, off\n\ts_mov_b64 exec, %0" : "=&s"(keep) : "v"(p), "v"(one) : "memory");
}
DI void spin_ge(unsigned& v, const unsigned* p, unsigned need) {
  unsigned t, n;
  asm volatile("s_mov_b32 %2, 0x40000\n\ts_nop 0\n"
               "1:\n\tv_readfirstlane_b32 %1, %0\n\ts_nop 3\n\ts_cmp_ge_u32 %1, %4\n\ts_cbranch_scc1 2f\n\t"
               "s_sub_u32 %2, %2, 1\n\ts_cbranch_scc1 2f\n\ts_sleep 1\n\t"
               "global_load_dword %0, %3, off sc1\n\ts_waitcnt vmcnt(0)\n\ts_branch 1b\n"
               "2:" : "+v"(v), "=&s"(t), "=&s"(n) : "v"(p), "s"(need) : "memory", "scc");
}
template <int OFF> DI void gload16_sc1(u32x4& dst, const void* sbase, unsigned voff) {
  asm volatile("global_load_dwordx4 %0, %1, %2 offset:%3 sc1" : "=v"(dst) : "v"(voff), "s"(sbase), "i"(OFF) : "memory");
}
constexpr int K3_LDS = 160 * 1024;
#define K3_P 4
constexpr int K3_Q = K3_P + 1;
DI void k3_wload(u32x4 (&dst)[4], const char* base, int s, int rot, unsigned voff) {
  const int sa = s + rot >= 9 ? s + rot - 9 : s + rot;
  sfor<4>([&](auto FI) { constexpr int f = decltype(FI)::value; gload16<f * 1024>(dst[f], base + sa * 4096, voff); });
}
template <int NTK, bool FIRST, bool NEXT, int QB, int NST, class HL>
DI void k3_unit(f32x4 (&acc)[2][8], u32x4 (&wq)[K3_Q][4], const char* cur, const char* nxt, const unsigned (&lb)[3], int rot, unsigned voff, HL&& hload) {
  constexpr int NG = NTK / 4;
  u32x4 af[2][4];
  auto aread = [&](auto II) {
    constexpr int it = decltype(II)::value, ks = it / NG, gp = it % NG;
#pragma unroll
    for (int tk = 0; tk < 4; ++tk) af[it & 1][tk] = *(const LAS u32x4*)(size_t)(lb[ks / 6] + ((ks % 6) * 8 + gp * 4 + tk) * 1024);
  };
  aread(std::integral_constant<int, 0>{});
  sfor<9>([&](auto SI) {
    constexpr int s = decltype(SI)::value;
    if constexpr (FIRST) {
      if constexpr (s == 1) { wait_vm<4>(); bar(); }
      if constexpr (s + 2 < 9) k3_wload(wq[(QB + s + 2) % K3_Q], cur, s + 2, rot, voff);
      if constexpr (s < 2) sfor<6>([&](auto HI) { hload(std::integral_constant<int, 6 + 6 * s + decltype(HI)::value>{}); });
      constexpr int n0 = s < 6 ? 0 : (K3_P * (s - 6)) / 3, n1 = s < 6 ? 0 : (K3_P * (s - 5)) / 3;
      constexpr int m0 = K3_P == 2 ? (s < 7 ? 0 : s - 7) : n0, m1 = K3_P == 2 ? (s < 7 ? 0 : s - 6) : n1;
      sfor<m1 - m0>([&](auto JI) { constexpr int j = m0 + decltype(JI)::value; static_assert(9 + j - K3_Q < s, "slot busy"); k3_wload(wq[(QB + 9 + j) % K3_Q], nxt, j, rot, voff); });
      if constexpr (s == 0) wait_vm<2 + 4 + 4 + 6>();
      else if constexpr (s >= 3) wait_vm<(s <= 6 ? 8 : (s == 7 ? 4 : 0)) + 4 * m1>();
      if constexpr (s == 3) bar();
    } else {
      if constexpr (s + K3_P < 9) k3_wload(wq[(QB + s + K3_P) % K3_Q], cur, s + K3_P, rot, voff);
      else if constexpr (NEXT) k3_wload(wq[(QB + s + K3_P) % K3_Q], nxt, s + K3_P - 9, rot, voff);
      constexpr int ny = NEXT ? K3_P : ((8 - s) < K3_P ? (8 - s) : K3_P);
      wait_vm<4 * ny + (s < K3_P ? NST : 0)>();
      if constexpr (!NEXT && s == K3_P) hload(std::integral_constant<int, 0>{});
      if constexpr (!NEXT && s == 6) hload(std::integral_constant<int, 1>{});
    }
    sfor<4>([&](auto FI) { pin(wq[(QB + s) % K3_Q][decltype(FI)::value]); });
    sfor<2 * NG>([&](auto QI) {
      constexpr int q = decltype(QI)::value, it = s * 2 * NG + q, ks = it / NG, gp = it % NG, kq = ks & 1;
      sfor<4>([&](auto TI) {
        constexpr int tk = decltype(TI)::value, ks1 = (it + 1) / NG, gp1 = (it + 1) % NG;
        acc[0][gp * 4 + tk] = mfma16(wq[(QB + s) % K3_Q][kq * 2], af[it & 1][tk], ks == 0 ? (f32x4){} : acc[0][gp * 4 + tk]);
        if constexpr (it + 1 < 18 * NG) af[(it + 1) & 1][tk] = *(const LAS u32x4*)(size_t)(lb[ks1 / 6] + ((ks1 % 6) * 8 + gp1 * 4 + tk) * 1024);
        sb0();
        acc[1][gp * 4 + tk] = mfma16(wq[(QB + s) % K3_Q][kq * 2 + 1], af[it & 1][tk], ks == 0 ? (f32x4){} : acc[1][gp * 4 + tk]);
        sb0();
      });
    });
  });
}

#define K4_LA 3
#define K4_LW 3
constexpr int K4_R = 8, K4_D = 4;
constexpr int K4_LDS = K4_R * 16384;
constexpr int K4_NST = 21;
struct K4Sched {
  int aslot[18], sslot[K4_NST], posA[18], posS[K4_NST], cntBar[K4_NST], cntSlot[162], posSt;
  constexpr K4Sched(int late) : aslot{}, sslot{}, posA{}, posS{}, cntBar{}, cntSlot{}, posSt{} {
    for (int a = 0; a < 18; ++a) aslot[a] = a < K4_LA ? -1 : 9 * (a - K4_LA) + 4;
    for (int s = 0; s < K4_NST; ++s) sslot[s] = s == 0 ? -1 : (s < K4_LW ? 0 : s - K4_LW);
    int c = 0;
    for (int s = 0; s < K4_LA; ++s) { c += 2; posA[s] = c; }
    if (!late) { c += 4; posSt = c; }
    for (int n = -K4_D; n < 162; ++n) {
      if ((n + K4_D) % 8 == 0 && (n + K4_D) < 162) {
        int i = (n + K4_D) / 8; cntBar[i] = c;
        for (int s = 0; s < K4_NST; ++s) if (sslot[s] == i) { c += 2; posS[s] = c; }
        if (late && i == 0) { c += 4; posSt = c; }
      }
      if (n >= 0) {
        for (int q = 0; q < 18; ++q) if (aslot[q] == n) { c += 2; posA[q] = c; }
        cntSlot[n] = c;
      }
    }
  }
};
template <int LATE> constexpr K4Sched k4sch{LATE};
#define K4_LATE 1
#define K4_SIGB 3
#define K4_PISS 6
#define K4_PCHK 8
static_assert(8 * K4_PCHK - K4_D <= k4sch<0>.aslot[10], "late activations must be requested behind the check");
template <int LATE, class EPI>
DI void k4_body(const char* __restrict__ ws_in, char* __restrict__ ws_out, u16* __restrict__ out, char* lds, int w, float rs, EPI&& last_epilogue) {
  const int tid = threadIdx.x, lane = tid & 63;
  const int b = blockIdx.x, xcd = b & 7, idx = b >> 3;
  const int ksl = xcd >> 1, mt = (xcd & 1) * 8 + (idx & 7), ng = idx >> 3;
  const int tg = w & 3, kh = w >> 2;
  const int tt = mt * 4 + tg;
  const unsigned ldsb = (unsigned)(size_t)(LAS char*)lds;
  const char* wsrc = ws_in + WS_WD + (size_t)((ng * 4 + ksl) * 2 + kh) * WD_STREAM * 1024 + tg * 2048;
  const char* asrc = ws_in + WS_ACT + ((size_t)tt * 288 + ksl * 72 + kh * 36) * 1024;
  const unsigned voff = lane * 16;
  const int g = lane >> 4;
  const unsigned avoff0 = (g >> 1) * 1024 + (g & 1) * 512 + (lane & 15) * 16 + kh * 256, avoff1 = avoff0 ^ 256;
  const int th = w & 1, a = w >> 1, ott = mt * 4 + ksl, tok = th * 16 + (lane & 15);
  const int col = 32 * a + 8 * g, col8 = 128 + 4 * g;
  u32x2 h0, h1, h2; unsigned mub;
  u32x4 af[K4_LA + 1][2];
  auto issue_stage = [&](auto SI) {
    constexpr int s = decltype(SI)::value;
    glds16x2(wsrc + (size_t)s * 8192, voff, ldsb + (s % K4_R) * 16384 + kh * 8192 + tg * 2048);
  };
  auto issue_a = [&](auto AI) {
    constexpr int a = decltype(AI)::value;
    constexpr int ka = k4_act(a);
    gload16_sc1<(ka % 2) * 2048>(af[a % (K4_LA + 1)][0], asrc + (ka / 2) * 4096, avoff0);
    gload16_sc1<(ka % 2) * 2048>(af[a % (K4_LA + 1)][1], asrc + (ka / 2) * 4096, avoff1);
  };
  sfor<K4_LA>([&](auto AI) { issue_a(AI); });
  if constexpr (!LATE) last_epilogue();
  f32x4 acc[9][2];
  u32x4 wf[K4_D + 1];
  unsigned* cntb = (unsigned*)(ws_out + WS_CNT);
  const unsigned* c2p = cntb + (384 + mt * 8 + ksl * 2 + kh) * 32;
  unsigned pv2 = 0;
  unsigned lrd0 = ldsb + kh * 8192 + lane * 16, lrd1 = lrd0 + 65536;
  unsigned xw = ldsb + w * 1024 + lane * 16, xw5 = xw + 5 * 16384;
  asm volatile("" : "+v"(lrd0), "+v"(lrd1), "+v"(xw), "+v"(xw5));
  auto xad = [&](unsigned lo, unsigned hi, int t) -> unsigned { return t < 6 ? hi + t * 8192 : lo + (t - 6) * 8192; };
  auto rd = [&](auto NI) {
    constexpr int n = decltype(NI)::value;
    if constexpr (n < 162) {
      constexpr int st = n / 8;
      if constexpr (n % 8 == 0) {
        if constexpr (st > 0) wait_vm<k4sch<LATE>.cntBar[st] - k4sch<LATE>.posS[st]>();
        if constexpr (st == (LATE ? K4_SIGB : 1)) {
          wait_vm<k4sch<LATE>.cntBar[st] - k4sch<LATE>.posSt>();
          sig_add(cntb + (384 + mt * 8 + ksl * 2 + (ng >> 1)) * 32);
        }
        if constexpr (st == K4_PCHK) {
          wait_vm<k4sch<LATE>.cntBar[K4_PCHK] - k4sch<LATE>.cntBar[K4_PISS]>();
          spin_ge(pv2, c2p, 16u);
        }
        bar();
        if constexpr (st == K4_PISS) pv2 = load4_sc1(c2p);
        if constexpr (st == 12) {
          const char* hfp = ws_in + WS_HF + ((size_t)ott * 36 + ng * 9) * 1024;
          auto ho = [&](int c) -> unsigned { return (c >> 4) * 1024 + (((c >> 2) & 1) * 32 + tok) * 16 + ((c >> 3) & 1) * 8; };
          gload8<0>(h0, hfp, ho(col)); gload8<512>(h1, hfp, ho(col)); gload8<0>(h2, hfp, ho(col8));
          gload4<0>(mub, ws_in + WS_MEAN + (size_t)ott * 128, tok * 4);
        }
        sfor<K4_NST>([&](auto SI) { if constexpr (k4sch<LATE>.sslot[decltype(SI)::value] == st) issue_stage(SI); });
        if constexpr (LATE && st == 0) last_epilogue();
      }
      constexpr int off = (st % K4_R) * 16384 + (n % 8) * 1024;
      if constexpr (st == 0) wf[n % (K4_D + 1)] = *(const LAS u32x4*)(size_t)(lrd1 + 81920 + n * 1024);
      else wf[n % (K4_D + 1)] = *(const LAS u32x4*)(size_t)((off < 65536 ? lrd0 : lrd1) + (off & 65535));
    }
  };
  sfor<K4_D>([&](auto I) { rd(I); });
  sfor<162>([&](auto NI) {
    constexpr int n = decltype(NI)::value;
    constexpr int ks = n / 9, t = n % 9, ab = ks % (K4_LA + 1);
    rd(std::integral_constant<int, n + K4_D>{});
    sfor<18>([&](auto AI) { if constexpr (k4sch<LATE>.aslot[decltype(AI)::value] == n) issue_a(AI); });
    if constexpr (t == 0) {
      wait_vm<k4sch<LATE>.cntSlot[n] - k4sch<LATE>.posA[ks]>();
      pin(af[ab][0]); pin(af[ab][1]);
    }
    acc[t][0] = mfma16(wf[n % (K4_D + 1)], af[ab][0], ks == 0 ? (f32x4){} : acc[t][0]);
    acc[t][1] = mfma16(wf[n % (K4_D + 1)], af[ab][1], ks == 0 ? (f32x4){} : acc[t][1]);
    if constexpr (n >= 155) *(LAS f32x4*)(size_t)xad(xw, xw5, n - 155) = acc[n - 155][1];
    sb0();
  });
  *(LAS f32x4*)(size_t)xad(xw, xw5, 7) = acc[7][1]; *(LAS f32x4*)(size_t)xad(xw, xw5, 8) = acc[8][1];
  wait_lgkm0(); bar();
  const unsigned xr = ldsb + (w ^ 4) * 1024 + lane * 16, xr5 = xr + 5 * 16384;
  f32x4 v[9];
#pragma unroll
  for (int t = 0; t < 9; ++t) v[t] = *(const LAS f32x4*)(size_t)xad(xr, xr5, t) + acc[t][0];
  if (tg == ksl) {
#pragma unroll
    for (int t = 0; t < 9; ++t) *(LAS f32x4*)(size_t)xad(xr, xr5, t) = v[t];
  } else {
    char* pp = ws_out + WS_PART + ((((size_t)ksl * 64 + tt) * 4 + ng) * 2 + kh) * 5120;
#pragma unroll
    for (int i = 0; i < 4; ++i)
      store16<2>(pp + i * 1024 + lane * 16, (u32x4){pk2(v[2 * i][0], v[2 * i][1]), pk2(v[2 * i][2], v[2 * i][3]), pk2(v[2 * i + 1][0], v[2 * i + 1][1]), pk2(v[2 * i + 1][2], v[2 * i + 1][3])});
    store8_sc1(pp + 4096 + lane * 8, (u32x2){pk2(v[8][0], v[8][1]), pk2(v[8][2], v[8][3])});
    wait_vm<0>();
    if (lane == 0) __hip_atomic_fetch_add((unsigned*)(ws_out + WS_CNT) + ((mt * 4 + ng) * 4 + tg) * 32, 1u, __ATOMIC_RELAXED, __HIP_MEMORY_SCOPE_AGENT);
  }
  if (w == ksl) {
    const unsigned* cnt = (const unsigned*)(ws_out + WS_CNT) + ((mt * 4 + ng) * 4 + ksl) * 32;
    unsigned spins = 0;
    while (__hip_atomic_load(cnt, __ATOMIC_RELAXED, __HIP_MEMORY_SCOPE_AGENT) < 6u) {
      __builtin_amdgcn_s_sleep(1);
      if (++spins > (1u << 22)) break;
    }
  }
  wait_lgkm0();
  bar();
  const unsigned ox = ldsb + ((th ^ 1) * 4 + ksl) * 1024 + lane * 16, oa = ox + (a < 3 ? 5 * 16384 + a * 16384 : 0);
  f32x4 s0 = *(const LAS f32x4*)(size_t)oa, s1 = *(const LAS f32x4*)(size_t)(oa + 8192), s2 = *(const LAS f32x4*)(size_t)(ox + 16384);
  const char* pb = ws_in + WS_PART + (((size_t)ott * 4 + ng) * 2 + th) * 5120;
  constexpr size_t KS = (size_t)64 * 4 * 2 * 5120;
  u32x4 pv[3]; unsigned pw[3][2];
#pragma unroll
  for (int k = 0; k < 3; ++k) {
    const char* q = pb + (size_t)((ksl + 1 + k) & 3) * KS;
    pv[k] = load16_sc1(q + a * 1024 + lane * 16);
    const char* q8 = q + (a == 0 ? 4096 + lane * 8 : a * 1024);
    pw[k][0] = load4_sc1(q8); pw[k][1] = load4_sc1(q8 + 4);
  }
  wait_vm<0>();
#pragma unroll
  for (int k = 0; k < 3; ++k) { pin(pv[k]); pin(pw[k][0]); pin(pw[k][1]); }
#pragma unroll
  for (int k = 0; k < 3; ++k) {
    const unsigned a0 = pv[k][0], a1 = pv[k][1], a2 = pv[k][2], a3 = pv[k][3], b0 = pw[k][0], b1 = pw[k][1];
    s0 += (f32x4){bflo(a0), bfhi(a0), bflo(a1), bfhi(a1)}; s1 += (f32x4){bflo(a2), bfhi(a2), bflo(a3), bfhi(a3)}; s2 += (f32x4){bflo(b0), bfhi(b0), bflo(b1), bfhi(b1)};
  }
  pin(h0); pin(h1); pin(h2); pin(mub);
  const float r2 = rs * rs, mu = __uint_as_float(mub);
  u16* orow = out + (size_t)(ott * 32 + tok) * D_ + ng * 144;
  s0 = s0 * r2 + mu; s1 = s1 * r2 + mu; s2 = s2 * r2 + mu;
  u32x4 o;
  o[0] = pk2(s0[0] + bflo(h0[0]), s0[1] + bfhi(h0[0])); o[1] = pk2(s0[2] + bflo(h0[1]), s0[3] + bfhi(h0[1]));
  o[2] = pk2(s1[0] + bflo(h1[0]), s1[1] + bfhi(h1[0])); o[3] = pk2(s1[2] + bflo(h1[1]), s1[3] + bfhi(h1[1]));
  *(u32x4*)(orow + col) = o;
  if (a == 0) *(u32x2*)(orow + col8) = (u32x2){pk2(s2[0] + bflo(h2[0]), s2[1] + bfhi(h2[0])), pk2(s2[2] + bflo(h2[1]), s2[3] + bfhi(h2[1]))};
}
template <int HALF>
DI void k3_body(const char* __restrict__ ws_in, char* __restrict__ ws_out, u16* __restrict__ out, char* lds, int w) {
  const int tid = threadIdx.x, lane = tid & 63;
  const int b = blockIdx.x, xcd = b & 7, idx = b >> 3;
  const int rt = (xcd & 1) * 8 + (idx & 7), fgp = (xcd >> 1) * 4 + (idx >> 3);
  const unsigned ldsb = (unsigned)(size_t)(LAS char*)lds;
  const unsigned voff = lane * 16;
  const int g = lane >> 4, l15 = lane & 15;
  const unsigned avoff = (g >> 1) * 1024 + (g & 1) * 512 + l15 * 16;
  const char* hsrc = ws_in + WS_HF + (size_t)(rt * 4 + (w >> 1)) * 36 * 1024 + (w & 1) * 256;
#define K3_ROT 0
  const int rot = K3_ROT;
  auto hload = [&](auto I) { constexpr int i = decltype(I)::value; const int ia = i + 2 * rot >= 18 ? i + 2 * rot - 18 : i + 2 * rot;
    glds16(hsrc + ia * 2048, avoff, ldsb + (i * 8 + w) * 1024); };
  auto nohl = [&](auto) {};
  const int t0 = w, t1 = w + 8, t2 = 16 + ((w >> 1) & 1);
  const char* wgu = ws_in + WS_WGU + (size_t)fgp * 18 * 36 * 1024;
  const char* sA = wgu + (size_t)t0 * 36 * 1024;
  const char* sB = wgu + (size_t)t1 * 36 * 1024;
  const char* sC = wgu + (size_t)t2 * 36 * 1024;
  u32x4 wq[K3_Q][4];
  u32x4 stv[2];
  {
    const char* sp = ws_in + WS_HST + (size_t)(rt * 128 + w * 16) * 128;
    gload16<0>(stv[0], sp, l15 * 128 + g * 32); gload16<16>(stv[1], sp, l15 * 128 + g * 32);
  }
  sfor<4>([&](auto I) { hload(I); });
  k3_wload(wq[0], sA, 0, rot, voff);
  hload(std::integral_constant<int, 4>{}); hload(std::integral_constant<int, 5>{});
  k3_wload(wq[1], sA, 1, rot, voff);
  wait_vm<10>();
  float* ctab = (float*)(lds + 144 * 1024);
  {
    pin(stv[0]); pin(stv[1]);
    float sm = (__uint_as_float(stv[0][0]) + __uint_as_float(stv[0][2])) + (__uint_as_float(stv[1][0]) + __uint_as_float(stv[1][2]));
    float sq = (__uint_as_float(stv[0][1]) + __uint_as_float(stv[0][3])) + (__uint_as_float(stv[1][1]) + __uint_as_float(stv[1][3]));
    sm += __shfl_xor(sm, 16); sq += __shfl_xor(sq, 16);
    sm += __shfl_xor(sm, 32); sq += __shfl_xor(sq, 32);
    const float mean = sm * (1.f / D_);
    const float rstd = ln_rstd_soft(sq * (1.f / D_) - mean * mean);
    if (lane < 16) {
      ctab[w * 16 + lane] = -1.4426950408889634f * rstd;
    }
  }
  wait_lgkm0();
  bar();
  float c1[8];
#pragma unroll
  for (int tk = 0; tk < 8; ++tk) c1[tk] = ctab[tk * 16 + l15];
  const float rs_out = ctab[(xcd >> 1) * 32 + (w & 1) * 16 + l15] * 0.6931471805599453f;
  unsigned lb[3] = {ldsb + voff, ldsb + voff + 49152, ldsb + voff + 98304};
  unsigned lh[3] = {lb[0] + (w & 1) * 4096, lb[1] + (w & 1) * 4096, lb[2] + (w & 1) * 4096};
  asm volatile("" : "+v"(lb[0]), "+v"(lb[1]), "+v"(lb[2]), "+v"(lh[0]), "+v"(lh[1]), "+v"(lh[2]));
  f32x4 acc[2][8];
  auto swiglu = [&](const f32x4& a, const f32x4& u, float c) -> u32x2 {
    float r[4];
#pragma unroll
    for (int j = 0; j < 4; ++j) { float e = __builtin_amdgcn_exp2f(a[j] * c); float pr = a[j] * u[j]; pin(pr); r[j] = pr * __builtin_amdgcn_rcpf(1.f + e); pin(r[j]); }
    return (u32x2){pk2(r[0], r[1]), pk2(r[2], r[3])};
  };
  char* actp = ws_out + WS_ACT + ((size_t)rt * 4 * 288 + fgp * 18) * 1024 + ((g & 1) * 32 + (g >> 1) * 16 + l15) * 16;
  auto epi2 = [&](int T, int tile32, const f32x4& g0, const f32x4& u0, float ca, const f32x4& g1, const f32x4& u1, float cb) {
    const u32x2 x0 = swiglu(g0, u0, ca), x1 = swiglu(g1, u1, cb);
    auto r0 = __builtin_amdgcn_permlane32_swap(x0[0], x1[0], false, false);
    auto r1 = __builtin_amdgcn_permlane32_swap(x0[1], x1[1], false, false);
    store16<2>(actp + ((size_t)tile32 * 288 + T) * 1024, (u32x4){r0[0], r1[0], r0[1], r1[1]});
  };
  auto epi_full = [&](int T) {
#pragma unroll
    for (int i = 0; i < 4; ++i) epi2(T, i, acc[0][2 * i], acc[1][2 * i], c1[2 * i], acc[0][2 * i + 1], acc[1][2 * i + 1], c1[2 * i + 1]);
  };
  k3_unit<8, true, true, 0, 0>(acc, wq, sA, HALF == 0 ? sC : sB, lb, rot, voff, hload);
  epi_full(t0);
  if constexpr (HALF == 0) {
    k3_unit<4, false, true, 9 % K3_Q, 4>(acc, wq, sC, sB, lh, rot, voff, nohl);
    const bool hi = w & 1;
#pragma unroll
    for (int i = 0; i < 2; ++i) {
      float a0 = c1[2 * i], a1 = c1[4 + 2 * i], b0 = c1[2 * i + 1], b1 = c1[5 + 2 * i]; pin(a0); pin(a1); pin(b0); pin(b1);
      const float ca = hi ? a1 : a0, cb = hi ? b1 : b0;
      epi2(t2, (w & 1) * 2 + i, acc[0][2 * i], acc[1][2 * i], ca, acc[0][2 * i + 1], acc[1][2 * i + 1], cb);
    }
  }
  unsigned* cnt = (unsigned*)(ws_out + WS_CNT);
  const unsigned* c1p = cnt + (256 + rt * 8 + (fgp >> 2) * 2 + HALF) * 32;
  unsigned pv = 0;
  auto hook = [&](auto I) {
    if constexpr (decltype(I)::value == 0) sig_add(cnt + (256 + rt * 8 + (fgp >> 1)) * 32);
    else {
      glds16x2(ws_in + WS_WD + (size_t)(((idx >> 3) * 4 + (xcd >> 1)) * 2 + HALF) * WD_STREAM * 1024 + (w & 3) * 2048, voff, ldsb + 144 * 1024 + HALF * 8192 + (w & 3) * 2048);
      pv = load4_sc1(c1p);
    }
  };
  k3_unit<8, false, false, (HALF == 0 ? 18 : 9) % K3_Q, HALF == 0 ? 2 : 4>(acc, wq, sB, sB, lb, rot, voff, hook);
  wait_vm<0>();
  spin_ge(pv, c1p, 16u);
  k4_body<(HALF == 1 && K4_LATE)>(ws_in, ws_out, out, lds, w, rs_out, [&]() { epi_full(t1); });
}

__global__ __launch_bounds__(512, 2) void k34_ffn(const char* __restrict__ ws_in, char* __restrict__ ws_out, u16* __restrict__ out) {
  asm volatile(".Lfk_text_begin:");
  extern __shared__ __attribute__((aligned(16))) char lds[];
  const int w = uni(threadIdx.x >> 6);
  if (w < 4) k3_body<0>(ws_in, ws_out, out, lds, w); else k3_body<1>(ws_in, ws_out, out, lds, w);
}

#define OUT_ST 2
#define OUT2_ST 0
#define GU_ST 2
#define PACK_ST 2
constexpr size_t WS_PQ   = (WS_END_FFN + 4095) / 4096 * 4096;
constexpr size_t WS_KVP  = WS_PQ + SZ_HF;
constexpr size_t SZ_KVP  = (size_t)16 * 8 * 16 * 1024;
constexpr size_t WS_KVF  = WS_KVP + SZ_KVP;
constexpr size_t SZ_KVF  = 64 * 1024;
constexpr size_t WS_WOP  = WS_KVF + SZ_KVF;
constexpr size_t SZ_WOP  = (size_t)4 * 4 * 9 * 5 * 1024;
constexpr size_t WS_MX   = WS_WOP + SZ_WOP;
constexpr size_t WS_END  = WS_MX + S_ * 4;


DI void pack_wo_task(const u16* wo, char* ws, int id) {
  const int lane = id & 63, fi = id >> 6, i = fi % 5, u = (fi / 5) % 9, w = (fi / 45) & 3, ng = fi / 180;
  const int row = lane & 31, h = lane >> 5;
  if (i == 4 && row >= 16) {
    if (row >= 18) store16<OUT_ST>(ws + WS_WOP + (size_t)id * 16, (u32x4){0u, 0u, 0u, 0u});
    return;
  }
  const int n = ng * 144 + i * 32 + row;
  unsigned v[8];
#pragma unroll
  for (int j = 0; j < 8; ++j) {
    const int e = u < 8 ? 18 * (8 * w + u) + 8 * (j >> 2) + 4 * h + (j & 3) : 18 * (8 * w + 4 * h + (j >> 1)) + 16 + (j & 1);
    v[j] = wo[e * D_ + n];
  }
  store16<OUT_ST>(ws + WS_WOP + (size_t)id * 16, (u32x4){v[0] | (v[1] << 16), v[2] | (v[3] << 16), v[4] | (v[5] << 16), v[6] | (v[7] << 16)});
}
DI void pack_worow_task(const u16* wo, char* ws, int e, int lane) {
  float s = 0.f;
#pragma unroll
  for (int i = 0; i < 9; ++i) s += bf2f(wo[e * D_ + i * 64 + lane]);
#pragma unroll
  for (int o = 32; o; o >>= 1) s += __shfl_xor(s, o);
  const unsigned hi = pk2(s, 0.f) & 0xffffu; const unsigned lo = pk2(s - bflo(hi), 0.f) & 0xffffu;
  const int hh = e / 18, el = e % 18, w = hh >> 3, ul = hh & 7;
  int u, h, j;
  if (el < 16) { u = ul; h = (el >> 2) & 1; j = (el >> 3) * 4 + (el & 3); } else { u = 8; h = ul >> 2; j = (ul & 3) * 2 + (el - 16); }
  if (lane < 8) {
    const int ng = lane >> 1, part = lane & 1;
    u16* p = (u16*)(ws + WS_WOP) + ((((size_t)(ng * 4 + w) * 9 + u) * 5 + 4) * 64 + h * 32 + 16 + part) * 8 + j;
    unsigned val = part ? lo : hi;
    asm volatile("global_store_short %0, %1, off sc1" :: "v"(p), "v"(val) : "memory");
  }
}

constexpr int K1_WST = 18432, K1_STG = K1_WST + 16384, K1_R = 4;
constexpr int K1_TAB = K1_STG * K1_R;
constexpr int K1_LDS = 8 * 18432;
static_assert(K1_TAB + 4 * 144 * 4 + 1024 <= K1_LDS, "lds");
#define K1_NGU 64
#define K1_DD 3
constexpr int K1_D = K1_DD;
DI float phi3(float u, float rs, float rl) { return fmaxf(fmaf(u, rs, 1e-6f), 1e-6f) + __builtin_amdgcn_exp2f(fminf(u * rl, 0.f)); }
template <bool ISQ, int NH> constexpr int k1_tile(int j) {
  if (ISQ) return NH ? 5 + j : j;
  if (NH == 0) return j < 2 ? j : (j == 2 ? 4 : j + 2);
  return j < 2 ? 2 + j : 5 + j;
}
template <bool ISQ> constexpr int k1_toff(int t) { return ISQ ? 32 * t : (t < 4 ? 32 * t : 144 + 32 * (t - 5)); }

__host__ __device__ constexpr int k1_img(int t) { return t < 8 ? t * 4096 : 3 * K1_STG + (t - 8) * 4096; }
template <bool ISQ, int NH, class ISSUE, class TAIL>
DI void k1_body(char* lds, unsigned ldsb, int lane, int tg, int rt, int grp, char* ws, ISSUE&& issue, TAIL&& tail) {
  constexpr int NT = NH ? 4 : 5, NWF = NT + K1_D;
  const int g = lane >> 4, l15 = lane & 15, q4 = (lane >> 2) & 3, p4 = lane & 3;
  const unsigned rrow = ldsb + (16 * (g >> 1) + 4 * (g & 1) + q4) * 288;
  unsigned rbA[2], rbB[2], rbX[2][2];
  rbA[0] = rrow + p4 * 8;
  rbB[0] = rrow + (p4 < 2 ? 128 + 8 * p4 : 272 + 8 * (p4 - 2));
  rbA[1] = rbA[0] + 2 * K1_STG; rbB[1] = rbB[0] + 2 * K1_STG;
  const unsigned xb = ldsb + K1_WST + tg * 4096 + l15 * 128;
#pragma unroll
  for (int kh = 0; kh < 2; ++kh) { rbX[kh][0] = xb + (((4 * kh + g) ^ ((l15 >> 1) & 7)) * 16); rbX[kh][1] = rbX[kh][0] + 2 * K1_STG; }
  asm volatile("" : "+v"(rbA[0]), "+v"(rbA[1]), "+v"(rbB[0]), "+v"(rbB[1]), "+v"(rbX[0][0]), "+v"(rbX[0][1]), "+v"(rbX[1][0]), "+v"(rbX[1][1]));
  u32x4 ones = {0x3f803f80u, 0x3f803f80u, 0x3f803f80u, 0x3f803f80u};
  pin(ones);
  f32x4 acc[NT][2], accs[NT], sst[2][2];
#pragma unroll
  for (int t = 0; t < NT; ++t) accs[t] = (f32x4){};
  u32x4 xf[2][2], wf[NWF];
  auto rd = [&](auto NI) {
    constexpr int n = decltype(NI)::value;
    if constexpr (n < 18 * NT) {
      constexpr int ks = n / NT, j = n % NT, st = ks >> 1, kh = ks & 1, slot = st % K1_R, hb = slot >> 1, so = (slot & 1) * K1_STG;
      constexpr int t = k1_tile<ISQ, NH>(j);
      constexpr int pos = n % (2 * NT);
      constexpr int nxt = st == 0 ? 3 : (st >= 2 && st + 2 < 9 ? st + 2 : -1);
      if constexpr (pos == 0) {
        constexpr int upto = st == 0 ? 2 : (st + 1 > 3 ? (st + 1 < 8 ? st + 1 : 8) : 3);
        if constexpr (NH == 1 && st == 8) tail(std::integral_constant<int, 0>{});
        else wait_vm<4 * (upto - st)>();
        bar();
        if constexpr (NH == 1 && st >= 7) tail(std::integral_constant<int, st>{});
      }
      if constexpr (j == 0) {
        xf[kh][0] = *(const LAS u32x4*)(size_t)(rbX[kh][hb] + so);
        xf[kh][1] = *(const LAS u32x4*)(size_t)(rbX[kh][hb] + so + 2048);
      }
      const unsigned a = ((!ISQ && t == 4) ? rbB[hb] : rbA[hb] + k1_toff<ISQ>(t)) + so + kh * 9216;
      u32x2 lo = __builtin_bit_cast(u32x2, tr_read(a)), hi = __builtin_bit_cast(u32x2, tr_read(a + 8 * 288));
      wf[n % NWF] = (u32x4){lo[0], lo[1], hi[0], hi[1]};
      if constexpr (nxt >= 0 && (pos & 1) == 1 && pos / 2 < 4) issue(std::integral_constant<int, nxt>{}, std::integral_constant<int, pos / 2>{});
    }
  };
  sfor<K1_D>([&](auto I) { rd(I); });
  sfor<18 * NT>([&](auto NI) {
    constexpr int n = decltype(NI)::value, ks = n / NT, j = n % NT, kh = ks & 1;
    rd(std::integral_constant<int, n + K1_D>{});
    const u32x4 wv = wf[n % NWF];
    sfor<2>([&](auto HI) {
      constexpr int th = decltype(HI)::value;
      const f32x4 c = ks == 0 ? (f32x4){} : acc[j][th];
      acc[j][th] = ISQ ? mfma16(wv, xf[kh][th], c) : mfma16(xf[kh][th], wv, c);
    });
    if constexpr (NH == 1 && j < 2) {
      sst[j][0] = mfma16(ones, xf[kh][j], ks == 0 ? (f32x4){} : sst[j][0]);
      sst[j][1] = mfma16(xf[kh][j], xf[kh][j], ks == 0 ? (f32x4){} : sst[j][1]);
    }
    if constexpr (j == NT - 1) {
      if (tg == (ks & 3)) {
        sfor<NT>([&](auto JI) { constexpr int jj = decltype(JI)::value; accs[jj] = mfma16(ones, wf[(ks * NT + jj) % NWF], accs[jj]); });
      }
    }
    sb0();
  });
  float* tab = (float*)(lds + K1_TAB);
  float* stt = tab + 4 * 144;
  if (g == 0) {
    sfor<NT>([&](auto JI) { constexpr int jj = decltype(JI)::value; tab[tg * 144 + k1_tile<ISQ, NH>(jj) * 16 + l15] = accs[jj][0]; });
  }
  if constexpr (NH == 1) {
    if (g == (l15 >> 2)) {
#pragma unroll
      for (int th = 0; th < 2; ++th) {
        const int r = l15 & 3;
        const float q = r == 0 ? sst[th][1][0] : (r == 1 ? sst[th][1][1] : (r == 2 ? sst[th][1][2] : sst[th][1][3]));
        const float mean = sst[th][0][0] * (1.f / D_);
        stt[tg * 32 + th * 16 + l15] = rbf(mean);
        if (ISQ && grp == 0) ((float*)(ws + WS_MX))[rt * 128 + tg * 32 + th * 16 + l15] = mean;
        stt[128 + tg * 32 + th * 16 + l15] = ln_rstd(q * (1.f / D_) - mean * mean);
      }
    }
  }
  wait_lgkm0(); bar();
  constexpr float L2E = 1.4426950408889634f;
  if constexpr (ISQ) {
    const int tt = rt * 4 + tg;
    float nm[2], rs[2], rl[2];
#pragma unroll
    for (int th = 0; th < 2; ++th) { nm[th] = -stt[tg * 32 + th * 16 + l15]; rs[th] = stt[128 + tg * 32 + th * 16 + l15]; rl[th] = rs[th] * L2E; }
    char* pq = ws + WS_PQ + ((size_t)tt * 36 + grp * 9) * 1024 + ((g & 1) * 32 + (g >> 1) * 16 + l15) * 16;
    sfor<NT>([&](auto JI) {
      constexpr int j = decltype(JI)::value, t = k1_tile<ISQ, NH>(j);
      const float* cp = tab + t * 16 + g * 4;
      const f32x4 cs = (*(const f32x4*)cp + *(const f32x4*)(cp + 144)) + (*(const f32x4*)(cp + 288) + *(const f32x4*)(cp + 432));
      unsigned pk[2][2];
#pragma unroll
      for (int th = 0; th < 2; ++th) {
        float v[4];
#pragma unroll
        for (int e = 0; e < 4; ++e) v[e] = phi3(fmaf(nm[th], cs[e], acc[j][th][e]), rs[th], rl[th]);
        pk[th][0] = pk2(v[0], v[1]); pk[th][1] = pk2(v[2], v[3]);
      }
      auto r0 = __builtin_amdgcn_permlane32_swap(pk[0][0], pk[1][0], false, false);
      auto r1 = __builtin_amdgcn_permlane32_swap(pk[0][1], pk[1][1], false, false);
      store16<OUT_ST>(pq + t * 1024, (u32x4){r0[0], r1[0], r0[1], r1[1]});
    });
  } else {
    f32x4 mn[2], rs[2], rl[2];
#pragma unroll
    for (int th = 0; th < 2; ++th) { mn[th] = *(const f32x4*)(stt + tg * 32 + th * 16 + g * 4); rs[th] = *(const f32x4*)(stt + 128 + tg * 32 + th * 16 + g * 4); rl[th] = rs[th] * L2E; }
    char* ob = lds + (tg * 64 + lane) * 16;
    sfor<NT>([&](auto JI) {
      constexpr int j = decltype(JI)::value, t = k1_tile<ISQ, NH>(j);
      const float* cp = tab + t * 16 + l15;
      const float ncs = -((cp[0] + cp[144]) + (cp[288] + cp[432]));
      u32x4 pv;
#pragma unroll
      for (int th = 0; th < 2; ++th) {
        float v[4];
#pragma unroll
        for (int e = 0; e < 4; ++e) {
          const float u = fmaf(mn[th][e], ncs, acc[j][th][e]);
          if (t < 4) v[e] = phi3(u, rs[th][e], rl[th][e]);
          else if (t > 4) v[e] = u * rs[th][e];
          else v[e] = l15 < 8 ? phi3(u, rs[th][e], rl[th][e]) : u * rs[th][e];
        }
        pv[2 * th] = pk2(v[0], v[1]); pv[2 * th + 1] = pk2(v[2], v[3]);
      }
      *(u32x4*)(ob + k1_img(t)) = pv;
      if constexpr (t == 4) *(u32x4*)(ob + k1_img(9)) = l15 < 8 ? ones : pv;
    });
    wait_lgkm0(); bar();
    const int w = NH * 4 + tg;
#pragma unroll
    for (int pi = 0; pi < 2; ++pi) {
      const int p = 2 * w + pi;
      int a, bt;
      if (p < 10) { a = (p + 1) / 3; bt = 5 + a + ((p + 1) % 3) - 1; } else if (p == 10) { a = 4; bt = 8; } else { a = p - 11; bt = 9; }
      f32x4 c = {};
#pragma unroll
      for (int i = 0; i < 4; ++i) {
        const u32x4 av = *(const u32x4*)(lds + a * 4096 + (i * 64 + lane) * 16);
        const u32x4 bv = *(const u32x4*)(lds + k1_img(bt) + (i * 64 + lane) * 16);
        c = mfma16(av, bv, c);
      }
      if constexpr (OUT_ST != 0) asm volatile("s_nop 9" : "+v"(c));
      store16<OUT_ST>(ws + WS_KVP + (((size_t)rt * 8 + grp) * 16 + p) * 1024 + lane * 16, __builtin_bit_cast(u32x4, c));
    }
  }
}

__global__ __launch_bounds__(512, 2) void k1_qkv(const u16* __restrict__ x, const u16* __restrict__ wqkv, const u16* __restrict__ wo,
                                                  const u16* __restrict__ wg, const u16* __restrict__ wu, const u16* __restrict__ wd, char* __restrict__ ws) {
  extern __shared__ __attribute__((aligned(16))) char lds[];
  const int tid = threadIdx.x, lane = tid & 63, w = uni(tid >> 6);
  const int b = blockIdx.x, xcd = b & 7, idx = b >> 3;
  const unsigned ldsb = (unsigned)(size_t)(LAS char*)lds;
  if (idx >= 24) {
    {
      const char* c0; unsigned len; text_range(c0, len);
      const unsigned o = (unsigned)((((idx - 24) * 8 + w) * 64 + lane) * 128);
      if ((unsigned)(((idx - 24) * 8 + w) * 8192) < len) l2touch(c0, o < len ? o : 0u, ldsb);
    }
    const int pw = ((idx - 24) * 8 + xcd) * 8 + w;
    if (pw < 8) ((unsigned*)(ws + WS_CNT))[(pw * 64 + lane) * 32] = 0u;
    const int u = (idx - 24) * 8 + w;
    pack_gu_load(wg, wu, xcd * 9 + u % 9, u / 9, ldsb + w * 16384 + 256, lane);
    for (int id = pw * 64 + lane; id < 4 * 4 * 9 * 5 * 64; id += 512 * 64) pack_wo_task(wo, ws, id);
    for (int e = pw; e < D_; e += 512) pack_worow_task(wo, ws, e, lane);
    wait_vm<0>();
    pack_gu_store<GU_ST, false>(ws, xcd * 9 + u % 9, u / 9, ldsb + w * 16384 + 256, lane);
    return;
  }
  const int rt = (xcd & 3) * 4 + (idx & 3), cgl = idx >> 2;
  const int ch = xcd >> 2;
  const bool isq = cgl < 2;
  const int grp = isq ? ch * 2 + cgl : ch * 4 + (cgl - 2);
  auto wsrc_off = [&](int n) -> unsigned {
    const int c = n * 64 + lane, rho = c / 18, cc = c % 18;
    const int k = (rho & ~12) | ((rho & 4) << 1) | ((rho & 8) >> 1);
    const int col = isq ? 144 * grp + 8 * cc : (cc < 9 ? 576 + 72 * grp + 8 * cc : 1152 + 72 * grp + 8 * (cc - 9));
    return (unsigned)((k * 1728 + col) * 2);
  };
  const char* xsrc = (const char*)x + (size_t)(rt * 128 + w * 16) * 1152;
  const unsigned xoff0 = (lane >> 3) * 1152 + (((lane & 7) ^ (lane >> 4)) * 16);
  const unsigned xoff1 = ((lane >> 3) + 8) * 1152 + (((lane & 7) ^ (4 + (lane >> 4))) * 16);
#define K1_ROT (((idx & 3) * 2 + cgl) % 9)
  const int rot = K1_ROT;
  glds16(xsrc + rot * 128, xoff0, ldsb + K1_WST + w * 2048);
  glds16(xsrc + rot * 128, xoff1, ldsb + K1_WST + w * 2048 + 1024);
  const unsigned woffA = wsrc_off(w), woffB = wsrc_off(w + 8), woffC = wsrc_off(16 + (w & 1));
  auto issue = [&](auto SI, auto PI) {
    constexpr int st = decltype(SI)::value, slot = st % K1_R, pc = decltype(PI)::value;
    const int sa = st + rot >= 9 ? st + rot - 9 : st + rot;
    const char* wb = (const char*)wqkv + (size_t)sa * 64 * 3456;
    const unsigned sb = ldsb + slot * K1_STG;
    if constexpr (pc == 0) glds16(xsrc + sa * 128, xoff0, sb + K1_WST + w * 2048);
    if constexpr (pc == 1) glds16(xsrc + sa * 128, xoff1, sb + K1_WST + w * 2048 + 1024);
    if constexpr (pc == 2) { if (w < 2) glds16(wb, woffC, sb + (16 + w) * 1024); glds16(wb, woffA, sb + w * 1024); }
    if constexpr (pc == 3) glds16(wb, woffB, sb + (w + 8) * 1024);
  };
  issue(std::integral_constant<int, 0>{}, std::integral_constant<int, 2>{});
  issue(std::integral_constant<int, 0>{}, std::integral_constant<int, 3>{});
  bar();
  sfor<4>([&](auto PI) { issue(std::integral_constant<int, 1>{}, PI); });
  sfor<4>([&](auto PI) { issue(std::integral_constant<int, 2>{}, PI); });
  const int tg = w & 3;
  const int pslot = w == 5 ? idx : (w == 6 ? (idx < 21 ? 24 + idx : 99) : ((w == 7 && isq) ? 45 + idx : 99));
  const unsigned pl = ldsb + (w == 6 ? 2 * K1_STG : K1_STG + (w == 7 ? 18432 : 0));
  auto tail = [&](auto SI) {
    constexpr int st = decltype(SI)::value;
    if constexpr (st == 0) { if (w == 5) wait_vm<18>(); else if (pslot < 99 && w == 7) wait_vm<16>(); else wait_vm<0>(); }
    else if constexpr (st == 9) {
      wait_vm<0>();
      if (pslot < 36) pack_wd_store<PACK_ST>(ws, xcd * 18 + (pslot >> 1), pslot & 1, pl, lane);
      else if (pslot < 53) pack_gu_store<GU_ST, false>(ws, xcd * 9 + (K1_NGU + pslot - 36) % 9, (K1_NGU + pslot - 36) / 9, pl, lane);
    } else if ((w == 6) == (st == 8)) {
      if (pslot < 36) pack_wd_load(wd, xcd * 18 + (pslot >> 1), pslot & 1, pl, lane);
      else if (pslot < 53) pack_gu_load(wg, wu, xcd * 9 + (K1_NGU + pslot - 36) % 9, (K1_NGU + pslot - 36) / 9, pl, lane);
    }
  };
  static_assert(K1_NGU == 64, "tail slot map");
  if (isq) { if (w < 4) k1_body<true, 0>(lds, ldsb, lane, tg, rt, grp, ws, issue, tail); else k1_body<true, 1>(lds, ldsb, lane, tg, rt, grp, ws, issue, tail); }
  else     { if (w < 4) k1_body<false, 0>(lds, ldsb, lane, tg, rt, grp, ws, issue, tail); else k1_body<false, 1>(lds, ldsb, lane, tg, rt, grp, ws, issue, tail); }
  tail(std::integral_constant<int, 9>{});
}

__global__ __launch_bounds__(256) void k1b_reduce(char* __restrict__ ws) {
  const int tid = threadIdx.x;
  const int b = blockIdx.x;
  const int hh = b, s = tid >> 7, l = (tid >> 1) & 63, r = tid & 1;
  const int row = l & 31, h = l >> 5;
  const int T = (18 * hh) / 16 + s;
  const int d0 = 16 * T + 8 * r + 4 * h;
  const int G = hh >> 2, hl = hh & 3;
  int el = -1; bool isz = false;
  if (row < 16) el = row;
  else if (row == 18 || row == 22) isz = true;
  else if ((hh & 7) < 4 ? (row == 16 || row == 17) : (row == 20 || row == 21)) el = 16 + (row & 1);
  const int dlo = 18 * hh, dhi = dlo + 18;
  f32x4 sum = {};
  if ((el >= 0 || isz) && d0 + 3 >= dlo && d0 < dhi) {
    const int dk = d0 - 72 * G, a = dk >> 4, dr = dk & 15;
    int p, col;
    if (isz) { p = 11 + a; col = 0; }
    else {
      const int ev = 18 * hl + el;
      if (ev >= 64) { p = 11 + a; col = 8 + ev - 64; }
      else { const int vt = ev >> 4; col = ev & 15; p = a == 4 ? 10 : 3 * a + (vt - a); }
    }
    const char* src = ws + WS_KVP + ((size_t)G * 16 + p) * 1024 + ((dr >> 2) * 16 + col) * 16;
    f32x4 v[16];
#pragma unroll
    for (int i = 0; i < 16; ++i) v[i] = *(const f32x4*)(src + (size_t)i * 8 * 16 * 1024);
#pragma unroll
    for (int i = 0; i < 16; ++i) sum += v[i];
#pragma unroll
    for (int j = 0; j < 4; ++j) if (d0 + j < dlo || d0 + j >= dhi) sum[j] = 0.f;
  }
  *(u32x2*)(ws + WS_KVF + ((size_t)(hh * 2 + s) * 64 + l) * 16 + r * 8) = (u32x2){pk2(sum[0], sum[1]), pk2(sum[2], sum[3])};
}

#define K2_WIN 16
constexpr int K2_LDS = 4 * 19 * 1024;
__global__ __launch_bounds__(256, 1) void k2_attn_out(const u16* __restrict__ x, const char* __restrict__ ws_in, char* __restrict__ ws) {
  extern __shared__ __attribute__((aligned(16))) char lds[];
  const int tid = threadIdx.x, lane = tid & 63, w = uni(tid >> 6);
  const int b = blockIdx.x, xcd = b & 7, idx = b >> 3;
  const int tt = xcd * 8 + (idx & 7), ng = idx >> 3;
  const int tok = lane & 31, h = lane >> 5;
  const char* pqp = ws_in + WS_PQ + ((size_t)tt * 36 + w * 9) * 1024;
  const char* kvp = ws_in + WS_KVF + (size_t)w * 16 * 1024;
  const char* wop = ws_in + WS_WOP + (size_t)(ng * 4 + w) * 45 * 1024;
  const unsigned voff = lane * 16, voffc = (tok < 24 ? lane : (lane & 32)) * 16;
  u32x4 pq[9], kvf[16], wf[45];
  auto issue = [&](auto II) {
    constexpr int i = decltype(II)::value;
    if constexpr (i == 0) gload16<0>(pq[0], pqp, voff);
    else if constexpr (i < 70) {
      constexpr int u = (i - 1) / 8, k = (i - 1) % 8;
      if constexpr (u < 8 && k < 2) gload16<((2 * u + k) % 4) * 1024>(kvf[2 * u + k], kvp + ((2 * u + k) / 4) * 4096, voffc);
      else if constexpr (u < 8 && k == 2) gload16<((u + 1) % 4) * 1024>(pq[u + 1], pqp + ((u + 1) / 4) * 4096, voff);
      else { constexpr int n = u < 8 ? 5 * u + k - 3 : 40 + k; gload16<(n % 4) * 1024>(wf[n], wop + (n / 4) * 4096, n % 5 == 4 ? voffc : voff); }
    }
  };
  const int u0 = w == 0 ? 0 : 2 * w + 1;
  u32x4 xr[3]; unsigned mxb;
  sfor<4>([&](auto I) { issue(I); });
  {
    const char* xb0 = (const char*)x + (size_t)tt * 32 * 1152 + ng * 288 + u0 * 32;
    const unsigned xo = tok * 1152 + h * 16;
    gload16<0>(xr[0], xb0, xo); gload16<32>(xr[1], xb0, xo);
    gload16<0>(xr[2], xb0 + (w == 0 ? 64 : 0), xo);
    gload4<0>(mxb, ws_in + WS_MX + (size_t)tt * 128, tok * 4);
  }
  sfor<K2_WIN - 4>([&](auto I) { issue(std::integral_constant<int, 4 + decltype(I)::value>{}); });
  u32x4 ofr; unsigned lo[8];
  f32x16 acc[5], c;
  auto ostep = [&](auto UI) {
    constexpr int u = decltype(UI)::value;
    if constexpr (u == 0) pin(pq[0]);
    pin(kvf[2 * u]); pin(kvf[2 * u + 1]); pin(pq[u + 1]);
    c = mfma32(kvf[2 * u], pq[u], (f32x16){});
    c = mfma32(kvf[2 * u + 1], pq[u + 1], c);
  };
  wait_vm<K2_WIN - 4 + 4>();
  ostep(std::integral_constant<int, 0>{});
  sfor<9>([&](auto UI) {
    constexpr int u = decltype(UI)::value;
    if constexpr (u < 8) {
      const float r = __builtin_amdgcn_rcpf(c[10] + 1e-6f);
      ofr = (u32x4){pk2(c[0] * r, c[1] * r), pk2(c[2] * r, c[3] * r), pk2(c[4] * r, c[5] * r), pk2(c[6] * r, c[7] * r)};
      lo[u] = pk2(c[8] * r, c[9] * r);
    } else {
      sfor<4>([&](auto J) { constexpr int j = decltype(J)::value; unsigned a = lo[j], d = lo[4 + j]; pin(a); pin(d); ofr[j] = h ? d : a; });
    }
    sfor<8>([&](auto KI) { issue(std::integral_constant<int, K2_WIN + 8 * u + decltype(KI)::value>{}); });
    constexpr int issued = (K2_WIN + 8 * (u + 1)) < 70 ? (K2_WIN + 8 * (u + 1)) : 70;
    constexpr int need = u < 7 ? 8 * (u + 1) + 4 : (u == 7 ? 65 : 70);
    wait_vm<issued - need>();
    if constexpr (u < 7) ostep(std::integral_constant<int, u + 1>{});
    sfor<5>([&](auto NI) {
      constexpr int i = decltype(NI)::value, n = 5 * u + i;
      pin(wf[n]);
      acc[i] = mfma32(wf[n], ofr, u == 0 ? (f32x16){} : acc[i]);
    });
    sb0();
  });
#pragma unroll
  for (int i = 0; i < 3; ++i) pin(xr[i]);
  pin(mxb);
  const float mx = __uint_as_float(mxb);
  f32x4* xb = (f32x4*)lds;
#pragma unroll
  for (int q = 0; q < 19; ++q) {
    const int i = q >> 2, rb = (q & 3) * 4;
    xb[(w * 19 + q) * 64 + lane] = (f32x4){acc[i][rb], acc[i][rb + 1], acc[i][rb + 2], acc[i][rb + 3]};
  }
#pragma unroll
  for (int i = 0; i < 3; ++i) {
    auto r0 = __builtin_amdgcn_permlane32_swap(xr[i][0], xr[i][2], false, false);
    auto r1 = __builtin_amdgcn_permlane32_swap(xr[i][1], xr[i][3], false, false);
    xr[i] = (u32x4){r0[0], r1[0], r0[1], r1[1]};
  }
  wait_lgkm0(); bar();
  float ms = 0.f;
#pragma unroll
  for (int k = 0; k < 4; ++k) { const f32x4 v = xb[(k * 19 + 18) * 64 + tok]; ms += v[0] + v[1]; }
  const float mean = rbf(mx + ms * (1.f / D_));
  if (ng == 0 && w == 0 && h == 0) ((float*)(ws + WS_MEAN))[tt * 32 + tok] = mean;
  float s1 = 0.f, s2 = 0.f;
#pragma unroll
  for (int i = 0; i < 3; ++i) {
    if (i < 2 || w == 0) {
      const int u = u0 + i;
      f32x4 a = {}, d = {};
#pragma unroll
      for (int k = 0; k < 4; ++k) { a += xb[(k * 19 + 2 * u) * 64 + lane]; d += xb[(k * 19 + 2 * u + 1) * 64 + lane]; }
      const unsigned at[4] = {pk2(a[0], a[1]), pk2(a[2], a[3]), pk2(d[0], d[1]), pk2(d[2], d[3])};
      u32x4 o;
#pragma unroll
      for (int j = 0; j < 4; ++j) {
        const unsigned xv = xr[i][j];
        const unsigned hb = pk2(bflo(xv) + bflo(at[j]), bfhi(xv) + bfhi(at[j]));
        s1 = dot2_ones(hb, s1); s2 = __builtin_amdgcn_fdot2_f32_bf16(as_bf2(hb), as_bf2(hb), s2, false);
        o[j] = pk2(bflo(hb) - mean, bfhi(hb) - mean);
      }
      store16<OUT2_ST>(ws + WS_HF + (((size_t)tt * 36 + ng * 9 + u) * 64 + lane) * 16, o);
    }
  }
  s1 += __shfl_xor(s1, 32); s2 += __shfl_xor(s2, 32);
  if (h == 0) *(f32x2*)(ws + WS_HST + ((size_t)(tt * 32 + tok) * 16 + ng * 4 + w) * 8) = (f32x2){s1, s2};
  asm volatile(".Lfk_text_end:");
}
}
extern "C" void kernel_launch(void* const* d_in, const int* in_sizes, int n_in, void* d_out, int out_size, void* d_ws, size_t ws_size, hipStream_t st) {
  using namespace fk;
  const u16* x = (const u16*)d_in[0]; const u16* wqkv = (const u16*)d_in[1]; const u16* wo = (const u16*)d_in[2];
  const u16* wg = (const u16*)d_in[4]; const u16* wu = (const u16*)d_in[5]; const u16* wd = (const u16*)d_in[6];
  char* ws = (char*)d_ws;
  (void)hipFuncSetAttribute((const void*)k1_qkv, hipFuncAttributeMaxDynamicSharedMemorySize, K1_LDS);
  (void)hipFuncSetAttribute((const void*)k2_attn_out, hipFuncAttributeMaxDynamicSharedMemorySize, K2_LDS);
  (void)hipFuncSetAttribute((const void*)k34_ffn, hipFuncAttributeMaxDynamicSharedMemorySize, K3_LDS);
  k1_qkv<<<256, 512, K1_LDS, st>>>(x, wqkv, wo, wg, wu, wd, ws);
  k1b_reduce<<<32, 256, 0, st>>>(ws);
  k2_attn_out<<<256, 256, K2_LDS, st>>>(x, ws, ws);
  k34_ffn<<<256, 512, K3_LDS, st>>>(ws, ws, (u16*)d_out);
}
```
